```python
import torch

def _softmax(x, axis=-1):
    return torch.softmax(x, dim=axis)
def _gelu(x):
    return torch.nn.functional.gelu(x, approximate="tanh")
def _silu(x):
    return torch.nn.functional.silu(x)
def _sigmoid(x):
    return torch.sigmoid(x)
def _layernorm(x, axis=-1, eps=1e-5):
    m = x.mean(dim=axis, keepdim=True)
    v = x.var(dim=axis, keepdim=True, unbiased=False)
    return (x - m) / torch.sqrt(v + eps)
def _scatter_add(tgt, idx, src, axis=0):
    return tgt.clone().index_add_(axis, idx, src)
def _segment_sum(src, seg, n):
    out = torch.zeros((n,) + src.shape[1:], dtype=src.dtype)
    return out.index_add_(0, seg, src)
def _pad(x, pw):
    flat = tuple(v for pair in reversed(pw) for v in pair)
    return torch.nn.functional.pad(x, flat)

kernel_name = 'interleave_layers__sliding_local_dilated__swiglu_ffn__prompt'

def setup_inputs():

    _w = (lambda a: (lambda t: t.to(torch.bfloat16) if t.is_floating_point() else t)(torch.as_tensor(a)))
    def _A_setup():
        B, S, D = 1, 2048, 1024
        return {
            "x": _w((lambda *s: torch.randn(*s, dtype=torch.bfloat16) * 0.05)(B, S, D)),
            "wqkv": _w((lambda *s: torch.randn(*s, dtype=torch.bfloat16) * 0.05)(D, 3 * D)),
            "wo": _w((lambda *s: torch.randn(*s, dtype=torch.bfloat16) * 0.05)(D, D)),
        }
    _A_in = _A_setup()

    def _B_setup():
        B, S, D, F = 1, 2048, 1024, 3584
        return {
            "x": _w((lambda *s: torch.randn(*s, dtype=torch.bfloat16) * 0.05)(B, S, D)),
            "w_gate": _w((lambda *s: torch.randn(*s, dtype=torch.bfloat16) * 0.05)(D, F)),
            "w_up": _w((lambda *s: torch.randn(*s, dtype=torch.bfloat16) * 0.05)(D, F)),
            "w_down": _w((lambda *s: torch.randn(*s, dtype=torch.bfloat16) * 0.05)(F, D)),
        }
    _B_in = _B_setup()

    return {
        "A_x": _A_in["x"],
        "A_wqkv": _A_in["wqkv"],
        "A_wo": _A_in["wo"],
        "B_x": _B_in["x"],
        "B_w_gate": _B_in["w_gate"],
        "B_w_up": _B_in["w_up"],
        "B_w_down": _B_in["w_down"],
    }

def reference(A_x, A_wqkv, A_wo, B_x, B_w_gate, B_w_up, B_w_down):
    def _A_ref(x, wqkv, wo):
        B, S, D = x.shape
        H = 32; Dh = D // H
        W, dil = 8, 2
        qkv = (lambda x, *d: x.permute(*d))((x @ wqkv).reshape(B, S, 3, H, Dh), 2, 0, 3, 1, 4)
        q, k, v = qkv[0], qkv[1], qkv[2]
        scale = Dh ** -0.5
        pos = torch.arange(S)
        off = torch.arange(W) * dil
        idx = pos[:, None] - off[None, :]
        mask = idx >= 0
        idx_c = torch.where(mask, idx, 0).reshape(-1)
        kg = k[:, :, idx_c, :].reshape(B, H, S, W, Dh)
        vg = v[:, :, idx_c, :].reshape(B, H, S, W, Dh)
        logits = torch.einsum("bhsd,bhswd->bhsw", q, kg) * scale
        logits = torch.where(mask[None, None, :, :], logits, -1e9)
        attn = _softmax(logits, axis=-1)
        o = torch.einsum("bhsw,bhswd->bhsd", attn, vg)
        return (lambda x, *d: x.permute(*d))(o, 0, 2, 1, 3).reshape(B, S, D) @ wo

    def _B_ref(x, w_gate, w_up, w_down):
        g = _silu(x @ w_gate)
        u = x @ w_up
        return (g * u) @ w_down

    x = A_x
    x = x + _A_ref(_layernorm(x, axis=-1), A_wqkv, A_wo)
    x = x + _B_ref(_layernorm(x, axis=-1), B_w_gate, B_w_up, B_w_down)
    x = x + _A_ref(_layernorm(x, axis=-1), A_wqkv, A_wo)
    x = x + _B_ref(_layernorm(x, axis=-1), B_w_gate, B_w_up, B_w_down)
    return x
```

```cpp
#include <hip/hip_runtime.h>
#include <hip/hip_bf16.h>
#include <stdint.h>

#pragma clang diagnostic ignored "-Winline-asm"
typedef unsigned short u16;
using bf16x8 = __attribute__((ext_vector_type(8))) short;
using s16x4  = __attribute__((ext_vector_type(4))) short;
using f32x4  = __attribute__((ext_vector_type(4))) float;
using f32x2  = __attribute__((ext_vector_type(2))) float;
using u32x2  = __attribute__((ext_vector_type(2))) unsigned;
using u32x4  = __attribute__((ext_vector_type(4))) unsigned;
typedef __bf16 bf2_t __attribute__((ext_vector_type(2)));
#define LDS_AS __attribute__((address_space(3)))
#define INL __attribute__((always_inline))

constexpr int STT = 22, STK = 10, MXP = 1, LPR = 1, SPU = 10, PRT = 20, WOLD = 12, WOBAR = 4, WVN = 2, GL2 = 16,
              QL1 = 16, QG1 = 10, GGS = 6, WV0 = 8, PV0 = 6, HPRIO = 2, SLP = 3, KIN = 9, KSKIP = 2, NSOLO = 4,
              FLAGST = 2, DBAR = 4, DBAR2 = 4;
constexpr int S_ = 2048, D_ = 1024, F_ = 3584;

__device__ __forceinline__ float bf2f(u16 v) { return __uint_as_float(((unsigned)v) << 16); }
__device__ __forceinline__ u16 f2bf(float f) {
    __hip_bfloat16 h = __float2bfloat16(f);
    return *reinterpret_cast<u16*>(&h);
}
__device__ __forceinline__ float rbf(float f) { return bf2f(f2bf(f)); }
__device__ __forceinline__ unsigned pk2(float a, float b) {
    const f32x2 v = {a, b};
    return __builtin_bit_cast(unsigned, __builtin_convertvector(v, bf2_t));
}
__device__ __forceinline__ float lo16(unsigned u) { return __uint_as_float(u << 16); }
__device__ __forceinline__ float hi16(unsigned u) { return __uint_as_float(u & 0xffff0000u); }

__device__ __forceinline__ float dot2(unsigned a, unsigned b, float c) {
    return __builtin_amdgcn_fdot2_f32_bf16(__builtin_bit_cast(bf2_t, a), __builtin_bit_cast(bf2_t, b), c, false);
}
__device__ __forceinline__ float addf(float a, float b) {
    float r;
    asm("v_add_f32 %0, %1, %2" : "=v"(r) : "v"(a), "v"(b));
    return r;
}
__device__ __forceinline__ float mulf(float a, float b) {
    float r;
    asm("v_mul_f32 %0, %1, %2" : "=v"(r) : "v"(a), "v"(b));
    return r;
}
__device__ __forceinline__ float addf_t(float a, float b) {
    float r;
    asm("s_nop 0\n\tv_add_f32 %0, %1, %2" : "=v"(r) : "v"(a), "v"(b));
    return r;
}
__device__ __forceinline__ float rcp_nop(float a) {
    float r;
    asm("v_rcp_f32 %0, %1\n\ts_nop 0" : "=v"(r) : "v"(a));
    return r;
}
__device__ __forceinline__ float subf(float a, float b) {
    float r;
    asm("v_sub_f32 %0, %1, %2" : "=v"(r) : "v"(a), "v"(b));
    return r;
}
__device__ __forceinline__ float max2(float a, float b) { return __builtin_amdgcn_fmed3f(a, b, __builtin_inff()); }

template <int V> struct CT { static constexpr int value = V; };
template <int I, int N, class F> __device__ __forceinline__ void static_for(F&& f) {
    if constexpr (I < N) { f(CT<I>{}); static_for<I + 1, N>(f); }
}

#define WAIT_LGKM0() asm volatile("s_waitcnt lgkmcnt(0)" ::: "memory")
#define BARRIER() do { asm volatile("" ::: "memory"); __builtin_amdgcn_s_barrier(); asm volatile("" ::: "memory"); } while (0)
#define SCHED() __builtin_amdgcn_sched_barrier(0)
template <int N> __device__ __forceinline__ void wait_vm() {
    static_assert(N >= 0 && N < 64, "vmcnt range");
    asm volatile("s_waitcnt vmcnt(%0)" :: "n"(N) : "memory");
}

__device__ __forceinline__ void glds16(const void* sbase, unsigned voff, unsigned lds_dst) {
    asm volatile("s_mov_b32 m0, %2\n\ts_nop 3\n\tglobal_load_lds_dwordx4 %0, %1" :: "v"(voff), "s"(sbase), "s"(__builtin_amdgcn_readfirstlane(lds_dst)) : "memory", "m0");
}
template <int NLANE>
__device__ __forceinline__ void glds16_part(const void* sbase, unsigned voff, unsigned lds_dst) {
    unsigned long long ex;
    asm volatile("s_mov_b64 %0, exec\n\ts_mov_b32 m0, %3\n\ts_mov_b64 exec, %4\n\ts_nop 1\n\t"
                 "global_load_lds_dwordx4 %1, %2 sc1\n\ts_mov_b64 exec, %0"
                 : "=&s"(ex) : "v"(voff), "s"(sbase), "s"(__builtin_amdgcn_readfirstlane(lds_dst)), "n"((1ull << NLANE) - 1) : "memory", "m0");
}
__device__ __forceinline__ void glds16_mask(const void* sbase, unsigned voff, unsigned lds_dst, unsigned long long mask) {
    unsigned long long ex;
    asm volatile("s_mov_b64 %0, exec\n\ts_mov_b32 m0, %3\n\ts_mov_b64 exec, %4\n\ts_nop 1\n\t"
                 "global_load_lds_dwordx4 %1, %2\n\ts_mov_b64 exec, %0"
                 : "=&s"(ex) : "v"(voff), "s"(sbase), "s"(__builtin_amdgcn_readfirstlane(lds_dst)), "s"(mask) : "memory", "m0");
}
__device__ __forceinline__ void glds16_dev(const void* sbase, unsigned voff, unsigned lds_dst) {
    asm volatile("s_mov_b32 m0, %2\n\ts_nop 3\n\tglobal_load_lds_dwordx4 %0, %1 sc1" :: "v"(voff), "s"(sbase), "s"(__builtin_amdgcn_readfirstlane(lds_dst)) : "memory", "m0");
}
template <int V> __device__ __forceinline__ void s_set(unsigned& d) { asm volatile("s_mov_b32 %0, %1" : "=s"(d) : "n"(V)); }
template <int V> __device__ __forceinline__ void m0_set(unsigned base) { asm volatile("s_add_i32 m0, %0, %1" :: "s"(base), "n"(V) : "m0", "scc"); }
template <bool DEV> __device__ __forceinline__ void blds16(__amdgpu_buffer_rsrc_t r, unsigned voff, unsigned soff) {
    if constexpr (DEV) asm volatile("buffer_load_dwordx4 %0, %1, %2 offen sc1 lds" :: "v"(voff), "s"(r), "s"(soff) : "memory");
    else asm volatile("buffer_load_dwordx4 %0, %1, %2 offen lds" :: "v"(voff), "s"(r), "s"(soff) : "memory");
}
__device__ __forceinline__ void gload16_dev(f32x4& d, const void* sbase, unsigned voff) {
    asm volatile("s_nop 4\n\tglobal_load_dwordx4 %0, %1, %2 sc1" : "=v"(d) : "v"(voff), "s"(sbase) : "memory");
}
template <int OFF> __device__ __forceinline__ void gload16_devo(f32x4& d, const void* sbase, unsigned voff) {
    asm volatile("global_load_dwordx4 %0, %1, %2 offset:%3 sc1" : "=v"(d) : "v"(voff), "s"(sbase), "n"(OFF) : "memory");
}
template <int OFF> __device__ __forceinline__ void gload16(f32x4& d, const void* sbase, unsigned voff) {
    asm volatile("s_nop 4\n\tglobal_load_dwordx4 %0, %1, %2 offset:%3" : "=v"(d) : "v"(voff), "s"(sbase), "n"(OFF) : "memory");
}
constexpr unsigned SINK = 163584;
__device__ __forceinline__ void touch1(unsigned sink, const void* sbase, unsigned voff) {
    asm volatile("s_mov_b32 m0, %2\n\ts_nop 3\n\tglobal_load_lds_dword %0, %1" :: "v"(voff), "s"(sbase), "s"(sink) : "memory", "m0");
}
__device__ __forceinline__ const char* uni(const char* p) {
    const unsigned long long v = (unsigned long long)p;
    const unsigned lo = __builtin_amdgcn_readfirstlane((unsigned)v), hi = __builtin_amdgcn_readfirstlane((unsigned)(v >> 32));
    return (const char*)(((unsigned long long)hi << 32) | lo);
}
__device__ __forceinline__ void store16_wt(void* base, unsigned off, u32x4 v) {
    const __amdgpu_buffer_rsrc_t r = __builtin_amdgcn_make_buffer_rsrc(base, 0, 0x7fffffff, 0x00020000);
    __builtin_amdgcn_raw_buffer_store_b128(v, r, off, 0, 16);
}
__device__ __forceinline__ unsigned opaque(unsigned v) { asm volatile("" : "+v"(v)); return v; }
__device__ __forceinline__ unsigned bid_() { return __builtin_amdgcn_readfirstlane(opaque(blockIdx.x)); }
__device__ __forceinline__ int wg_id() { const unsigned b = bid_(); return (b & 7) * 32 + (b >> 3); }
template <class T> __device__ __forceinline__ T* sopq(T* p) { asm volatile("" : "+s"(p)); return p; }
__device__ __forceinline__ void code_touch(unsigned sink, unsigned lane) {
    const char* pc;
    int off;
    unsigned len;
    asm volatile("s_getpc_b64 %0\n.Lct%=:\n\ts_mov_b32 %1, body_k-.Lct%=\n\ts_mov_b32 %2, .Lbody_end-body_k"
                 : "=s"(pc), "=s"(off), "=s"(len));
    const unsigned o = ((bid_() >> 3) + lane * 32u) * 128u;
    touch1(sink, pc + off, o < len ? o : 0u);
}
template <int OFF> __device__ __forceinline__ bf16x8 lds_a(unsigned base) {
    static_assert(OFF >= 0 && OFF < 65536, "ds offset");
    return *(const LDS_AS bf16x8*)(size_t)(base + OFF);
}
template <int OFF0, int OFF1> __device__ __forceinline__ bf16x8 lds_b(unsigned base0, unsigned base1) {
    static_assert(OFF0 >= 0 && OFF0 < 65536 && OFF1 >= 0 && OFF1 < 65536, "ds offset");
    s16x4 lo = __builtin_amdgcn_ds_read_tr16_b64_v4i16((LDS_AS s16x4*)(size_t)(base0 + OFF0));
    s16x4 hi = __builtin_amdgcn_ds_read_tr16_b64_v4i16((LDS_AS s16x4*)(size_t)(base1 + OFF1));
    return __builtin_shufflevector(lo, hi, 0, 1, 2, 3, 4, 5, 6, 7);
}
template <int OFF> __device__ __forceinline__ s16x4 lds_h(unsigned base) {
    static_assert(OFF >= 0 && OFF < 65536, "ds offset");
    return __builtin_amdgcn_ds_read_tr16_b64_v4i16((LDS_AS s16x4*)(size_t)(base + OFF));
}
__device__ __forceinline__ bf16x8 cat(s16x4 lo, s16x4 hi) { return __builtin_shufflevector(lo, hi, 0, 1, 2, 3, 4, 5, 6, 7); }
__device__ __forceinline__ f32x4 mma(bf16x8 a, bf16x8 b, f32x4 c) {
    return __builtin_amdgcn_mfma_f32_16x16x32_bf16(a, b, c, 0, 0, 0);
}

__device__ __forceinline__ void mma_ip(bf16x8 a, bf16x8 b, f32x4& c) {
    asm volatile("v_mfma_f32_16x16x32_bf16 %0, %1, %2, %0" : "+v"(c) : "v"(a), "v"(b));
}

__device__ __forceinline__ float sum8(float d) {
    d += __uint_as_float(__builtin_amdgcn_update_dpp(0u, __float_as_uint(d), 0xB1, 0xf, 0xf, false));
    d += __uint_as_float(__builtin_amdgcn_update_dpp(0u, __float_as_uint(d), 0x4E, 0xf, 0xf, false));
    d += __uint_as_float(__builtin_amdgcn_update_dpp(0u, __float_as_uint(d), 0x141, 0xf, 0xf, false));
    return d;
}
__device__ __forceinline__ int res_row(int tid, int it) { return (tid >> 6) * 16 + it * 8 + ((tid >> 3) & 7); }
__device__ __forceinline__ void res_tail(const char* smem, const u32x4 (&resv)[2], u16* C, float* sp, int m0, int n0, int tid) {
    constexpr int CSTR = 64 * 2 + 16;
    const int row = res_row(tid, 0), ch = tid & 7;
    u32x4 v[2];
#pragma unroll
    for (int it = 0; it < 2; ++it) v[it] = *(const u32x4*)(smem + (row + it * 8) * CSTR + ch * 16);
#pragma unroll
    for (int it = 0; it < 2; ++it) {
#pragma unroll
        for (int e = 0; e < 4; ++e)
            v[it][e] = pk2(lo16(resv[it][e]) + lo16(v[it][e]), hi16(resv[it][e]) + hi16(v[it][e]));
        store16_wt(C, (unsigned)((m0 + row + it * 8) * D_ + n0 + ch * 8) * 2u, v[it]);
    }
    SCHED();
    if (sp == nullptr) return;
    float2 st[2];
#pragma unroll
    for (int it = 0; it < 2; ++it) {
        float ss = 0.f, qq = 0.f;
#pragma unroll
        for (int e = 0; e < 4; ++e) {
            ss = dot2(v[it][e], 0x3f803f80u, ss);
            qq = dot2(v[it][e], v[it][e], qq);
        }
        st[it] = make_float2(sum8(ss), sum8(qq));
    }
    if (ch < 2) {
        const float2 t = ch ? st[1] : st[0];
        const __amdgpu_buffer_rsrc_t r = __builtin_amdgcn_make_buffer_rsrc(sp, 0, 0x7fffffff, 0x00020000);
        __builtin_amdgcn_raw_buffer_store_b64(u32x2{__float_as_uint(t.x), __float_as_uint(t.y)}, r,
                                              (unsigned)(((m0 + row) >> 5) * 512 + (n0 >> 6) * 32 + (row & 31) + ch * 8) * 8u, 0, 16);
    }
}

constexpr int ACS = 192 * 2 + 16;
struct AttC {
    unsigned qa, kxa, kya, vxa, vya, oo, mx01, mx23;
    f32x4 cx, cy;
};
__device__ __forceinline__ void att_setup(AttC& c, unsigned lds0, int wid, int lane, int m0, int tn) {
    const int l15 = lane & 15, g = lane >> 4, q = l15 >> 2, p4 = l15 & 3;
    const int par = wid & 1, R0 = (wid >> 1) * 32;
    const unsigned cb = lds0 + par * ACS;
    c.qa = opaque(cb + (R0 + 2 * l15 + 16) * ACS + g * 16);
    c.kxa = opaque(cb + 128 + (R0 + 2 * l15) * ACS + g * 16);
    c.kya = opaque(cb + 128 + (R0 + 2 * (l15 & 7) + 32) * ACS + g * 16);
    c.vxa = opaque(cb + 256 + (R0 + 2 * (g * 4 + q)) * ACS + p4 * 16);
    c.vya = opaque(cb + 256 + (R0 + 2 * ((g & 1) * 4 + q) + 32) * ACS + p4 * 16);
    c.oo = opaque((unsigned)((m0 + R0 + 2 * (l15 & 7) + par) * D_ + tn * 64 + (l15 >> 3) * 32 + g * 8) * 2u);
    const int kmin = m0 + R0 == 0 ? 8 : 0;
    unsigned mx01 = 0u, mx23 = 0u;
#pragma unroll
    for (int r = 0; r < 4; ++r) {
        const int d = l15 - g * 4 - r;
        const bool vx = (d < 0) & (d >= -8) & (g * 4 + r >= kmin), vy = d >= 8;
        c.cx[r] = __uint_as_float(opaque(vx ? 0u : 0xff61b1e6u));
        c.cy[r] = __uint_as_float(opaque(vy ? 0u : 0xff61b1e6u));
        const unsigned mb = d < 0 ? ((r & 1) ? 0xffff0000u : 0xffffu) : 0u;
        if (r < 2) mx01 |= mb; else mx23 |= mb;
    }
    c.mx01 = opaque(mx01);
    c.mx23 = opaque(mx23);
}
constexpr int ATX = 155648;
__device__ __forceinline__ void att_put(const AttC& c, char* smem, int lane) {
    u32x4* const p = (u32x4*)(smem + ATX) + lane;
    p[0] = u32x4{c.qa, c.kxa, c.kya, c.vxa};
    p[64] = u32x4{c.vya, c.oo, c.mx01, c.mx23};
    p[128] = __builtin_bit_cast(u32x4, c.cx);
    p[192] = __builtin_bit_cast(u32x4, c.cy);
}
__device__ __forceinline__ void att_get(AttC& c, const char* smem, int wid, int lane, int m0) {
    const u32x4* const p = (const u32x4*)(smem + ATX) + lane;
    const u32x4 a = p[0], b = p[64];
    const int dr = (wid >> 1) * 32 + (wid & 1) - 64;
    c.qa = a[0] + dr * ACS; c.kxa = a[1] + dr * ACS; c.kya = a[2] + dr * ACS; c.vxa = a[3] + dr * ACS; c.vya = b[0] + dr * ACS;
    c.oo = b[1] + dr * D_ * 2; c.mx01 = b[2]; c.mx23 = b[3];
    c.cx = __builtin_bit_cast(f32x4, p[128]);
    c.cy = __builtin_bit_cast(f32x4, p[192]);
    const bool front = lane < (m0 == 0 && wid < 2 ? 32 : 0);
#pragma unroll
    for (int r = 0; r < 4; ++r) c.cx[r] = front ? -3e38f : c.cx[r];
}
__device__ __forceinline__ void att_run(const AttC& c, u16* C) {
    const f32x4 z4 = f32x4{0.f, 0.f, 0.f, 0.f};
    u32x4 ov[2];
    static_for<0, 2>([&](auto pc) INL {
        constexpr int PO = decltype(pc)::value * 64;
        const bf16x8 qf = lds_a<PO>(c.qa), kx = lds_a<PO>(c.kxa), ky = lds_a<PO>(c.kya);
        const bf16x8 v0 = lds_b<PO, PO>(c.vxa, c.vya), v1 = lds_b<PO + 8, PO + 8>(c.vxa, c.vya);
        const f32x4 sx = mma(kx, qf, c.cx), sy = mma(ky, qf, c.cy);
        float lg[4];
#pragma unroll
        for (int r = 0; r < 4; ++r) lg[r] = max2(sx[r], sy[r]);
        float mx = max2(max2(lg[0], lg[1]), max2(lg[2], lg[3]));
        {
            auto t = __builtin_amdgcn_permlane16_swap(__float_as_uint(mx), __float_as_uint(mx), false, false);
            mx = max2(__uint_as_float(t[0]), __uint_as_float(t[1]));
            t = __builtin_amdgcn_permlane32_swap(__float_as_uint(mx), __float_as_uint(mx), false, false);
            mx = max2(__uint_as_float(t[0]), __uint_as_float(t[1]));
        }
        float pr[4];
#pragma unroll
        for (int r = 0; r < 4; ++r) pr[r] = __builtin_amdgcn_exp2f(mulf(subf(lg[r], mx), 0.17677669529663687f * 1.44269504088896341f));
        float sum = addf(addf_t(pr[0], pr[1]), addf_t(pr[2], pr[3]));
        {
            auto t = __builtin_amdgcn_permlane16_swap(__float_as_uint(sum), __float_as_uint(sum), false, false);
            sum = addf(__uint_as_float(t[0]), __uint_as_float(t[1]));
            t = __builtin_amdgcn_permlane32_swap(__float_as_uint(sum), __float_as_uint(sum), false, false);
            sum = addf(__uint_as_float(t[0]), __uint_as_float(t[1]));
        }
        const float inv = rcp_nop(sum);
        const unsigned p01 = pk2(mulf(pr[0], inv), mulf(pr[1], inv)), p23 = pk2(mulf(pr[2], inv), mulf(pr[3], inv));
        const u32x4 pw = {p01 & c.mx01, p23 & c.mx23, p01 & ~c.mx01, p23 & ~c.mx23};
        const bf16x8 pf = __builtin_bit_cast(bf16x8, pw);
        const f32x4 o0 = mma(v0, pf, z4), o1 = mma(v1, pf, z4);
        ov[decltype(pc)::value] = u32x4{pk2(o0[0], o0[1]), pk2(o0[2], o0[3]), pk2(o1[0], o1[1]), pk2(o1[2], o1[3])};
    });
    u32x4 sa, sb;
#pragma unroll
    for (int e = 0; e < 4; ++e) {
        sa[e] = __builtin_amdgcn_update_dpp(ov[0][e], ov[1][e], 0x128, 0xf, 0xc, false);
        sb[e] = __builtin_amdgcn_update_dpp(ov[1][e], ov[0][e], 0x128, 0xf, 0x3, false);
    }
    store16_wt(C, c.oo, sa);
    store16_wt(C, c.oo + 16 * D_ * 2, sb);
}

typedef unsigned long long u64;
typedef __attribute__((address_space(1))) u64 gu64;
template <int NY = 0>
__device__ __forceinline__ void publish(unsigned* f, u64 tok, unsigned sink = SINK) {
    wait_vm<NY>();
    BARRIER();
    if (opaque(threadIdx.x) == 0) __hip_atomic_store((gu64*)f, tok, __ATOMIC_RELAXED, __HIP_MEMORY_SCOPE_AGENT);
    const unsigned t = opaque(threadIdx.x);
    if (__builtin_amdgcn_readfirstlane(t >> 6) == 2) {
        code_touch(sink, t & 63u);
        wait_vm<0>();
    }
}
__device__ __forceinline__ void await(unsigned& have, unsigned need, const unsigned* f, u64 tok) {
    if (__builtin_expect((have & need) != need, 0)) {
#pragma unroll 1
        for (unsigned spins = 0; spins < (1u << 22); ++spins) {
            have = (unsigned)__ballot(__hip_atomic_load((gu64*)f, __ATOMIC_RELAXED, __HIP_MEMORY_SCOPE_AGENT) == tok);
            if ((have & need) == need) break;
            __builtin_amdgcn_s_sleep(1);
        }
        asm volatile("" ::: "memory");
    }
}
__device__ __forceinline__ unsigned gu_bsrc(int o, int tn) {
    const int rem = (o >> 4) % (64 * 14);
    const int rowp = rem / 14, chn = rem % 14;
    const int kr = rowp ^ (4 * ((rowp >> 3) & 1));
    return (unsigned)(kr * F_ + tn * 112 + chn * 8) * 2u;
}
__device__ __forceinline__ unsigned b_off(int h, int lane, int ld, int col) {
    const int row = h * 8 + (lane >> 3), cp = lane & 7;
    const int jj = (cp >> 1) ^ (((row >> 1) & 1) | ((row >> 2) & 2));
    return (unsigned)(row * ld + col + jj * 16 + (cp & 1) * 8) * 2u;
}

constexpr int WOB = 65536, WONB = 12;
__device__ __forceinline__ int wo_k(int i) { return (int)(0xb3fedca987654210ull >> (4 * i)) & 15; }
__device__ __forceinline__ unsigned wo_boff(int h, int lane) {
    return b_off(h, lane, D_, (((wg_id()) % 64) / 4) * 64);
}
__device__ __forceinline__ void wo_stage_b(unsigned lds0, const u16* B, unsigned boff, int h, int step) {
#pragma unroll
    for (int i = 0; i < 2; ++i)
        glds16(uni((const char*)B + (wo_k(step) * 64 + i * 32) * D_ * 2), boff,
               lds0 + WOB + (step < WONB ? step : step - WONB) * 8192 + h * 1024 + i * 4096);
}
__device__ __forceinline__ void wo_phase(char* smem, const u16* A, const u16* __restrict__ B, u16* C, const u16* R, float* sp,
                                         const unsigned* pflags, unsigned* oflags, bool first, const u16* __restrict__ wg,
                                         const u16* __restrict__ wu, u64 tok) {
    constexpr int NK = 16, ASZ = 16384, CSTR = 64 * 2 + 16;
    const int tid = (int)opaque(threadIdx.x), lane = tid & 63;
    const int wid = __builtin_amdgcn_readfirstlane(tid >> 6);
    const int id = wg_id();
    const int tm = (id / 64) * 4 + id % 4, tn = (id % 64) / 4;
    const int m0 = tm * 128, n0 = tn * 64;
    const unsigned lds0 = __builtin_amdgcn_readfirstlane((unsigned)(size_t)(LDS_AS char*)smem);
    f32x4 resf[2];
    auto load_res = [&]() INL {
#pragma unroll
        for (int it = 0; it < 2; ++it) gload16_dev(resf[it], R, (unsigned)((m0 + res_row(tid, it)) * D_ + n0 + (tid & 7) * 8) * 2u);
    };
    unsigned goff[7] = {0u, 0u, 0u, 0u, 0u, 0u, 0u};
    auto stage_g = [&](int i, int kt) INL {
        const int p = wid - 4 + 4 * i;
        glds16(uni((const char*)(p >= 14 ? wu : wg) + kt * 64 * F_ * 2), goff[i], lds0 + 98304 + kt * 28672 + p * 1024);
    };
    if (wid >= 4) {
        const int h = wid - 4;
        const int row = h * 8 + (lane >> 3);
        const unsigned aoff = (unsigned)((m0 + row) * D_ + ((lane & 7) ^ ((row >> 1) & 7)) * 8) * 2u;
        const unsigned boff = wo_boff(h, lane);
        const unsigned* const f = pflags + (tm * 16 + (lane & 15)) * 32;
        unsigned have = 0u;

        auto stage = [&](int st, auto bc) INL {
            await(have, 1u << wo_k(st), f, tok);
#pragma unroll
            for (int i = 0; i < 4; ++i)
                glds16_dev(uni((const char*)A + wo_k(st) * 128 + i * 32 * D_ * 2), aoff, lds0 + (st & 3) * ASZ + h * 1024 + i * 4096);
            if constexpr (decltype(bc)::value) wo_stage_b(lds0, B, boff, h, st);
        };
        stage(0, CT<0>{});
        stage(1, CT<0>{});
#pragma unroll
        for (int i = 0; i < 7; ++i) goff[i] = opaque(gu_bsrc((h + 4 * i) * 1024 + (int)opaque(lane) * 16, id >> 3));
        wait_vm<4>();
        BARRIER();
        stage(2, CT<0>{});
        stage(3, CT<0>{});
#pragma unroll 1
        for (int st = 4; st < WONB; ++st) {
            wait_vm<8>();
            BARRIER();
            stage(st, CT<0>{});
        }
#pragma unroll 1
        for (int st = WONB; st < NK; ++st) {
            wait_vm<8>();
            BARRIER();
            stage(st, CT<1>{});
        }
        wait_vm<12>();
        BARRIER();
        load_res(); stage_g(0, 0); stage_g(1, 0);
        wait_vm<10>();
        BARRIER();
        stage_g(2, 0); stage_g(3, 0); stage_g(4, 0);
        wait_vm<7>();
        BARRIER();
        stage_g(5, 0); stage_g(6, 0);
        wait_vm<7>();
        asm volatile("" : "+v"(resf[0]), "+v"(resf[1]));

    } else {
        const int wm = wid >> 1, wn = wid & 1;
        const int l15 = lane & 15, g = lane >> 4, q = l15 >> 2, p4 = l15 & 3;
        unsigned ab[2], bb[2];
#pragma unroll
        for (int x = 0; x < 2; ++x) {
            ab[x] = opaque(lds0 + (wm * 64 + l15) * 128 + (((x * 4 + g) ^ (l15 >> 1)) * 16));
            const unsigned b = lds0 + WOB + (g * 8 + q) * 128 + p4 * 8 + (((wn * 2 + x) ^ ((q >> 1) | ((g & 1) << 1))) << 5);
            bb[x] = opaque(b);
        }
        f32x4 acc[4][2];
#pragma unroll
        for (int i = 0; i < 4; ++i)
#pragma unroll
            for (int j = 0; j < 2; ++j) {
                acc[i][j] = f32x4{0.f, 0.f, 0.f, 0.f};
                asm volatile("" : "+v"(acc[i][j]));
            }
        auto ldop = [&](bf16x8 (&af)[8], s16x4 (&bf)[8], auto ktc, auto oc) INL {
            constexpr int KT = decltype(ktc)::value, O = decltype(oc)::value;
            if constexpr (O == 2 || (O >= 5 && O < 12)) {
                constexpr int X = O == 2 ? 0 : O - 4;
                af[(X & 1) * 4 + X / 2] = lds_a<(KT % 4) * ASZ + (X / 2) * 2048>(ab[X & 1]);
            } else {
                constexpr int X = O < 2 ? O : (O < 5 ? O - 1 : O - 8), J = X / 4;
                bf[X] = lds_h<(KT % 4) * 8192 + ((X >> 1) & 1) * 4096 + (X & 1) * 512>(bb[J]);
            }
        };
        auto ktile = [&](bf16x8 (&af)[8], s16x4 (&bf)[8], bf16x8 (&an)[8], s16x4 (&bn)[8], auto nktc) INL {
            static_for<0, 16>([&](auto tc) INL {
                constexpr int T = decltype(tc)::value, j = T / 8, i = j ? 3 - (T / 2) % 4 : (T / 2) % 4, kk = T & 1;
                if constexpr (T == WOBAR && decltype(nktc)::value < NK) {
                    WAIT_LGKM0();
                    BARRIER();
                }
                mma_ip(cat(bf[j * 4 + kk * 2], bf[j * 4 + kk * 2 + 1]), af[kk * 4 + i], acc[i][j]);
                SCHED();
                if constexpr (decltype(nktc)::value < NK && T >= WOBAR) {
                    constexpr int U = T - WOBAR, lo_ = (U * 16 + WOLD - 1) / WOLD, hi_ = ((U + 1) * 16 + WOLD - 1) / WOLD;
                    static_for<lo_, (hi_ < 16 ? hi_ : 16)>([&](auto oc) INL { ldop(an, bn, nktc, oc); });
                }
                SCHED();
            });
        };
        if (first && wid == 0) {
            const int l = lane < 12 ? lane : 0, cb = (id >> 3) * 7 / 4 + l % 3;
            const int job = ((cb < 56 ? cb : 55) + (l >= 6 ? 56 : 0)) * 2 + (l / 3 & 1), oid = job + 8 * ((job + 32) / 56);
            unsigned have = 0u;
            await(have, ~0u, pflags + (((oid / 128) * 8 + oid % 8) * 16 + (oid % 128) / 8) * 32, tok);
        }
        bf16x8 a0[8], a1[8];
        s16x4 b0[8], b1[8];
        BARRIER();
        static_for<0, 16>([&](auto oc) INL { ldop(a0, b0, CT<0>{}, oc); });
#pragma unroll 1
        for (int it = 0; it < 3; ++it) {
            ktile(a0, b0, a1, b1, CT<1>{});
            ktile(a1, b1, a0, b0, CT<2>{});
            ktile(a0, b0, a1, b1, CT<3>{});
            const unsigned d = it == 2 ? -65536 : 32768;
            bb[0] += d; bb[1] += d;
            ktile(a1, b1, a0, b0, CT<4>{});
        }
        ktile(a0, b0, a1, b1, CT<13>{});
        load_res();
        SCHED();
        ktile(a1, b1, a0, b0, CT<14>{});
        ktile(a0, b0, a1, b1, CT<15>{});
        ktile(a1, b1, a0, b0, CT<16>{});
        asm volatile("s_nop 15\n\ts_nop 7" ::: "memory");
        SCHED();
#pragma unroll
        for (int i = 0; i < 4; ++i)
#pragma unroll
            for (int j = 0; j < 2; ++j) {
                u32x2 v = {pk2(acc[i][j][0], acc[i][j][1]), pk2(acc[i][j][2], acc[i][j][3])};
                *(u32x2*)(smem + (wm * 64 + i * 16 + l15) * CSTR + (wn * 32 + j * 16 + g * 4) * 2) = v;
            }
        wait_vm<0>();
        asm volatile("" : "+v"(resf[0]), "+v"(resf[1]));
    }
    WAIT_LGKM0();
    BARRIER();
    u32x4 resv[2];
#pragma unroll
    for (int it = 0; it < 2; ++it) resv[it] = __builtin_bit_cast(u32x4, resf[it]);
    res_tail(smem, resv, C, sp, m0, n0, tid);
    publish(oflags + (tm * 16 + tn) * 32, tok);
    if (wid >= 4) {
#pragma unroll
        for (int i = 0; i < 7; ++i) stage_g(i, 1);
    }
}

constexpr int QAS = 144 * 128, QBS = 192 * 128, QBR = 57344, QST = 159744;
__device__ __forceinline__ int qkv_k(int i) { return (int)(0xb3a2fedc98765410ull >> (4 * i)) & 15; }
__device__ __forceinline__ unsigned qkv_boff(int h, int lane) {
    return b_off(h, lane, 3 * D_, (((wg_id()) % 128) / 8) * 64);
}
__device__ __forceinline__ void qkv_stage_b(unsigned lds0, const u16* B, unsigned boff, int h, int kt, int sl) {
#pragma unroll
    for (int i = 0; i < 6; ++i)
        glds16(uni((const char*)B + ((kt * 64 + (i & 1) * 32) * 3 * D_ + (i >> 1) * D_) * 2), boff,
               lds0 + QBR + sl * QBS + h * 1024 + i * 4096);
}
__device__ __forceinline__ void qkv_phase(char* smem, const u16* A, const u16* __restrict__ B, u16* C, float* cs,
                                          const float* sp, const unsigned* pflags, unsigned* oflags,
                                          const u16* __restrict__ wo, bool first, const u16* __restrict__ wg,
                                          const u16* __restrict__ wu, u64* epoch, u64& tok) {
    constexpr int NK = 16;
    const int tid = (int)opaque(threadIdx.x), lane = tid & 63;
    const int wid = __builtin_amdgcn_readfirstlane(tid >> 6);
    const int id = wg_id();
    const int tm = (id / 128) * 8 + id % 8, tn = (id % 128) / 8;
    const int m0 = tm * 128;
    const unsigned lds0 = __builtin_amdgcn_readfirstlane((unsigned)(size_t)(LDS_AS char*)smem);
    float* const stat = (float*)(smem + QST);
    float* const csl = stat + 2 * 144;
    float* const cso = cs + 16 * F_ + id * 192;
    auto token = [&]() INL {
        asm volatile("s_load_dwordx2 %0, %1, 0x0\n\ts_waitcnt lgkmcnt(0)" : "=s"(tok) : "s"(epoch) : "memory");
        tok += 0x9e3779b97f4a7c15ull;
    };
    AttC atc;
    if (wid >= 4) {
        const int h = wid - 4, hl = tid - 256;
        const int row = h * 8 + (lane >> 3), hr = h * 4 + ((lane >> 3) & 3);
        const unsigned la = (unsigned)((m0 + row) * D_ + ((lane & 7) ^ ((row >> 1) & 7)) * 8) * 2u;
        const unsigned lh = (unsigned)((tm ? m0 - 16 + hr : 0) * D_ + ((lane & 7) ^ ((hr >> 1) & 7)) * 8) * 2u;
        const unsigned lb = qkv_boff(h, lane);
        const unsigned wb = wo_boff(h, lane);
        auto wo_pre = [&](int i) INL {
#pragma unroll
            for (int e = 0; e < 3; ++e) wo_stage_b(lds0, wo, wb, h, i * 3 - 1 + e);
        };
        if (first) {
            const unsigned long long hmask = h == 0 ? 0xffffff00ull : 0xffffffffull;
            auto piece = [&](int kt, int p) INL {
                const unsigned da = lds0 + (kt % 3) * QAS;
                if (p < 4) glds16(uni((const char*)A + kt * 128 + p * 32 * D_ * 2), la, da + h * 1024 + p * 4096);
                else if (p == 4) glds16_mask(uni((const char*)A + kt * 128), lh, da + 16384 + h * 512, hmask);
                else glds16(uni((const char*)B + ((kt * 64 + ((p - 5) & 1) * 32) * 3 * D_ + ((p - 5) >> 1) * D_) * 2), lb,
                            lds0 + QBR + (kt % 3) * QBS + h * 1024 + (p - 5) * 4096);
            };
            __builtin_amdgcn_s_setprio(HPRIO);
            constexpr unsigned tw = SINK;
            const unsigned one2 = opaque(0x3f803f80u);
            if (hl < 24) *(u32x4*)(smem + (hl >> 3) * QAS + 128 * 128 + (hl & 7) * 16) = u32x4{one2, one2, one2, one2};
            const bool idle = (tn & 7) == 3;
            const int job = idle ? 0 : id - 8 * ((id + 32) >> 6);
            const int cb = job >> 1, kh = job & 1;
            const char* const wp = uni((const char*)((cb < 56 ? wg : wu) + (long long)kh * 512 * F_ + (cb % 56) * 64));
            const unsigned wpo = (unsigned)((idle ? 0 : hl >> 3) * F_ + (hl & 7) * 8) * 2u;
            const int wst = idle ? 0 : 64 * F_;
            f32x4 wv[NK];
            float sm[8];
#pragma unroll
            for (int e = 0; e < 8; ++e) sm[e] = 0.f;
            auto wadd1 = [&](int r, int e) INL {
                float t0, t1;
                asm volatile("v_lshlrev_b32 %2, 16, %4\n\tv_and_b32 %3, 0xffff0000, %4\n\tv_add_f32 %0, %0, %2\n\tv_add_f32 %1, %1, %3"
                             : "+v"(sm[2 * e]), "+v"(sm[2 * e + 1]), "=&v"(t0), "=&v"(t1) : "v"(wv[r][e]));
            };
            auto wadd = [&](int r) INL {
#pragma unroll
                for (int e = 0; e < 4; ++e) wadd1(r, e);
            };
            const unsigned bx = bid_();
            const int kq = bx >> 4;
            auto sweep = [&]() INL {
                touch1(tw, B, (unsigned)(bx * 192 + (hl < 192 ? hl : 0)) * 128u);
                touch1(tw, A, (unsigned)(((bx & 15) * 128 + (hl & 127)) * 2048 + kq * 128));
            };
            if (kq < KIN && kq >= KSKIP) {
                if (h >= 2) {
                    for (int i = (kq - KSKIP + 1) * SLP; i > 0; --i) __builtin_amdgcn_s_sleep(1);
                    touch1(tw, B, (unsigned)(bx * 192 + (hl & 127)) * 128u);
                    touch1(tw, A, (unsigned)(((bx & 15) * 128 + (hl & 127)) * 2048 + kq * 128));
                    if (h == 2) touch1(tw, B, (unsigned)(bx * 192 + hl) * 128u);
                }
            }
            token();
            static_for<0, NK>([&](auto kc) INL {
                constexpr int KT = decltype(kc)::value;
                constexpr auto ex = [](int k) { return (k >= WV0 && k < WV0 + 16 / WVN ? WVN : 0) + (k == PV0 ? 1 : 0) + (k == NK - 2 ? 6 : 0); };
                wait_vm<(KT + 1 < NK ? 11 : 0) + ex(KT - 2) + ex(KT - 1)>();
                BARRIER();
                static_for<0, 11>([&](auto pc) INL {
                    constexpr int P = decltype(pc)::value;
                    if constexpr (KT + 2 < NK) piece(KT + 2, P);
                    if constexpr (KT - 3 >= WV0 && KT - 3 < WV0 + 16 / WVN && P < 8) {
#pragma unroll
                        for (int u = 0; u < WVN / 2; ++u) wadd1((KT - 3 - WV0) * WVN + u * 2 + P / 4, P % 4);
                    }
                    SCHED();
                });
                if constexpr (KT + KIN < NK) { if (kq == KT + KIN) sweep(); }
                if constexpr (KT >= WV0 && KT < WV0 + 16 / WVN) {
#pragma unroll
                    for (int u = 0; u < WVN; ++u) gload16<0>(wv[(KT - WV0) * WVN + u], uni(wp + ((KT - WV0) * WVN + u) * wst), wpo);
                }
                if constexpr (KT == PV0) touch1(tw, wo, (unsigned)(bx * 64 + (hl & 63)) * 128u);
                if constexpr (KT == NK - 2) wo_pre(1);
                if constexpr (KT == 7) {
                    att_setup(atc, lds0, wid, lane, m0, tn);
                    if (h == 0) att_put(atc, smem, lane);
                }
                if constexpr (KT == 4) {
                    if (h == 0) touch1(tw, oflags - 9 * 8192, ((lane < 10 ? lane : 0) * 256 + bx) * 128u);
                }
                WAIT_LGKM0();
            });
            __builtin_amdgcn_s_setprio(0);
            wo_pre(2);
            BARRIER();
            {
                const int l = lane & 15, gi = h * 3 + (lane < 48 ? lane >> 4 : 0), mr = gi * 15 + l - 1;
                const float* const raw = (const float*)(smem + 131072 + gi * 1024);
                const float mean = mulf(raw[l * 4], 1.f / D_);
                const float var = fmaxf(subf(mulf(raw[((l >> 2) * 16 + l) * 4 + (l & 3)], 1.f / D_), mulf(mean, mean)), 0.f);
                const int row = l == 0 ? 0 : (gi < 9 ? 16 + (mr < 128 ? mr : 127) : l);
                const float sd = rbf(__builtin_amdgcn_sqrtf(rbf(addf(rbf(var), 1e-5f))));
                if (gi < 10) *(float2*)(stat + 2 * row) = make_float2(rbf(mean), __builtin_amdgcn_rcpf(sd));
            }
            WAIT_LGKM0();
            BARRIER();
            if (hl < 48) store16_wt(cso, (unsigned)hl * 16u, *(const u32x4*)(csl + hl * 4));
            wait_vm<6>();
#pragma unroll
            for (int r = NK - 3 > WV0 ? (NK - 3 - WV0) * WVN : 0; r < NK; ++r) wadd(r);
            asm volatile("s_nop 1\n\tv_add_f32_dpp %0, %0, %0 row_ror:8 row_mask:0xf bank_mask:0xf\n\tv_add_f32_dpp %1, %1, %1 row_ror:8 row_mask:0xf bank_mask:0xf\n\t"
                         "v_add_f32_dpp %2, %2, %2 row_ror:8 row_mask:0xf bank_mask:0xf\n\tv_add_f32_dpp %3, %3, %3 row_ror:8 row_mask:0xf bank_mask:0xf\n\t"
                         "v_add_f32_dpp %4, %4, %4 row_ror:8 row_mask:0xf bank_mask:0xf\n\tv_add_f32_dpp %5, %5, %5 row_ror:8 row_mask:0xf bank_mask:0xf\n\t"
                         "v_add_f32_dpp %6, %6, %6 row_ror:8 row_mask:0xf bank_mask:0xf\n\tv_add_f32_dpp %7, %7, %7 row_ror:8 row_mask:0xf bank_mask:0xf\n\t"
                         "s_nop 1\n\tv_permlane16_swap_b32 %0, %4\n\tv_permlane16_swap_b32 %1, %5\n\tv_permlane16_swap_b32 %2, %6\n\t"
                         "v_permlane16_swap_b32 %3, %7\n\tv_add_f32 %0, %0, %4\n\tv_add_f32 %1, %1, %5\n\tv_add_f32 %2, %2, %6\n\t"
                         "v_add_f32 %3, %3, %7\n\ts_nop 1\n\tv_permlane32_swap_b32 %0, %2\n\tv_permlane32_swap_b32 %1, %3\n\t"
                         "v_add_f32 %0, %0, %2\n\tv_add_f32 %1, %1, %3"
                         : "+v"(sm[0]), "+v"(sm[1]), "+v"(sm[2]), "+v"(sm[3]), "+v"(sm[4]), "+v"(sm[5]), "+v"(sm[6]), "+v"(sm[7]));
            if ((lane & 8) == 0 && !idle) {
                const __amdgpu_buffer_rsrc_t r = __builtin_amdgcn_make_buffer_rsrc(cs, 0, 0x7fffffff, 0x00020000);
                __builtin_amdgcn_raw_buffer_store_b64(u32x2{__float_as_uint(sm[0]), __float_as_uint(sm[1])}, r,
                    (unsigned)((kh * 4 + h) * 2 * F_ + cb * 64 + (lane & 7) * 8 + ((lane >> 4) & 1) * 4 + (lane >> 5) * 2) * 4u, 0, 16);
            }
        } else {
            const unsigned* const f = pflags + ((tm && (lane & 16) ? tm - 1 : tm) * 16 + (lane & 15)) * 32;
            unsigned have = 0u;
            auto stage = [&](int st, int sl, auto bc) INL {
                const int kt = qkv_k(st);
                await(have, 0x10001u << kt, f, tok);
                const unsigned da = lds0 + sl * QAS;
#pragma unroll
                for (int i = 0; i < 4; ++i) glds16_dev(uni((const char*)A + kt * 128 + i * 32 * D_ * 2), la, da + h * 1024 + i * 4096);
                glds16_part<32>(uni((const char*)A + kt * 128), lh, da + 16384 + h * 512);
                if constexpr (decltype(bc)::value) qkv_stage_b(lds0, B, lb, h, kt, sl);
            };
            stage(0, 0, CT<0>{});
            stage(1, 1, CT<0>{});
            wait_vm<5>();
            BARRIER();
            stage(2, 2, CT<0>{});
            att_setup(atc, lds0, wid, lane, m0, tn);
            if (h == 0) att_put(atc, smem, lane);
            wait_vm<5>();
            BARRIER();
            stage(3, 0, CT<1>{});
            int sl = 1;
#pragma unroll 1
            for (int st = 4; st < NK; ++st) {
                wait_vm<11>();
                BARRIER();
                stage(st, sl, CT<1>{});
                sl = sl == 2 ? 0 : sl + 1;
            }
            f32x4 spv[8], cstv;
            {
                const int srow = m0 - 16 + (hl < 144 ? hl & ~1 : 0);
                const int sr = srow < 0 ? 0 : srow;
                const unsigned so = (unsigned)((sr >> 5) * 512 + (hl & 1) * 256 + (sr & 31)) * 8u;
                gload16_dev(spv[0], sp, so);
                static_for<1, 8>([&](auto ic) INL { gload16_devo<decltype(ic)::value * 256>(spv[decltype(ic)::value], sp, so); });
                gload16_dev(cstv, cso, (unsigned)(hl < 48 ? hl : 0) * 16u);
            }
            wait_vm<20>();
            BARRIER();
            wo_pre(1);
            wait_vm<15>();
            BARRIER();
            wo_pre(2);
            wait_vm<12>();
            asm volatile("" : "+v"(cstv));
            float sm[4];
#pragma unroll
            for (int i = 0; i < 8; ++i) {
                asm volatile("" : "+v"(spv[i]));
#pragma unroll
                for (int e = 0; e < 4; ++e) sm[e] = i ? addf(sm[e], spv[i][e]) : spv[i][e];
            }
#pragma unroll
            for (int e = 0; e < 4; ++e)
                sm[e] = addf(sm[e], __uint_as_float(__builtin_amdgcn_update_dpp(0u, __float_as_uint(sm[e]), 0xB1, 0xf, 0xf, false)));
            const float mean = mulf(hl & 1 ? sm[2] : sm[0], 1.f / D_);
            const float var = fmaxf(subf(mulf(hl & 1 ? sm[3] : sm[1], 1.f / D_), mulf(mean, mean)), 0.f);
            const float sd = rbf(sqrtf(rbf(addf(rbf(var), 1e-5f))));
            if (hl < 144) *(float2*)(stat + 2 * hl) = make_float2(rbf(mean), 1.f / sd);
            if (hl < 48) *(f32x4*)(csl + hl * 4) = cstv;
            WAIT_LGKM0();
            BARRIER();
        }
        wo_stage_b(lds0, wo, wb, h, 0);
        wo_stage_b(lds0, wo, wb, h, 1);
    } else {
        const int wm = wid >> 1, wn = wid & 1;
        const int l15 = lane & 15, g = lane >> 4, q = l15 >> 2, p4 = l15 & 3;
        unsigned ab[2], hb[2], bj[6];
#pragma unroll
        for (int kk = 0; kk < 2; ++kk) {
            const unsigned sw = ((kk * 4 + g) ^ (l15 >> 1)) * 16;
            ab[kk] = opaque(lds0 + (wm * 64 + l15) * 128 + sw);
            hb[kk] = opaque(lds0 + (128 + l15) * 128 + sw);
        }
#pragma unroll
        for (int j = 0; j < 6; ++j) {
            const int jj = wm && j >= 2 ? 3 - (j >> 1) : j >> 1;
            bj[j] = opaque(lds0 + QBR + jj * 8192 + (g * 8 + q) * 128 + p4 * 8 + (((wn * 2 + ((j & 1) ^ (j < 2 ? wm : 0))) ^ ((q >> 1) | ((g & 1) << 1))) << 5));
        }
        f32x4 acc[5][6];
        constexpr auto qcol = [](int c) { return c == 0 ? 1 : (c < 3 ? c + 3 : (c == 3 ? 0 : c - 2)); };
        auto ldop = [&](bf16x8 (&af)[10], s16x4 (&bf)[24], auto ktc, auto oc) INL {
            constexpr int KT = decltype(ktc)::value, O = decltype(oc)::value;
            if constexpr (O == 2 || (O >= 5 && O < 12)) {
                constexpr int X = O == 2 ? 0 : O - 4;
                af[X] = lds_a<(KT % 3) * QAS + (X / 2) * 2048>(ab[X & 1]);
            } else if constexpr (O == 24 || O == 25) {
                af[O - 16] = lds_a<(KT % 3) * QAS>(hb[O & 1]);
            } else {
                constexpr int X = O < 2 ? O : (O < 5 ? O - 1 : (O < 24 ? O - 8 : O - 10));
                bf[X] = lds_h<(KT % 3) * QBS + ((X >> 1) & 1) * 4096 + (X & 1) * 512>(bj[qcol(X / 4)]);
            }
        };
        unsigned ha[3][2];
        f32x4 rg[3];
        bf16x8 gf[2];
        auto hrow = [&](int i, int l) INL {
            const int gi = wid * 3 + i, mr = gi * 15 + l - 1;
            return l == 0 ? 128 : (gi < 9 ? (mr < 128 ? mr : 127) : 128 + l);
        };
        auto unit = [&](bf16x8 (&af)[10], s16x4 (&bf)[24], bf16x8 (&an)[10], s16x4 (&bn)[24], auto ktc, auto hc, auto fic) INL {
            constexpr bool FI = decltype(fic)::value;
            constexpr int KT = decltype(ktc)::value, HF = decltype(hc)::value;
            static_for<0, 26>([&](auto tc) INL {
                constexpr int T = decltype(tc)::value, X = HF * 26 + T, kk = X & 1;
                constexpr int c = X < 32 ? X / 8 : (X < 42 ? 4 : 5), p = (X < 32 ? X % 8 : (X < 42 ? X - 32 : X - 42)) / 2;
                constexpr int i = c == 5 ? 4 - p : (c & 1 ? 3 - p : p);
                mma_ip(cat(bf[c * 4 + kk * 2], bf[c * 4 + kk * 2 + 1]), af[i * 2 + kk], acc[i][qcol(c)]);
                SCHED();
                if constexpr (HF == 1) {
                    if constexpr (KT + 1 < NK && T < QG1) ldop(an, bn, CT<KT + 1>{}, tc);
                    if constexpr (KT + 1 < NK && T >= QL1 && T < QL1 + 16 - QG1) ldop(an, bn, CT<KT + 1>{}, CT<T - QL1 + QG1>{});
                } else if constexpr (FI) {
                    if constexpr (T % 8 < 2 && T < 24) gf[T & 1] = lds_a<(KT % 3) * QAS>(ha[T / 8][T & 1]);
                    else if constexpr (T < 20) ldop(af, bf, ktc, CT<14 + T>{});
                } else {
                    if constexpr (T < 18) ldop(af, bf, ktc, CT<16 + T>{});
                }
                SCHED();
                if constexpr (FI && HF == 0 && T % 8 == 7) {
                    mma_ip(gf[0], gf[0], rg[T / 8]);
                    SCHED();
                    if constexpr (T < 16) ldop(af, bf, ktc, CT<16 + T - 1>{});
                    SCHED();
                    mma_ip(gf[1], gf[1], rg[T / 8]);
                    SCHED();
                    if constexpr (T < 16) ldop(af, bf, ktc, CT<16 + T>{});
                    SCHED();
                }
                if constexpr (FI && X == 31) {
                    mma_ip(cat(bf[12], bf[13]), af[8], acc[4][0]);
                    SCHED();
                    mma_ip(cat(bf[14], bf[15]), af[9], acc[4][0]);
                    SCHED();
                }
            });
        };
        if (first) token();
        if (!first && id == 0 && wid == 3) {
#pragma unroll 1
            for (unsigned spins = 0; spins < (1u << 22); ++spins) {
                bool ok = true;
#pragma unroll
                for (int i = 0; i < 4; ++i)
                    ok &= __hip_atomic_load((gu64*)(oflags + 8192 + (i * 64 + lane) * 32), __ATOMIC_RELAXED, __HIP_MEMORY_SCOPE_AGENT) == tok;
                if (__all(ok)) break;
            }
            if (lane == 0) __hip_atomic_store((gu64*)epoch, tok, __ATOMIC_RELAXED, __HIP_MEMORY_SCOPE_AGENT);
        }
        bf16x8 a0[10], a1[10], a2[10];
        s16x4 b0[24], b1[24], b2[24];
        auto loop = [&](auto fic) INL {
#pragma unroll
        for (int i = 0; i < 5; ++i)
#pragma unroll
            for (int j = 0; j < 6; ++j)
                if (i < 4 || (j >> 1) == 1) {
                    acc[i][j] = f32x4{0.f, 0.f, 0.f, 0.f};
                    asm volatile("" : "+v"(acc[i][j]));
                }
            if constexpr (decltype(fic)::value) wait_vm<11>();
            BARRIER();
            static_for<0, 16>([&](auto oc) INL { ldop(a0, b0, CT<0>{}, oc); });
            auto ktile = [&](bf16x8 (&af)[10], s16x4 (&bf)[24], bf16x8 (&an)[10], s16x4 (&bn)[24], auto kc) INL {
                unit(af, bf, an, bn, kc, CT<0>{}, fic);
                WAIT_LGKM0();
                if constexpr (decltype(kc)::value == 0 && decltype(fic)::value) wait_vm<0>();
                BARRIER();
                unit(af, bf, an, bn, kc, CT<1>{}, fic);
            };
#pragma unroll 1
            for (int it = 0; it < 5; ++it) {
                ktile(a0, b0, a1, b1, CT<0>{});
                ktile(a1, b1, a2, b2, CT<1>{});
                ktile(a2, b2, a0, b0, CT<2>{});
            }
            unit(a0, b0, a1, b1, CT<NK - 1>{}, CT<0>{}, fic);
            if constexpr (decltype(fic)::value) {
                asm volatile("s_nop 15" ::: "memory");
                SCHED();
#pragma unroll
                for (int i = 0; i < 3; ++i) *(f32x4*)(smem + 131072 + (wid * 3 + i) * 1024 + (opaque(threadIdx.x) & 63u) * 16) = rg[i];
                WAIT_LGKM0();
                BARRIER();
                SCHED();
            }
            unit(a0, b0, a1, b1, CT<NK - 1>{}, CT<1>{}, fic);
            asm volatile("s_nop 15\n\ts_nop 7" ::: "memory");
            SCHED();
        };
        if (first) {
#pragma unroll
            for (int i = 0; i < 3; ++i) {
                const int hr = hrow(i, l15);
#pragma unroll
                for (int kk = 0; kk < 2; ++kk) ha[i][kk] = opaque(lds0 + hr * 128 + (((kk * 4 + g) ^ ((hr >> 1) & 7)) * 16));
                rg[i] = f32x4{0.f, 0.f, 0.f, 0.f};
                asm volatile("" : "+v"(rg[i]));
            }
            acc[4][0] = f32x4{0.f, 0.f, 0.f, 0.f};
            asm volatile("" : "+v"(acc[4][0]));
            loop(CT<1>{});
            const int le = (int)opaque(threadIdx.x) & 63, l = le & 15;
            if (l == 0) {
#pragma unroll
                for (int x = 0; x < 3; ++x) {
                    const int col = (x ? (1 + wm) * 64 + (x - 1) * 16 : wm * 16) + wn * 32 + (le >> 4) * 4;
                    *(f32x4*)(csl + col) = acc[4][x ? 1 + x : 0];
                }
            }
        } else {
            loop(CT<0>{});
        }
        const int tide = (int)opaque(threadIdx.x);
        const int l15e = tide & 15, ge = (tide & 63) >> 4;
        WAIT_LGKM0();
        BARRIER();
        f32x4 ccv[6];
        float2 stv[5];
        auto colof = [&](int j) INL { return (wm && j >= 2 ? 3 - (j >> 1) : j >> 1) * 64 + wn * 32 + ((j & 1) ^ (j < 2 ? wm : 0)) * 16 + ge * 4; };
#pragma unroll
        for (int j = 0; j < 6; ++j) ccv[j] = *(const f32x4*)(csl + colof(j));
#pragma unroll
        for (int i = 0; i < 5; ++i) stv[i] = *(const float2*)(stat + 2 * ((i < 4 ? 16 + wm * 64 + i * 16 : 0) + l15e));
#pragma unroll
        for (int i = 0; i < 5; ++i) {
            const int row = (i < 4 ? 16 + wm * 64 + i * 16 : 0) + l15e;
            const float rstd = stv[i].y, mr = -stv[i].x * rstd;
#pragma unroll
            for (int j = 0; j < 6; ++j)
                if (i < 4 || (j >> 1) == 1) {
                    const f32x2 r2 = {rstd, rstd}, m2 = {mr, mr};
                    const f32x2 y0 = __builtin_elementwise_fma(acc[i][j].xy, r2, m2 * ccv[j].xy);
                    const f32x2 y1 = __builtin_elementwise_fma(acc[i][j].zw, r2, m2 * ccv[j].zw);
                    u32x2 v = {pk2(y0.x, y0.y), pk2(y1.x, y1.y)};
                    *(u32x2*)(smem + row * ACS + colof(j) * 2) = v;
                }
        }
        SCHED();
        att_get(atc, smem, wid, tide & 63, m0);
    }
    WAIT_LGKM0();
    BARRIER();
    asm volatile("" ::: "memory");
    att_run(atc, C);
    publish(oflags + (tm * 16 + tn) * 32, tok, 65280);
    if (wid >= 4) {
        const unsigned wb = wo_boff(wid - 4, lane);
#pragma unroll
        for (int st = 8; st < WONB; ++st) wo_stage_b(lds0, wo, wb, wid - 4, st);
    }
}

__device__ __forceinline__ unsigned dn_boff(int h, int lane) { return b_off(h, lane, D_, (bid_() >> 5) * 128); }
__device__ __forceinline__ void dn_stage_b(unsigned lds0, const u16* B, unsigned boff, int kh, int h, int i, int kg, int sl) {
    glds16(uni((const char*)B + (long long)(kg * 64 + (i & 1) * 32) * D_ * 2 + ((i >> 1) ^ kh) * 128), boff,
           lds0 + h * 1024 + sl * 32768 + 16384 + i * 4096);
}
__device__ __forceinline__ void dn_pre(unsigned lds0, const u16* B, int wid, int lane) {
    const unsigned boff = dn_boff(wid & 3, lane);
    const int kh = (bid_() >> 2) & 1, kg0 = kh * ((F_ / 64 - NSOLO) / 2);
#pragma unroll
    for (int p = 0; p < 8; ++p) {
        const int sl = (p >> 2) * 2 + (wid >> 2);
        dn_stage_b(lds0, B, boff, kh, wid & 3, p & 3, kg0 + sl, sl);
    }
}
__device__ __forceinline__ void dn_phase(char* smem, const u16* A, const u16* __restrict__ B, u16* C, const u16* R, float* sp,
                                         float* slab, unsigned* flags, const unsigned* gflags, unsigned* oflags,
                                         const u16* pf, u64 tok) {
    constexpr int BM = 128, NS = 4, NSO = NSOLO, NSH = (F_ / 64 - NSO) / 2, NK = NSH + NSO;
    static_assert(NSO % 2 == 0, "k-tile program");
    constexpr int NRT = (NK - 1 < NSH ? NK - 1 : NSH) / NS * NS;
    constexpr int ASZ = BM * 128, SLOT = 2 * ASZ;
    constexpr int lda = F_, ldc = D_;
    constexpr int BOX = 16384, CSTR = 64 * 2 + 16;

    const int tid = (int)opaque(threadIdx.x), lane = tid & 63;
    const int wid = __builtin_amdgcn_readfirstlane(tid >> 6);
    const unsigned bx = bid_();
    const int xcd = bx & 7, r_ = bx >> 3;
    const int kh = xcd >> 2, tm = (xcd & 3) * 4 + (r_ & 3), tn = r_ >> 2;
    const int m0 = tm * BM, n0 = tn * 128 + kh * 64;
    const int tile = tm * 8 + tn;
    const unsigned lds0 = __builtin_amdgcn_readfirstlane((unsigned)(size_t)(LDS_AS char*)smem);
    f32x4 resf[2];
    auto load_res = [&]() INL {
#pragma unroll
        for (int it = 0; it < 2; ++it) gload16_dev(resf[it], R, (unsigned)((m0 + res_row(tid, it)) * ldc + n0 + (tid & 7) * 8) * 2u);
    };
    if (wid >= 4) {
        const int h = wid - 4;
        const unsigned aoff = (unsigned)((m0 / 4 + h * 2 + (lane >> 5)) * (8 * F_) + (((lane >> 3) & 3) ^ (h & 1)) * 128 + (lane & 7) * 16);
        const unsigned boff = dn_boff(h, lane);
        const unsigned* const f = gflags + ((tm >> 1) * 32 + (lane & 31)) * 32;
        unsigned have = 0u;
        auto stage = [&](int kg, int sl, auto nbc) INL {
            await(have, (1u << (kg * 4 / 7)) | (1u << ((kg * 64 + 63) / 112)), f, tok);
#pragma unroll
            for (int i = 0; i < 4; ++i)
                glds16_dev(uni((const char*)A + kg * 512 + i * 32 * lda * 2), aoff, lds0 + h * 1024 + sl * SLOT + i * 4096);
#pragma unroll
            for (int i = 0; i < decltype(nbc)::value; ++i) dn_stage_b(lds0, B, boff, kh, h, i, kg, sl);
        };
        const int kg0 = kh * NSH;
        constexpr int NB0 = 0;
        stage(kg0, 0, CT<NB0>{});
        stage(kg0 + 1, 1, CT<NB0>{});
        wait_vm<4 + NB0>();
        BARRIER();
        stage(kg0 + 2, 2, CT<NB0>{});
        stage(kg0 + 3, 3, CT<NB0>{});
        wait_vm<8 + 2 * NB0>();
        BARRIER();
        stage(kg0 + 4, 0, CT<4>{});
        wait_vm<12 + NB0>();
        BARRIER();
        stage(kg0 + 5, 1, CT<4>{});
#pragma unroll 1
        for (int kt = 2; kt < NSH - NS; ++kt) {
            wait_vm<16>();
            BARRIER();
            stage(kg0 + kt + NS, kt & 3, CT<4>{});
        }
        const unsigned* const myf = flags + (tile * 2 + kh) * 16 + h * 2;
        u64 look[2];
        unsigned got = 0u;
        auto fetch = [&]() INL {
#pragma unroll
            for (int i = 0; i < 4; ++i)
                glds16_dev(uni((const char*)slab + (long long)(tile * 2 + kh) * BOX + (h * 4 + i) * 1024), lane * 16,
                           lds0 + NS * SLOT + (h * 4 + i) * 1024);
        };
        static_for<NSH - NS, NK - 1>([&](auto kc) INL {
            constexpr int KT = decltype(kc)::value;
            constexpr int P2 = KT + 2 < NSH ? 8 : (KT + 2 < NK ? 6 : 0), P3 = KT + 3 < NSH ? 8 : (KT + 3 < NK ? 6 : 0);
            constexpr int PQ = KT > NK - 4 ? (KT - NK + 4) * 6 : 0;
            if (PQ && pf != nullptr) wait_vm<P2 + P3 + PQ>(); else wait_vm<P2 + P3>();
            BARRIER();
            if constexpr (KT == NK - 6 || KT == NK - 4)
                asm volatile("s_nop 4\n\tglobal_load_dwordx2 %0, %1, %2 sc1" : "=v"(look[KT == NK - 4]) : "v"(0u), "s"(myf) : "memory");
            if constexpr (KT >= NK - 3) {
                asm volatile("" : "+v"(look[KT - NK + 3]));
                if (!got && (u64)uni((const char*)look[KT - NK + 3]) == tok) { got = 1u; fetch(); }
            }
            if constexpr (KT + NS < NK) stage(NSH + KT + NS, KT & 3, CT<2>{});
            if constexpr (KT >= NK - 4) { if (pf != nullptr) qkv_stage_b(lds0, pf, qkv_boff(h, lane), h, qkv_k(KT - NK + 4), KT - NK + 4); }
        });
        if (!got) {
            await(got, 1u, myf, tok);
            fetch();
        }
        load_res();
        wait_vm<2>();
        BARRIER();
        wait_vm<0>();
        asm volatile("" : "+v"(resf[0]), "+v"(resf[1]));
    } else {
        const int wm = wid >> 1, wn = wid & 1;
        const int l15 = lane & 15, g = lane >> 4, q = l15 >> 2, p4 = l15 & 3;
        unsigned ab[2][2], bb[2][2];
#pragma unroll
        for (int x = 0; x < 2; ++x) {
            const unsigned a = lds0 + (wm * 16 + q) * 512 + (((x * 2 + (g >> 1)) ^ (l15 >> 3)) * 128) + p4 * 32 + (g & 1) * 16;
            const unsigned b = lds0 + ASZ + (g * 8 + q) * 128 + p4 * 8 + (((wn * 2 + x) ^ ((q >> 1) | ((g & 1) << 1))) << 5);
            ab[x][0] = opaque(a); ab[x][1] = opaque(a + 2 * SLOT);
            bb[x][0] = opaque(b); bb[x][1] = opaque(b + 2 * SLOT);
        }
        f32x4 acc[4][4];
#pragma unroll
        for (int i = 0; i < 4; ++i)
#pragma unroll
            for (int j = 0; j < 4; ++j) {
                acc[i][j] = f32x4{0.f, 0.f, 0.f, 0.f};
                asm volatile("" : "+v"(acc[i][j]));
            }
        auto ldop = [&](bf16x8 (&af)[8], s16x4 (&bf)[16], auto slc, auto j0c, auto oc) INL {
            constexpr int SL = decltype(slc)::value, O = decltype(oc)::value;
            if constexpr (O == 2 || (O >= 5 && O < 12)) {
                constexpr int X = O == 2 ? 0 : O - 4;
                af[(X & 1) * 4 + X / 2] = lds_a<(SL & 1) * SLOT + (X / 2) * 2048>(ab[X & 1][SL >> 1]);
            } else {
                constexpr int X = decltype(j0c)::value * 4 + (O < 2 ? O : (O < 5 ? O - 1 : O - 8)), J = X / 4;
                bf[X] = lds_h<(SL & 1) * SLOT + (J < 2 ? 8192 : 0) + ((X >> 1) & 1) * 4096 + (X & 1) * 512>(bb[J & 1][SL >> 1]);
            }
        };
        auto ktile = [&](bf16x8 (&af)[8], s16x4 (&bf)[16], bf16x8 (&an)[8], s16x4 (&bn)[16], auto nslc, auto j0c, auto jnc, auto&& pre) INL {
            constexpr int J0 = decltype(j0c)::value, JN = decltype(jnc)::value;
            constexpr int NM = 8 * (4 - J0), NL = JN < 4 ? 8 + 4 * (4 - JN) : 0, TB = J0 ? DBAR2 : DBAR;
            constexpr int NG = NM - TB < NL ? NM - TB : NL;
            static_for<0, NM>([&](auto tc) INL {
                constexpr int T = decltype(tc)::value, j = J0 + T / 8, i = j & 1 ? 3 - (T / 2) % 4 : (T / 2) % 4, kk = T & 1;
                if constexpr (T == TB) {
                    pre();
                    if constexpr (NL > 0) {
                        WAIT_LGKM0();
                        BARRIER();
                    }
                }
                mma_ip(cat(bf[j * 4 + kk * 2], bf[j * 4 + kk * 2 + 1]), af[kk * 4 + i], acc[i][j]);
                SCHED();
                if constexpr (NL > 0 && T >= TB) {
                    constexpr int U = T - TB, lo_ = (U * NL + NG - 1) / NG, hi_ = ((U + 1) * NL + NG - 1) / NG;
                    static_for<lo_, (hi_ < NL ? hi_ : NL)>([&](auto oc) INL { ldop(an, bn, nslc, jnc, oc); });
                }
                SCHED();
            });
        };
        bf16x8 a0[8], a1[8];
        s16x4 b0[16], b1[16];
        wait_vm<0>();
        BARRIER();
        static_for<0, 24>([&](auto oc) INL { ldop(a0, b0, CT<0>{}, CT<0>{}, oc); });
        auto none = [&]() INL {};
#pragma unroll 1
        for (int kt = 0; kt < NRT; kt += NS) {
            ktile(a0, b0, a1, b1, CT<1>{}, CT<0>{}, CT<0>{}, none);
            ktile(a1, b1, a0, b0, CT<2>{}, CT<0>{}, CT<0>{}, none);
            ktile(a0, b0, a1, b1, CT<3>{}, CT<0>{}, CT<0>{}, none);
            ktile(a1, b1, a0, b0, CT<0>{}, CT<0>{}, CT<0>{}, none);
        }
        static_for<NRT, NK>([&](auto sc) INL {
            constexpr int KT = decltype(sc)::value, ST = KT - NSH;
            constexpr int J0 = KT < NSH ? 0 : 2, JN = KT + 1 < NSH ? 0 : (KT + 1 < NK ? 2 : 4);
            if constexpr (ST == 0) {
                char* out = (char*)slab + (long long)(tile * 2 + (kh ^ 1)) * BOX;
#pragma unroll
                for (int i = 0; i < 4; ++i) {
                    const u32x4 w = {pk2(acc[i][0][0], acc[i][0][1]), pk2(acc[i][0][2], acc[i][0][3]),
                                     pk2(acc[i][1][0], acc[i][1][1]), pk2(acc[i][1][2], acc[i][1][3])};
                    store16_wt(out, (unsigned)(((wid * 4 + i) * 64 + lane) * 16), w);
                }
                SCHED();
            }
            auto pre = [&]() INL {
                if constexpr (ST == FLAGST) {
                    wait_vm<0>();
                    if (lane == 0)
                        __hip_atomic_store((gu64*)(flags + (tile * 2 + (kh ^ 1)) * 16 + wid * 2), tok, __ATOMIC_RELAXED,
                                           __HIP_MEMORY_SCOPE_AGENT);
                    SCHED();
                }
                if constexpr (KT == NK - 1) { load_res(); SCHED(); }
            };
            if constexpr (KT & 1) ktile(a1, b1, a0, b0, CT<(KT + 1) % NS>{}, CT<J0>{}, CT<JN>{}, pre);
            else ktile(a0, b0, a1, b1, CT<(KT + 1) % NS>{}, CT<J0>{}, CT<JN>{}, pre);
        });
        asm volatile("s_nop 15\n\ts_nop 7" ::: "memory");
        SCHED();
        BARRIER();
        asm volatile("" ::: "memory");
        u32x4 pv[4];
#pragma unroll
        for (int i = 0; i < 4; ++i) pv[i] = *(const u32x4*)(smem + NS * SLOT + ((wid * 4 + i) * 64 + lane) * 16);
#pragma unroll
        for (int i = 0; i < 4; ++i)
#pragma unroll
            for (int x = 0; x < 2; ++x) {
                const unsigned w0 = pv[i][2 * x], w1 = pv[i][2 * x + 1];
                const f32x4 y = acc[i][2 + x] + f32x4{lo16(w0), hi16(w0), lo16(w1), hi16(w1)};
                u32x2 v = {pk2(y[0], y[1]), pk2(y[2], y[3])};
                *(u32x2*)(smem + (wm * 64 + i * 16 + l15) * CSTR + (wn * 32 + x * 16 + g * 4) * 2) = v;
            }
        wait_vm<0>();
        asm volatile("" : "+v"(resf[0]), "+v"(resf[1]));
    }
    constexpr unsigned tq = SINK;
    if (pf != nullptr && (bx & 1) == 0 && wid >= 6) {
        const unsigned ix = (bx >> 3) * 96 + (tid < 480 ? tid - 384 : 0);
        touch1(tq, pf, ix * 2048 + ((bx & 3) * 4 + 3) * 128);
    }
    WAIT_LGKM0();
    BARRIER();
    u32x4 resv[2];
#pragma unroll
    for (int it = 0; it < 2; ++it) {
        asm volatile("" : "+v"(resf[it]));
        resv[it] = __builtin_bit_cast(u32x4, resf[it]);
    }
    res_tail(smem, resv, C, sp, m0, n0, tid);
    if (pf != nullptr) publish(oflags + (tm * 16 + (n0 >> 6)) * 32, tok);
}

__device__ __forceinline__ void gu_phase(char* smem, const u16* A, const u16* B0, const u16* B1, u16* C, const float* cs,
                                         const float* sp, const u16* wd, bool first, unsigned* flags, u64 tok) {
    constexpr int BM = 256, BN = 224, TM = 64, MI = 4, NI = 7, NK = D_ / 64;
    constexpr int ASZ = BM * 128, BSZ = BN * 128, NSA = 3, NSB = 2;
    constexpr int BRING = NSA * ASZ, RING = BRING + NSB * BSZ;

    const int tid = (int)opaque(threadIdx.x), lane = tid & 63;
    const int wid = __builtin_amdgcn_readfirstlane(tid >> 6);
    const int wm = wid >> 1, wn = wid & 1;
    const int id = wg_id();
    const int tm = id & 7, tn = id >> 3, m0 = tm * BM;
    float* const csl = (float*)(smem + RING);
    const unsigned lds0 = __builtin_amdgcn_readfirstlane((unsigned)(size_t)(LDS_AS char*)smem);
    const unsigned ldsw = lds0 + wid * 1024;

    unsigned aoff[4], boff[4];
#pragma unroll
    for (int i = 0; i < 4; ++i) {
        const int row = tid >> 3, o = i * 8192 + tid * 16;
        aoff[i] = opaque((unsigned)((m0 + i * 64 + row) * D_ + ((tid & 7) ^ ((row >> 1) & 7)) * 8) * 2u);
        boff[i] = opaque(gu_bsrc(o < BSZ ? o : 0, tn));
    }
    const __amdgpu_buffer_rsrc_t ra = __builtin_amdgcn_make_buffer_rsrc((void*)A, 0, 0x7fffffff, 0x00020000);
    const __amdgpu_buffer_rsrc_t rb[4] = {__builtin_amdgcn_make_buffer_rsrc((void*)B0, 0, 0x7fffffff, 0x00020000),
                                          __builtin_amdgcn_make_buffer_rsrc((void*)(wid >= 6 ? B1 : B0), 0, 0x7fffffff, 0x00020000),
                                          __builtin_amdgcn_make_buffer_rsrc((void*)B1, 0, 0x7fffffff, 0x00020000),
                                          __builtin_amdgcn_make_buffer_rsrc((void*)B1, 0, wid < 4 ? 0x7fffffff : 0, 0x00020000)};
    const unsigned b3d[2] = {wid < 4 ? ldsw + BRING + 24576 : lds0 + 161792, wid < 4 ? ldsw + BRING + BSZ + 24576 : lds0 + 161792};
    unsigned soff = 0u;
    auto prep = [&](auto bc, auto pc, auto ktc) INL {
        constexpr int P = decltype(pc)::value, KT = decltype(ktc)::value;
        constexpr bool ISB = decltype(bc)::value;
        if constexpr (P == 0) s_set<(ISB ? KT * 64 * F_ * 2 : KT * 128)>(soff);
        if constexpr (ISB && P == 3) m0_set<0>(b3d[KT % NSB]);
        else m0_set<(ISB ? BRING + (KT % NSB) * BSZ : (KT % NSA) * ASZ) + P * 8192>(ldsw);
    };
    auto go = [&](auto bc, auto pc) INL {
        if constexpr (decltype(bc)::value) blds16<false>(rb[decltype(pc)::value], boff[decltype(pc)::value], soff);
        else blds16<true>(ra, aoff[decltype(pc)::value], soff);
    };
    auto stage_a = [&](auto ktc) INL {
        static_for<0, 4>([&](auto pc) INL { prep(CT<0>{}, pc, ktc); asm volatile("s_nop 0"); go(CT<0>{}, pc); });
    };
    unsigned tof;
    {
        const unsigned t = tid & 255, kr = ((id >> 7) & 1) * 1664, j = (id & 31) * 256 + t;
        unsigned rw = j >> 2;
        rw = rw < 1664 ? kr + rw : (rw < 1920 ? rw + 1664 : kr);
        tof = opaque(wid < 4 ? rw * 2048 + 256 + (j & 1) * 128 + (j & 2) * 512 : (first ? (id * 224 + (t < 224 ? t : 0u)) * 128u : 0u));
    }
    __builtin_amdgcn_s_setprio(2);

    const int l15 = lane & 15, g = lane >> 4;
    const int q = l15 >> 2, p4 = l15 & 3;
    unsigned ab[2][2], bb[2][2];
#pragma unroll
    for (int kk = 0; kk < 2; ++kk) {
        const unsigned sw = ((kk * 4 + g) ^ (l15 >> 1)) * 16;
        ab[kk][0] = opaque(lds0 + (wm * TM + l15) * 128 + sw);
        ab[kk][1] = opaque(lds0 + (wm * TM + l15) * 128 + sw + 65536);
        const unsigned r = lds0 + BRING + ((g * 8 + kk * 4 + q) ^ (4 * (g & 1))) * BN;
        bb[kk][0] = opaque(r + wn * 128 + p4 * 8);
        bb[kk][1] = opaque(r + 96 + wn * 16 + (p4 & 1) * 8 + (p4 >> 1) * (BSZ / 2));
    }
    f32x4 acc[MI][NI];
#pragma unroll
    for (int i = 0; i < MI; ++i)
#pragma unroll
        for (int j = 0; j < NI; ++j) {
            acc[i][j] = f32x4{0.f, 0.f, 0.f, 0.f};
            asm volatile("" : "+v"(acc[i][j]));
        }
    bf16x8 af[2][2 * MI];
    s16x4 bf[2][4 * NI];
    auto ldop = [&](auto ktc, auto oc) INL {
        constexpr int KT = decltype(ktc)::value, O = decltype(oc)::value;
        if constexpr (O == 2 || (O >= 5 && O < 12)) {
            constexpr int X = O == 2 ? 0 : O - 4, off = (KT % NSA) * ASZ + (X / 2) * 2048;
            af[KT & 1][X] = lds_a<(off & 65535)>(ab[X & 1][off >> 16]);
        } else {
            constexpr int X = O < 2 ? O : (O < 5 ? O - 1 : O - 8), J = X / 4;
            bf[KT & 1][X] = lds_h<(KT % NSB) * BSZ + ((X >> 1) & 1) * 32 * BN + (J == 6 ? 0 : (J / 3) * (BSZ / 2) + (J % 3) * 32)>(bb[X & 1][J == 6]);
        }
    };
    f32x4 stn[4];
    float sx[8];
    const unsigned sto = opaque(wid * 4096 + g * 1024 + l15 * 16);
    const char* const spw = uni((const char*)sp + m0 * 128);
    auto stats = [&](auto zc) INL {
        constexpr int Z = decltype(zc)::value;
        if constexpr (Z == 0) {
#pragma unroll
            for (int i = 0; i < 4; ++i) asm volatile("" : "+v"(stn[i]));
        }
        if constexpr (Z < 8) asm volatile("v_add_f32 %0, %1, %2" : "=v"(sx[Z]) : "v"(stn[Z / 4 * 2][Z % 4]), "v"(stn[Z / 4 * 2 + 1][Z % 4]));
        else if constexpr (Z < 12) asm volatile("v_add_f32 %0, %0, %1" : "+v"(sx[(Z & 1) * 2 + (Z / 2 & 1)]) : "v"(sx[4 + (Z & 1) * 2 + (Z / 2 & 1)]));
        else if constexpr (Z < 14) asm volatile("v_permlane16_swap_b32 %0, %1" : "+v"(sx[Z - 12]), "+v"(sx[Z - 10]));
        else if constexpr (Z < 16) asm volatile("v_add_f32 %0, %0, %1" : "+v"(sx[Z - 14]) : "v"(sx[Z - 12]));
        else if constexpr (Z == 17 || Z == 22) asm volatile("v_permlane32_swap_b32 %0, %1" : "+v"(sx[0]), "+v"(sx[1]));
        else if constexpr (Z == 18) asm volatile("v_add_f32 %0, %0, %1" : "+v"(sx[0]) : "v"(sx[1]));
        else if constexpr (Z == 19) asm volatile("v_mul_f32 %0, 0x3a800000, %1" : "=v"(sx[1]) : "v"(sx[0]));
        else if constexpr (Z == 20) asm volatile("v_mul_f32 %0, 0x3a800000, %0" : "+v"(sx[0]));
        else if constexpr (Z == 23) asm volatile("v_mul_f32 %0, %1, %1" : "=v"(sx[2]) : "v"(sx[0]));
        else if constexpr (Z == 24) asm volatile("v_sub_f32 %0, %0, %1" : "+v"(sx[1]) : "v"(sx[2]));
        else if constexpr (Z == 25) asm volatile("v_max_f32 %0, 0, %0" : "+v"(sx[1]));
        else if constexpr (Z == 26) asm volatile("v_cvt_pk_bf16_f32 %0, %0, -%1" : "+v"(sx[1]) : "v"(sx[0]));
        else if constexpr (Z == 27) asm volatile("v_and_b32 %0, 0xffff0000, %1" : "=v"(sx[0]) : "v"(sx[1]));
        else if constexpr (Z == 28 || Z == 31) asm volatile("v_lshlrev_b32 %0, 16, %0" : "+v"(sx[1]));
        else if constexpr (Z == 29) asm volatile("v_add_f32 %0, 0x3727c5ac, %0" : "+v"(sx[1]));
        else if constexpr (Z == 30 || Z == 33) asm volatile("v_cvt_pk_bf16_f32 %0, %0, %0" : "+v"(sx[1]));
        else if constexpr (Z == 32) asm volatile("v_sqrt_f32 %0, %0" : "+v"(sx[1]));
        else if constexpr (Z == 34) asm volatile("v_lshlrev_b32 %0, 16, %1" : "=v"(sx[2]) : "v"(sx[1]));
        else if constexpr (Z == 35) asm volatile("v_rcp_f32 %0, %1" : "=v"(sx[1]) : "v"(sx[2]));
        else if constexpr (Z == 36) asm volatile("v_mul_f32 %0, %0, %0" : "+v"(sx[2]));
        else if constexpr (Z == 37) asm volatile("v_mul_f32 %0, 0xbfb8aa3b, %0" : "+v"(sx[1]));
        else if constexpr (Z == 38) *(LDS_AS f32x4*)(size_t)(lds0 + sto + ASZ) = f32x4{sx[0], sx[1], sx[2], sx[3]};
    };
    static_assert((NK % NSA) == 1 && (2 * (NK - 2) + 1 - (2 * STK + 5)) * SPU >= 39 && (2 * (NK - 3) + 1 - (2 * STK + 5)) * SPU <= 38, "statistics slot");
    f32x4 ccv[NI], stv[MI];
    const unsigned cca = opaque(lds0 + RING + (wn * 64 + g * 4) * 4), cc6 = opaque(lds0 + RING + (48 + wn * 8 + (g & 1) * 4 + (g >> 1) * 112) * 4);
    const unsigned sta = opaque(lds0 + ASZ + wm * 8192 + (l15 & 1) * 1024 + (l15 >> 1) * 16);
    const unsigned gof = opaque(((m0 + wm * TM) / 4 + (g & 1) * 4 + q) * (8 * F_) + (tn * 7 + wn * 4) * 128 + p4 * 32 + (g >> 1) * 16);
    const unsigned gofm = opaque(((m0 + wm * TM + wn * 32) / 4 + (lane >> 3)) * (8 * F_) + (tn * 7 + 3) * 128 + (lane & 7) * 16);
    const unsigned hsw = opaque(lds0 + 2 * ASZ + (wm * TM + (g >> 1) * 16 + l15) * 32 + wn * 16 + (g & 1) * 8);
    const unsigned hsr = opaque(lds0 + 2 * ASZ + wid * 1024 + lane * 16);
    auto eld = [&](auto oc) INL {
        constexpr int O = decltype(oc)::value;
        constexpr int SI = O == 0 ? 0 : (O == 3 ? 1 : (O == 7 ? 2 : (O == 8 ? 3 : -1)));
        constexpr int J = O < 3 ? O * 3 - 3 : (O < 6 ? O * 3 - 11 : (O == 6 ? 6 : O * 3 - 25));
        if constexpr (SI >= 0) stv[SI] = *(const LDS_AS f32x4*)(size_t)(sta + (SI >> 1) * 4096 + (SI & 1) * 128);
        else if constexpr (J == 6) ccv[6] = *(const LDS_AS f32x4*)(size_t)(cc6);
        else ccv[J] = *(const LDS_AS f32x4*)(size_t)(cca + ((J / 3) * 112 + (J % 3) * 16) * 4);
    };
    auto unit = [&](auto ktc, auto kkc) INL {
        constexpr int KT = decltype(ktc)::value, KK = decltype(kkc)::value;
        constexpr int NKT = KK ? KT + 1 : KT;
        static_for<0, MI * NI>([&](auto tc) INL {
            constexpr int T = decltype(tc)::value, X = KK * MI * NI + T, j = X / 8, i = j & 1 ? MI - 1 - (X / 2) % MI : (X / 2) % MI;
            mma_ip(cat(bf[KT & 1][4 * j + 2 * (X & 1)], bf[KT & 1][4 * j + 2 * (X & 1) + 1]), af[KT & 1][2 * i + (X & 1)], acc[i][j]);
            SCHED();
            constexpr bool RQ = KT + 2 < NK && T < 4 * GGS;
            if constexpr (RQ && T % GGS == 0) prep(kkc, CT<T / GGS>{}, CT<KT + 2>{});
            if constexpr (RQ && T % GGS == 1) go(kkc, CT<T / GGS>{});
            constexpr int O = T - (T + GGS - 2) / GGS;
            if constexpr (NKT < NK && T % GGS != 1 && O < (KK ? 12 : 36 - GL2)) ldop(CT<NKT>{}, CT<(KK ? O : O + GL2)>{});
            if constexpr (NKT < NK && KK == 1 && T >= 20 && T < 8 + GL2) ldop(CT<NKT>{}, CT<T - 8>{});
            if constexpr (KT == STK && KK == 1 && T >= STT && T < STT + 4) gload16_devo<(T - STT) * 256>(stn[T - STT], spw, sto);
            constexpr int Z = (2 * KT + KK - 2 * STK - 5) * SPU + T - (MI * NI - SPU);
            if constexpr (Z >= 0 && Z < 39 && T >= MI * NI - SPU) stats(CT<Z>{});
            if constexpr (NKT == NK && T < 11) eld(tc);
            if constexpr (KK == 0 && T == PRT && KT < NK - 1) __builtin_amdgcn_s_setprio(0);
            SCHED();
        });
    };

    if (wid == 0) {
        const int c = (lane < BN / 4 ? lane : 0) * 4;
        const unsigned co = (unsigned)((c >= BN / 2 ? F_ + c - BN / 2 : c) + tn * (BN / 2)) * 4u;
        f32x4 cp[8];
#pragma unroll
        for (int i = 0; i < 8; ++i) gload16_dev(cp[i], cs + i * 2 * F_, co);
        wait_vm<0>();
#pragma unroll
        for (int i = 0; i < 8; ++i) {
            asm volatile("" : "+v"(cp[i]));
            if (i) cp[0] += cp[i];
        }
        if (lane < BN / 4) *(f32x4*)(csl + lane * 4) = cp[0];
    }
    if (wid == 1) {
        unsigned have = 0u;
        await(have, ~0u, flags + (tm * 32 + (lane & 31)) * 32, tok);
    }
    BARRIER();
    stage_a(CT<0>{});
    wait_vm<0>();
    BARRIER();
    stage_a(CT<1>{});
    static_for<0, GL2>([&](auto oc) INL { ldop(CT<0>{}, oc); });
    static_for<0, NK>([&](auto kc) INL {
        constexpr int KT = decltype(kc)::value;
        unit(CT<KT>{}, CT<0>{});
        if constexpr (KT < NK - 1) {
            WAIT_LGKM0();
            if constexpr (KT + 2 < NK) wait_vm<(KT == STK + 1 ? 8 : 4)>(); else wait_vm<0>();
            BARRIER();
            if constexpr (PRT < 28) __builtin_amdgcn_s_setprio(2);
            if constexpr (KT == NK - 2) { touch1(SINK, wd, tof); SCHED(); }
        }
        unit(CT<KT>{}, CT<1>{});
    });
    asm volatile("s_nop 15\n\ts_nop 7" ::: "memory");
    SCHED();
    const int tide = (int)opaque(threadIdx.x);
    {
        __builtin_amdgcn_s_setprio(0);
        static_assert((NK - 1) % NSA == 0, "staging area");
        auto center = [&](f32x4 a, f32x4 c, float nm) INL {
            float a0, a1, a2, a3;
            asm("v_fma_f32 %0, %4, %5, %9\n\tv_fma_f32 %1, %4, %6, %10\n\tv_fma_f32 %2, %4, %7, %11\n\tv_fma_f32 %3, %4, %8, %12"
                : "=&v"(a0), "=&v"(a1), "=&v"(a2), "=&v"(a3)
                : "v"(nm), "v"(c[0]), "v"(c[1]), "v"(c[2]), "v"(c[3]), "v"(a[0]), "v"(a[1]), "v"(a[2]), "v"(a[3]));
            return f32x4{a0, a1, a2, a3};
        };
        auto glu = [&](f32x4 gp, f32x4 up, float c, float k) INL {
            float t0, t1, t2, t3, u0 = up[0], u1 = up[1], u2 = up[2], u3 = up[3];
            asm("v_mul_f32 %0, %12, %8\n\tv_mul_f32 %1, %12, %9\n\tv_mul_f32 %2, %12, %10\n\tv_mul_f32 %3, %12, %11\n\t"
                "v_exp_f32 %0, %0\n\tv_exp_f32 %1, %1\n\tv_exp_f32 %2, %2\n\tv_exp_f32 %3, %3\n\t"
                "v_mul_f32 %4, %4, %8\n\tv_mul_f32 %5, %5, %9\n\t"
                "v_fma_f32 %0, %0, %13, %13\n\tv_fma_f32 %1, %1, %13, %13\n\t"
                "v_fma_f32 %2, %2, %13, %13\n\tv_fma_f32 %3, %3, %13, %13\n\t"
                "v_rcp_f32 %0, %0\n\tv_rcp_f32 %1, %1\n\tv_rcp_f32 %2, %2\n\tv_rcp_f32 %3, %3\n\t"
                "v_mul_f32 %6, %6, %10\n\tv_mul_f32 %7, %7, %11\n\t"
                "v_mul_f32 %4, %4, %0\n\tv_mul_f32 %5, %5, %1\n\tv_mul_f32 %6, %6, %2\n\tv_mul_f32 %7, %7, %3"
                : "=&v"(t0), "=&v"(t1), "=&v"(t2), "=&v"(t3), "+v"(u0), "+v"(u1), "+v"(u2), "+v"(u3)
                : "v"(gp[0]), "v"(gp[1]), "v"(gp[2]), "v"(gp[3]), "v"(c), "v"(k));
            return u32x2{pk2(u0, u1), pk2(u2, u3)};
        };
        auto centerp = [&](f32x4 a, f32x4 c, float nm) INL {
            const f32x2 n2 = {nm, nm};
            const f32x2 lo = __builtin_elementwise_fma(n2, c.xy, a.xy), hi = __builtin_elementwise_fma(n2, c.zw, a.zw);
            return f32x4{lo.x, lo.y, hi.x, hi.y};
        };
        auto glu2 = [&](f32x2 gp, f32x2 up, float c, float k) INL {
            const f32x2 ea = gp * f32x2{c, c}, k2 = {k, k};
            const f32x2 d = __builtin_elementwise_fma(f32x2{__builtin_amdgcn_exp2f(ea.x), __builtin_amdgcn_exp2f(ea.y)}, k2, k2);
            const f32x2 hv = (gp * up) * f32x2{__builtin_amdgcn_rcpf(d.x), __builtin_amdgcn_rcpf(d.y)};
            return pk2(hv.x, hv.y);
        };
        auto glup = [&](f32x4 gp, f32x4 up, float c, float k) INL {
            return u32x2{glu2(gp.xy, up.xy, c, k), glu2(gp.zw, up.zw, c, k)};
        };
        u32x2 hp[2];
        auto blk = [&](int i, int j, auto pkc) INL {
            if constexpr (decltype(pkc)::value)
                hp[i & 1] = glup(centerp(acc[i][j], ccv[j], stv[i][0]), centerp(acc[i][j + 3], ccv[j + 3], stv[i][0]), stv[i][1], stv[i][2]);
            else
                hp[i & 1] = glu(center(acc[i][j], ccv[j], stv[i][0]), center(acc[i][j + 3], ccv[j + 3], stv[i][0]), stv[i][1], stv[i][2]);
        };
        auto blk2 = [&](int i, int j) INL {
            f32x2 gp[4], up[4], ea[4];
#pragma unroll
            for (int x = 0; x < 4; ++x) {
                const f32x4 a = acc[i + (x >> 1)][j], c = ccv[j];
                const float nm = stv[i + (x >> 1)][0];
                gp[x] = __builtin_elementwise_fma(f32x2{nm, nm}, x & 1 ? c.zw : c.xy, x & 1 ? a.zw : a.xy);
            }
            SCHED();
#pragma unroll
            for (int x = 0; x < 4; ++x) { const float c = stv[i + (x >> 1)][1]; ea[x] = gp[x] * f32x2{c, c}; }
            SCHED();
#pragma unroll
            for (int x = 0; x < 4; ++x) {
                const f32x4 a = acc[i + (x >> 1)][j + 3], c = ccv[j + 3];
                const float nm = stv[i + (x >> 1)][0];
                ea[x] = f32x2{__builtin_amdgcn_exp2f(ea[x].x), __builtin_amdgcn_exp2f(ea[x].y)};
                up[x] = __builtin_elementwise_fma(f32x2{nm, nm}, x & 1 ? c.zw : c.xy, x & 1 ? a.zw : a.xy);
                SCHED();
            }
#pragma unroll
            for (int x = 0; x < 4; ++x) { const float k = stv[i + (x >> 1)][2]; ea[x] = __builtin_elementwise_fma(ea[x], f32x2{k, k}, f32x2{k, k}); }
            SCHED();
#pragma unroll
            for (int x = 0; x < 4; ++x) {
                ea[x] = f32x2{__builtin_amdgcn_rcpf(ea[x].x), __builtin_amdgcn_rcpf(ea[x].y)};
                up[x] = gp[x] * up[x];
                SCHED();
            }
#pragma unroll
            for (int x = 0; x < 4; ++x) up[x] = up[x] * ea[x];
            SCHED();
#pragma unroll
            for (int x = 0; x < 4; ++x) hp[x & 1][x >> 1] = pk2(up[(x & 1) * 2 + (x >> 1)].x, up[(x & 1) * 2 + (x >> 1)].y);
        };
        auto flush = [&](int hf, int j) INL {
            const auto t0 = __builtin_amdgcn_permlane16_swap(hp[0][0], hp[1][0], false, false);
            const auto t1 = __builtin_amdgcn_permlane16_swap(hp[0][1], hp[1][1], false, false);
            store16_wt(C, gof + hf * 8 * (8 * F_) + j * 128, u32x4{t0[0], t1[0], t0[1], t1[1]});
            SCHED();
        };
        auto head = [&](auto pkc) INL { blk(0, 0, pkc); blk(1, 0, pkc); flush(0, 0); blk(0, 1, pkc); };
        auto sync = [&]() INL {
            WAIT_LGKM0();
            BARRIER();
            asm volatile("" ::: "memory");
            SCHED();
        };
        const bool late = wid >= 4;
        if (!late) head(CT<0>{});
        sync();
#pragma unroll
        for (int i = 0; i < MI; ++i)
#pragma unroll
            for (int j = 0; j < NI; ++j) asm volatile("" : "+v"(acc[i][j]));
#pragma unroll
        for (int i = 0; i < MI; i += 2) {
            const f32x4 x = centerp(acc[i][6], ccv[6], stv[i][0]), y = centerp(acc[i + 1][6], ccv[6], stv[i + 1][0]);
            f32x4 gm, um;
#pragma unroll
            for (int r = 0; r < 4; ++r) {
                const auto t = __builtin_amdgcn_permlane32_swap(__float_as_uint(x[r]), __float_as_uint(y[r]), false, false);
                gm[r] = __uint_as_float(t[0]); um[r] = __uint_as_float(t[1]);
            }
            const bool hi = (tide & 32) != 0;
            *(LDS_AS u32x2*)(size_t)(hsw + i * 512) =
                glup(gm, um, hi ? stv[i + 1][1] : stv[i][1], hi ? stv[i + 1][2] : stv[i][2]);
        }
        u32x4 mv;
        auto mixed = [&]() INL {
            sync();
            mv = *(const LDS_AS u32x4*)(size_t)(hsr);
            SCHED();
        };
        if constexpr (MXP == 0) { mixed(); store16_wt(C, gofm, mv); }
        if (late) {
            __builtin_amdgcn_s_setprio(LPR);
            blk2(0, 0);
            flush(0, 0);
            blk2(0, 1);
        } else {
            blk(1, 1, CT<1>{});
        }
        flush(0, 1);
        if constexpr (MXP == 1) mixed();
        blk2(0, 2);
        if constexpr (MXP == 1) store16_wt(C, gofm, mv);
        flush(0, 2);
#pragma unroll
        for (int j = 0; j < 3; ++j) {
            blk2(2, j);
            flush(1, j);
        }
        __builtin_amdgcn_s_setprio(0);
        publish(flags + (512 + tm * 32 + tn) * 32, tok);
        dn_pre(lds0, wd, wid, tide & 63);
    }
}

constexpr long long W_OB = 12, W_H = 16, W_XA = 32, W_XB = 36, W_CS = 40, W_SPA = 42, W_SPB = 43, W_FL = 44, W_EP = 45, W_SL = 48, W_L1 = 48;
extern "C" __global__ __launch_bounds__(512, 2) void body_k(const u16* __restrict__ x0, const u16* __restrict__ wqkv,
                                                 const u16* __restrict__ wo, const u16* __restrict__ wg,
                                                 const u16* __restrict__ wu, const u16* __restrict__ wd, u16* out,
                                                 char* ws) {
    extern __shared__ __attribute__((aligned(16))) char smem[];
    {
        const int tid = threadIdx.x, lane = tid & 63, wid = __builtin_amdgcn_readfirstlane(tid >> 6);
        if (wid >= 2 && wid < 6) {
            const int id = (blockIdx.x & 7) * 32 + (blockIdx.x >> 3);
            const int tm = (id / 128) * 8 + id % 8, tn = (id % 128) / 8, h = wid - 2;
            const int row = h * 8 + (lane >> 3), hr = h * 4 + ((lane >> 3) & 3);
            const unsigned la = (unsigned)((tm * 128 + row) * D_ + ((lane & 7) ^ ((row >> 1) & 7)) * 8) * 2u;
            const unsigned lh = (unsigned)((tm ? tm * 128 - 16 + hr : 0) * D_ + ((lane & 7) ^ ((hr >> 1) & 7)) * 8) * 2u;
            const unsigned lb = b_off(h, lane, 3 * D_, tn * 64);
#pragma unroll 1
            for (int kt = 0; kt < 2; ++kt) {
#pragma unroll
                for (int i = 0; i < 4; ++i) glds16(uni((const char*)x0 + kt * 128 + i * 32 * D_ * 2), la, kt * QAS + h * 1024 + i * 4096);
                glds16_mask(uni((const char*)x0 + kt * 128), lh, kt * QAS + 16384 + h * 512, h == 0 ? 0xffffff00ull : 0xffffffffull);
                qkv_stage_b(0, wqkv, lb, h, kt, kt);
            }
        } else if (wid == 1) {
            code_touch(SINK, lane);
        }
    }
    u64 tok = 0;
#pragma unroll 1
    for (int L = 0; L < 2; ++L) {
        auto at = [&](long long mb, int l) INL { return sopq(ws) + ((mb + l * W_L1) << 20); };
        auto fl = [&](int ph) INL { return (unsigned*)at(W_FL, 0) + 16384 + ph * 8192; };
        qkv_phase(smem, L ? (u16*)at(W_XB, 0) : x0, sopq(wqkv), (u16*)at(W_OB, L), (float*)at(W_CS, 0), (float*)at(W_SPB, 0), fl(4),
                  fl(7 - L), sopq(wo), L == 0, sopq(wg), sopq(wu), (u64*)at(W_EP, 0), tok);
        wo_phase(smem, (u16*)at(W_OB, L), sopq(wo), (u16*)at(W_XA, L), L ? (u16*)at(W_XB, 0) : x0, (float*)at(W_SPA, L), fl(7 - L),
                 fl(L), L == 0, sopq(wg), sopq(wu), tok);
        gu_phase(smem, (u16*)at(W_XA, L), sopq(wg), sopq(wu), (u16*)at(W_H, L), (float*)at(W_CS, 0), (float*)at(W_SPA, L), sopq(wd),
                 L == 0, fl(L), tok);
        dn_phase(smem, (u16*)at(W_H, L), sopq(wd), L ? out : (u16*)at(W_XB, 0), (u16*)at(W_XA, L), L ? nullptr : (float*)at(W_SPB, 0),
                 (float*)at(W_SL, L), (unsigned*)at(W_FL, 0) + L * 4096, fl(2 + L), fl(4 + L), L ? nullptr : sopq(wqkv), tok);
    }
    asm volatile(".Lbody_end:");
}

extern "C" void kernel_launch(void* const* d_in, const int* in_sizes, int n_in, void* d_out, int out_size,
                              void* d_ws, size_t ws_size, hipStream_t stream) {
    const u16* x0 = (const u16*)d_in[0];
    const u16* wqkv = (const u16*)d_in[1];
    const u16* wo = (const u16*)d_in[2];
    const u16* wg = (const u16*)d_in[4];
    const u16* wu = (const u16*)d_in[5];
    const u16* wd = (const u16*)d_in[6];
    constexpr int smem = 163840;
    (void)hipFuncSetAttribute((const void*)body_k, hipFuncAttributeMaxDynamicSharedMemorySize, smem);
    body_k<<<256, 512, smem, stream>>>(x0, wqkv, wo, wg, wu, wd, (u16*)d_out, (char*)d_ws);
}
```

```cpp
#include <hip/hip_runtime.h>
#include <hip/hip_bf16.h>
#include <stdint.h>

#pragma clang diagnostic ignored "-Winline-asm"
typedef unsigned short u16;
using bf16x8 = __attribute__((ext_vector_type(8))) short;
using s16x4  = __attribute__((ext_vector_type(4))) short;
using f32x4  = __attribute__((ext_vector_type(4))) float;
using f32x2  = __attribute__((ext_vector_type(2))) float;
using u32x2  = __attribute__((ext_vector_type(2))) unsigned;
using u32x4  = __attribute__((ext_vector_type(4))) unsigned;
typedef __bf16 bf2_t __attribute__((ext_vector_type(2)));
#define LDS_AS __attribute__((address_space(3)))
#define INL __attribute__((always_inline))

constexpr int TCU = -1, TCT = 25, STT = 22, STK = 10, MXP = 1, LPR = 1, SPU = 10, PRT = 20, WOLD = 12, WOBAR = 4,
              WVN = 2, GL2 = 16, QL1 = 16, QG1 = 10, GGS = 6, WV0 = 8, PV0 = 6, HPRIO = 2, SLP = 3, KIN = 9, KSKIP = 2,
              NSOLO = 4, QPS = 0, XO = 4, XA = 28, XB = 8, DBAR = 4, DBAR2 = 4;
constexpr unsigned long long DSCH = 0x87654321a9000000ull;
constexpr int S_ = 2048, D_ = 1024, F_ = 3584;

__device__ __forceinline__ float bf2f(u16 v) { return __uint_as_float(((unsigned)v) << 16); }
__device__ __forceinline__ u16 f2bf(float f) {
    __hip_bfloat16 h = __float2bfloat16(f);
    return *reinterpret_cast<u16*>(&h);
}
__device__ __forceinline__ float rbf(float f) { return bf2f(f2bf(f)); }
__device__ __forceinline__ unsigned pk2(float a, float b) {
    const f32x2 v = {a, b};
    return __builtin_bit_cast(unsigned, __builtin_convertvector(v, bf2_t));
}
__device__ __forceinline__ float addf(float a, float b) {
    float r;
    asm("v_add_f32 %0, %1, %2" : "=v"(r) : "v"(a), "v"(b));
    return r;
}
__device__ __forceinline__ float mulf(float a, float b) {
    float r;
    asm("v_mul_f32 %0, %1, %2" : "=v"(r) : "v"(a), "v"(b));
    return r;
}
__device__ __forceinline__ float addf_t(float a, float b) {
    float r;
    asm("s_nop 0\n\tv_add_f32 %0, %1, %2" : "=v"(r) : "v"(a), "v"(b));
    return r;
}
__device__ __forceinline__ float rcp_nop(float a) {
    float r;
    asm("v_rcp_f32 %0, %1\n\ts_nop 0" : "=v"(r) : "v"(a));
    return r;
}
__device__ __forceinline__ float subf(float a, float b) {
    float r;
    asm("v_sub_f32 %0, %1, %2" : "=v"(r) : "v"(a), "v"(b));
    return r;
}
__device__ __forceinline__ float max2(float a, float b) { return __builtin_amdgcn_fmed3f(a, b, __builtin_inff()); }

template <int V> struct CT { static constexpr int value = V; };
template <int I, int N, class F> __device__ __forceinline__ void static_for(F&& f) {
    if constexpr (I < N) { f(CT<I>{}); static_for<I + 1, N>(f); }
}

#define WAIT_LGKM0() asm volatile("s_waitcnt lgkmcnt(0)" ::: "memory")
#define BARRIER() do { asm volatile("" ::: "memory"); __builtin_amdgcn_s_barrier(); asm volatile("" ::: "memory"); } while (0)
#define SCHED() __builtin_amdgcn_sched_barrier(0)
template <int N> __device__ __forceinline__ void wait_vm() {
    static_assert(N >= 0 && N < 64, "vmcnt range");
    asm volatile("s_waitcnt vmcnt(%0)" :: "n"(N) : "memory");
}

__device__ __forceinline__ void glds16(const void* sbase, unsigned voff, unsigned lds_dst) {
    asm volatile("s_mov_b32 m0, %2\n\ts_nop 3\n\tglobal_load_lds_dwordx4 %0, %1" :: "v"(voff), "s"(sbase), "s"(__builtin_amdgcn_readfirstlane(lds_dst)) : "memory", "m0");
}
template <int NLANE>
__device__ __forceinline__ void glds16_part(const void* sbase, unsigned voff, unsigned lds_dst) {
    unsigned long long ex;
    asm volatile("s_mov_b64 %0, exec\n\ts_mov_b32 m0, %3\n\ts_mov_b64 exec, %4\n\ts_nop 1\n\t"
                 "global_load_lds_dwordx4 %1, %2 sc1\n\ts_mov_b64 exec, %0"
                 : "=&s"(ex) : "v"(voff), "s"(sbase), "s"(__builtin_amdgcn_readfirstlane(lds_dst)), "n"((1ull << NLANE) - 1) : "memory", "m0");
}
__device__ __forceinline__ void glds16_mask(const void* sbase, unsigned voff, unsigned lds_dst, unsigned long long mask) {
    unsigned long long ex;
    asm volatile("s_mov_b64 %0, exec\n\ts_mov_b32 m0, %3\n\ts_mov_b64 exec, %4\n\ts_nop 1\n\t"
                 "global_load_lds_dwordx4 %1, %2\n\ts_mov_b64 exec, %0"
                 : "=&s"(ex) : "v"(voff), "s"(sbase), "s"(__builtin_amdgcn_readfirstlane(lds_dst)), "s"(mask) : "memory", "m0");
}
__device__ __forceinline__ void glds16_dev(const void* sbase, unsigned voff, unsigned lds_dst) {
    asm volatile("s_mov_b32 m0, %2\n\ts_nop 3\n\tglobal_load_lds_dwordx4 %0, %1 sc1" :: "v"(voff), "s"(sbase), "s"(__builtin_amdgcn_readfirstlane(lds_dst)) : "memory", "m0");
}
template <int V> __device__ __forceinline__ void s_set(unsigned& d) { asm volatile("s_mov_b32 %0, %1" : "=s"(d) : "n"(V)); }
template <int V> __device__ __forceinline__ void m0_set(unsigned base) { asm volatile("s_add_i32 m0, %0, %1" :: "s"(base), "n"(V) : "m0", "scc"); }
template <bool DEV> __device__ __forceinline__ void blds16(__amdgpu_buffer_rsrc_t r, unsigned voff, unsigned soff) {
    if constexpr (DEV) asm volatile("buffer_load_dwordx4 %0, %1, %2 offen sc1 lds" :: "v"(voff), "s"(r), "s"(soff) : "memory");
    else asm volatile("buffer_load_dwordx4 %0, %1, %2 offen lds" :: "v"(voff), "s"(r), "s"(soff) : "memory");
}
__device__ __forceinline__ void gload16_dev(f32x4& d, const void* sbase, unsigned voff) {
    asm volatile("s_nop 4\n\tglobal_load_dwordx4 %0, %1, %2 sc1" : "=v"(d) : "v"(voff), "s"(sbase) : "memory");
}
__device__ __forceinline__ void gload8_dev(unsigned long long& d, const void* p) {
    asm volatile("global_load_dwordx2 %0, %1, off sc1" : "=v"(d) : "v"(p) : "memory");
}
template <int OFF, class T> __device__ __forceinline__ void gload16_devo(T& d, const void* sbase, unsigned voff) {
    asm volatile("global_load_dwordx4 %0, %1, %2 offset:%3 sc1" : "=v"(d) : "v"(voff), "s"(sbase), "n"(OFF) : "memory");
}
template <int OFF> __device__ __forceinline__ void gload16(f32x4& d, const void* sbase, unsigned voff) {
    asm volatile("s_nop 4\n\tglobal_load_dwordx4 %0, %1, %2 offset:%3" : "=v"(d) : "v"(voff), "s"(sbase), "n"(OFF) : "memory");
}
constexpr unsigned SINK = 163584;
__device__ __forceinline__ void touch1(unsigned sink, const void* sbase, unsigned voff) {
    asm volatile("s_mov_b32 m0, %2\n\ts_nop 3\n\tglobal_load_lds_dword %0, %1" :: "v"(voff), "s"(sbase), "s"(sink) : "memory", "m0");
}
__device__ __forceinline__ const char* uni(const char* p) {
    const unsigned long long v = (unsigned long long)p;
    const unsigned lo = __builtin_amdgcn_readfirstlane((unsigned)v), hi = __builtin_amdgcn_readfirstlane((unsigned)(v >> 32));
    return (const char*)(((unsigned long long)hi << 32) | lo);
}
__device__ __forceinline__ void store16_wt(void* base, unsigned off, u32x4 v) {
    const __amdgpu_buffer_rsrc_t r = __builtin_amdgcn_make_buffer_rsrc(base, 0, 0x7fffffff, 0x00020000);
    __builtin_amdgcn_raw_buffer_store_b128(v, r, off, 0, 16);
}
__device__ __forceinline__ unsigned opaque(unsigned v) { asm volatile("" : "+v"(v)); return v; }
__device__ __forceinline__ unsigned bid_() { unsigned b = blockIdx.x; asm volatile("" : "+s"(b)); return b; }
__device__ __forceinline__ int wg_id() { const unsigned b = bid_(); return (b & 7) * 32 + (b >> 3); }
template <class T> __device__ __forceinline__ T* sopq(T* p) { asm volatile("" : "+s"(p)); return p; }
__device__ __forceinline__ void code_touch(unsigned sink, unsigned lane) {
    const char* pc;
    int off;
    unsigned len;
    asm volatile("s_getpc_b64 %0\n.Lct%=:\n\ts_mov_b32 %1, body_k-.Lct%=\n\ts_mov_b32 %2, .Lbody_end-body_k"
                 : "=s"(pc), "=s"(off), "=s"(len));
    const unsigned o = ((bid_() >> 3) + lane * 32u) * 128u;
    touch1(sink, pc + off, o < len ? o : 0u);
}
template <int OFF> __device__ __forceinline__ bf16x8 lds_a(unsigned base) {
    static_assert(OFF >= 0 && OFF < 65536, "ds offset");
    return *(const LDS_AS bf16x8*)(size_t)(base + OFF);
}
template <int OFF0, int OFF1> __device__ __forceinline__ bf16x8 lds_b(unsigned base0, unsigned base1) {
    static_assert(OFF0 >= 0 && OFF0 < 65536 && OFF1 >= 0 && OFF1 < 65536, "ds offset");
    s16x4 lo = __builtin_amdgcn_ds_read_tr16_b64_v4i16((LDS_AS s16x4*)(size_t)(base0 + OFF0));
    s16x4 hi = __builtin_amdgcn_ds_read_tr16_b64_v4i16((LDS_AS s16x4*)(size_t)(base1 + OFF1));
    return __builtin_shufflevector(lo, hi, 0, 1, 2, 3, 4, 5, 6, 7);
}
template <int OFF> __device__ __forceinline__ s16x4 lds_h(unsigned base) {
    static_assert(OFF >= 0 && OFF < 65536, "ds offset");
    return __builtin_amdgcn_ds_read_tr16_b64_v4i16((LDS_AS s16x4*)(size_t)(base + OFF));
}
__device__ __forceinline__ bf16x8 cat(s16x4 lo, s16x4 hi) { return __builtin_shufflevector(lo, hi, 0, 1, 2, 3, 4, 5, 6, 7); }
__device__ __forceinline__ f32x4 mma(bf16x8 a, bf16x8 b, f32x4 c) {
    return __builtin_amdgcn_mfma_f32_16x16x32_bf16(a, b, c, 0, 0, 0);
}

__device__ __forceinline__ void mma_ip(bf16x8 a, bf16x8 b, f32x4& c) {
    asm volatile("v_mfma_f32_16x16x32_bf16 %0, %1, %2, %0" : "+v"(c) : "v"(a), "v"(b));
}

constexpr int CSTR = 64 * 2 + 16;
struct ResC { unsigned rd, co, so; };
__device__ __forceinline__ void res_setup(ResC& c, unsigned lds, int m0, int n0, int tid) {
    const int row = (tid >> 6) * 16 + ((tid >> 3) & 7), ch = tid & 7;
    c.rd = opaque(lds + row * CSTR + ch * 16);
    c.co = opaque((unsigned)((m0 + row) * D_ + n0 + ch * 8) * 2u);
    c.so = opaque((unsigned)(((m0 + row) >> 5) * 512 + (n0 >> 6) * 32 + (row & 31) + (ch >> 2) * 8) * 8u);
}
__device__ __forceinline__ void res_tail(const ResC& c, u16* C, float* sp) {
    const u32x4 v = *(const LDS_AS u32x4*)(size_t)c.rd, w = *(const LDS_AS u32x4*)(size_t)(c.rd + 8 * CSTR);
    const __amdgpu_buffer_rsrc_t rc = __builtin_amdgcn_make_buffer_rsrc(C, 0, 0x7fffffff, 0x00020000);
    __builtin_amdgcn_raw_buffer_store_b128(v, rc, c.co, 0, 16);
    __builtin_amdgcn_raw_buffer_store_b128(w, rc, c.co, 8 * D_ * 2, 16);
    SCHED();
    if (sp == nullptr) return;
    unsigned s0, q0, s1, q1;
    asm("v_dot2_f32_bf16 %0, %4, %12, 0\n\tv_dot2_f32_bf16 %1, %4, %4, 0\n\tv_dot2_f32_bf16 %0, %5, %12, %0\n\tv_dot2_f32_bf16 %1, %5, %5, %1\n\t"
        "v_dot2_f32_bf16 %0, %6, %12, %0\n\tv_dot2_f32_bf16 %1, %6, %6, %1\n\tv_dot2_f32_bf16 %0, %7, %12, %0\n\tv_dot2_f32_bf16 %1, %7, %7, %1\n\t"
        "v_dot2_f32_bf16 %2, %8, %12, 0\n\tv_dot2_f32_bf16 %3, %8, %8, 0\n\tv_dot2_f32_bf16 %2, %9, %12, %2\n\tv_dot2_f32_bf16 %3, %9, %9, %3\n\t"
        "v_dot2_f32_bf16 %2, %10, %12, %2\n\tv_dot2_f32_bf16 %3, %10, %10, %3\n\tv_dot2_f32_bf16 %2, %11, %12, %2\n\tv_dot2_f32_bf16 %3, %11, %11, %3\n\t"
        "v_add_f32_dpp %0, %0, %0 row_half_mirror row_mask:0xf bank_mask:0x5\n\tv_add_f32_dpp %1, %1, %1 row_half_mirror row_mask:0xf bank_mask:0x5\n\t"
        "v_add_f32_dpp %0, %2, %2 row_half_mirror row_mask:0xf bank_mask:0xa\n\tv_add_f32_dpp %1, %3, %3 row_half_mirror row_mask:0xf bank_mask:0xa\n\t"
        "s_nop 0\n\tv_add_f32_dpp %0, %0, %0 quad_perm:[1,0,3,2] row_mask:0xf bank_mask:0xf\n\t"
        "v_add_f32_dpp %1, %1, %1 quad_perm:[1,0,3,2] row_mask:0xf bank_mask:0xf\n\t"
        "s_nop 0\n\tv_add_f32_dpp %0, %0, %0 quad_perm:[2,3,0,1] row_mask:0xf bank_mask:0xf\n\t"
        "v_add_f32_dpp %1, %1, %1 quad_perm:[2,3,0,1] row_mask:0xf bank_mask:0xf"
        : "=&v"(s0), "=&v"(q0), "=&v"(s1), "=&v"(q1)
        : "v"(v[0]), "v"(v[1]), "v"(v[2]), "v"(v[3]), "v"(w[0]), "v"(w[1]), "v"(w[2]), "v"(w[3]), "s"(0x3f803f80u));
    const __amdgpu_buffer_rsrc_t r = __builtin_amdgcn_make_buffer_rsrc(sp, 0, 0x7fffffff, 0x00020000);
    __builtin_amdgcn_raw_buffer_store_b64(u32x2{s0, q0}, r, c.so, 0, 16);
}
__device__ __forceinline__ bf16x8 pick(bool on, int e) {
    const unsigned one = on ? 0x3f80u << (e & 1) * 16 : 0u;
    bf16x8 m = __builtin_bit_cast(bf16x8, u32x4{(e >> 1) == 0 ? one : 0u, (e >> 1) == 1 ? one : 0u, (e >> 1) == 2 ? one : 0u, (e >> 1) == 3 ? one : 0u});
    asm volatile("" : "+v"(m));
    return m;
}

constexpr int ACS = 192 * 2 + 16;
struct AttC {
    unsigned qa, kxa, kya, vxa, vya, oo, mx01, mx23;
    f32x4 cx, cy;
};
__device__ __forceinline__ void att_setup(AttC& c, unsigned lds0, int wid, int lane, int m0, int tn) {
    const int l15 = lane & 15, g = lane >> 4, q = l15 >> 2, p4 = l15 & 3;
    const int par = wid & 1, R0 = (wid >> 1) * 32;
    const unsigned cb = lds0 + par * ACS;
    c.qa = opaque(cb + (R0 + 2 * l15 + 16) * ACS + g * 16);
    c.kxa = opaque(cb + 128 + (R0 + 2 * l15) * ACS + g * 16);
    c.kya = opaque(cb + 128 + (R0 + 2 * (l15 & 7) + 32) * ACS + g * 16);
    c.vxa = opaque(cb + 256 + (R0 + 2 * (g * 4 + q)) * ACS + p4 * 16);
    c.vya = opaque(cb + 256 + (R0 + 2 * ((g & 1) * 4 + q) + 32) * ACS + p4 * 16);
    c.oo = opaque((unsigned)((m0 + R0 + 2 * (l15 & 7) + par) * D_ + tn * 64 + (l15 >> 3) * 32 + g * 8) * 2u);
    const int kmin = m0 + R0 == 0 ? 8 : 0;
    unsigned mx01 = 0u, mx23 = 0u;
#pragma unroll
    for (int r = 0; r < 4; ++r) {
        const int d = l15 - g * 4 - r;
        const bool vx = (d < 0) & (d >= -8) & (g * 4 + r >= kmin), vy = d >= 8;
        c.cx[r] = __uint_as_float(opaque(vx ? 0u : 0xff61b1e6u));
        c.cy[r] = __uint_as_float(opaque(vy ? 0u : 0xff61b1e6u));
        const unsigned mb = d < 0 ? ((r & 1) ? 0xffff0000u : 0xffffu) : 0u;
        if (r < 2) mx01 |= mb; else mx23 |= mb;
    }
    c.mx01 = opaque(mx01);
    c.mx23 = opaque(mx23);
}
constexpr int ATX = 155648;
__device__ __forceinline__ void att_put(const AttC& c, char* smem, int lane) {
    u32x4* const p = (u32x4*)(smem + ATX) + lane;
    p[0] = u32x4{c.qa, c.kxa, c.kya, c.vxa};
    p[64] = u32x4{c.vya, c.oo, c.mx01, c.mx23};
    p[128] = __builtin_bit_cast(u32x4, c.cx);
    p[192] = __builtin_bit_cast(u32x4, c.cy);
}
__device__ __forceinline__ void att_get(AttC& c, const char* smem, int wid, int lane, int m0) {
    const u32x4* const p = (const u32x4*)(smem + ATX) + lane;
    const u32x4 a = p[0], b = p[64];
    const int dr = (wid >> 1) * 32 + (wid & 1) - 64;
    c.qa = a[0] + dr * ACS; c.kxa = a[1] + dr * ACS; c.kya = a[2] + dr * ACS; c.vxa = a[3] + dr * ACS; c.vya = b[0] + dr * ACS;
    c.oo = b[1] + dr * D_ * 2; c.mx01 = b[2]; c.mx23 = b[3];
    c.cx = __builtin_bit_cast(f32x4, p[128]);
    c.cy = __builtin_bit_cast(f32x4, p[192]);
    const bool front = lane < (m0 == 0 && wid < 2 ? 32 : 0);
#pragma unroll
    for (int r = 0; r < 4; ++r) c.cx[r] = front ? -3e38f : c.cx[r];
}
__device__ __forceinline__ void att_run(const AttC& c, u16* C) {
    const f32x4 z4 = f32x4{0.f, 0.f, 0.f, 0.f};
    u32x4 ov[2];
    static_for<0, 2>([&](auto pc) INL {
        constexpr int PO = decltype(pc)::value * 64;
        const bf16x8 qf = lds_a<PO>(c.qa), kx = lds_a<PO>(c.kxa), ky = lds_a<PO>(c.kya);
        const bf16x8 v0 = lds_b<PO, PO>(c.vxa, c.vya), v1 = lds_b<PO + 8, PO + 8>(c.vxa, c.vya);
        const f32x4 sx = mma(kx, qf, c.cx), sy = mma(ky, qf, c.cy);
        float lg[4];
#pragma unroll
        for (int r = 0; r < 4; ++r) lg[r] = max2(sx[r], sy[r]);
        float mx = max2(max2(lg[0], lg[1]), max2(lg[2], lg[3]));
        {
            auto t = __builtin_amdgcn_permlane16_swap(__float_as_uint(mx), __float_as_uint(mx), false, false);
            mx = max2(__uint_as_float(t[0]), __uint_as_float(t[1]));
            t = __builtin_amdgcn_permlane32_swap(__float_as_uint(mx), __float_as_uint(mx), false, false);
            mx = max2(__uint_as_float(t[0]), __uint_as_float(t[1]));
        }
        float pr[4];
#pragma unroll
        for (int r = 0; r < 4; ++r) pr[r] = __builtin_amdgcn_exp2f(mulf(subf(lg[r], mx), 0.17677669529663687f * 1.44269504088896341f));
        float sum = addf(addf_t(pr[0], pr[1]), addf_t(pr[2], pr[3]));
        {
            auto t = __builtin_amdgcn_permlane16_swap(__float_as_uint(sum), __float_as_uint(sum), false, false);
            sum = addf(__uint_as_float(t[0]), __uint_as_float(t[1]));
            t = __builtin_amdgcn_permlane32_swap(__float_as_uint(sum), __float_as_uint(sum), false, false);
            sum = addf(__uint_as_float(t[0]), __uint_as_float(t[1]));
        }
        const float inv = rcp_nop(sum);
        const unsigned p01 = pk2(mulf(pr[0], inv), mulf(pr[1], inv)), p23 = pk2(mulf(pr[2], inv), mulf(pr[3], inv));
        const u32x4 pw = {p01 & c.mx01, p23 & c.mx23, p01 & ~c.mx01, p23 & ~c.mx23};
        const bf16x8 pf = __builtin_bit_cast(bf16x8, pw);
        const f32x4 o0 = mma(v0, pf, z4), o1 = mma(v1, pf, z4);
        ov[decltype(pc)::value] = u32x4{pk2(o0[0], o0[1]), pk2(o0[2], o0[3]), pk2(o1[0], o1[1]), pk2(o1[2], o1[3])};
    });
    u32x4 sa, sb;
#pragma unroll
    for (int e = 0; e < 4; ++e) {
        sa[e] = __builtin_amdgcn_update_dpp(ov[0][e], ov[1][e], 0x128, 0xf, 0xc, false);
        sb[e] = __builtin_amdgcn_update_dpp(ov[1][e], ov[0][e], 0x128, 0xf, 0x3, false);
    }
    store16_wt(C, c.oo, sa);
    store16_wt(C, c.oo + 16 * D_ * 2, sb);
}

typedef unsigned long long u64;
typedef __attribute__((address_space(1))) u64 gu64;
template <int NY = 0>
__device__ __forceinline__ void publish(unsigned* f, u64 tok, unsigned sink = SINK) {
    wait_vm<NY>();
    BARRIER();
    if (opaque(threadIdx.x) == 0) __hip_atomic_store((gu64*)f, tok, __ATOMIC_RELAXED, __HIP_MEMORY_SCOPE_AGENT);
    const unsigned t = opaque(threadIdx.x);
    if (__builtin_amdgcn_readfirstlane(t >> 6) == 2) {
        code_touch(sink, t & 63u);
        wait_vm<0>();
    }
}
__device__ __forceinline__ void await(unsigned& have, unsigned need, const unsigned* f, u64 tok) {
    if (__builtin_expect((have & need) != need, 0)) {
#pragma unroll 1
        for (unsigned spins = 0; spins < (1u << 22); ++spins) {
            have = (unsigned)__ballot(__hip_atomic_load((gu64*)f, __ATOMIC_RELAXED, __HIP_MEMORY_SCOPE_AGENT) == tok);
            if ((have & need) == need) break;
            __builtin_amdgcn_s_sleep(1);
        }
        asm volatile("" ::: "memory");
    }
}
__device__ __forceinline__ unsigned gu_bsrc(int o, int tn) {
    const int rem = (o >> 4) % (64 * 14);
    const int rowp = rem / 14, chn = rem % 14;
    const int kr = rowp ^ (4 * ((rowp >> 3) & 1));
    return (unsigned)(kr * F_ + tn * 112 + chn * 8) * 2u;
}
__device__ __forceinline__ unsigned b_off(int h, int lane, int ld, int col) {
    const int row = h * 8 + (lane >> 3), cp = lane & 7;
    const int jj = (cp >> 1) ^ (((row >> 1) & 1) | ((row >> 2) & 2));
    return (unsigned)(row * ld + col + jj * 16 + (cp & 1) * 8) * 2u;
}

constexpr int WOB = 65536, WONB = 12;
constexpr unsigned BOXO = 36u << 20;
__device__ __forceinline__ int wo_k(int i) { return (int)(0xb3fedca987654210ull >> (4 * i)) & 15; }
__device__ __forceinline__ unsigned wo_boff(int h, int lane) {
    return b_off(h, lane, D_, (((wg_id()) % 64) / 4) * 64);
}
__device__ __forceinline__ void wo_stage_b(unsigned lds0, const u16* B, unsigned boff, int h, int step) {
#pragma unroll
    for (int i = 0; i < 2; ++i)
        glds16(uni((const char*)B + (wo_k(step) * 64 + i * 32) * D_ * 2), boff,
               lds0 + WOB + (step < WONB ? step : step - WONB) * 8192 + h * 1024 + i * 4096);
}
__device__ __forceinline__ void wo_phase(char* smem, const u16* A, const u16* __restrict__ B, u16* C, const bf16x8 (&rr)[4], float* sp,
                                         const unsigned* pflags, unsigned* oflags, bool first, const u16* __restrict__ wg,
                                         const u16* __restrict__ wu, u64 tok) {
    constexpr int NK = 16, ASZ = 16384;
    const int tid = (int)opaque(threadIdx.x), lane = tid & 63;
    const int wid = __builtin_amdgcn_readfirstlane(tid >> 6);
    const int id = wg_id();
    const int tm = (id / 64) * 4 + id % 4, tn = (id % 64) / 4;
    const int m0 = tm * 128, n0 = tn * 64;
    const unsigned lds0 = __builtin_amdgcn_readfirstlane((unsigned)(size_t)(LDS_AS char*)smem);
    ResC rc;
    unsigned goff[7] = {0u, 0u, 0u, 0u, 0u, 0u, 0u};
    auto stage_g = [&](int i, int kt) INL {
        const int p = wid - 4 + 4 * i;
        glds16(uni((const char*)(p >= 14 ? wu : wg) + kt * 64 * F_ * 2), goff[i], lds0 + 98304 + kt * 28672 + p * 1024);
    };
    if (wid >= 4) {
        const int h = wid - 4;
        const int row = h * 8 + (lane >> 3);
        const unsigned aoff = (unsigned)((m0 + row) * D_ + ((lane & 7) ^ ((row >> 1) & 7)) * 8) * 2u;
        const unsigned boff = wo_boff(h, lane);
        const unsigned* const f = pflags + (tm * 16 + (lane & 15)) * 32;
        unsigned have = 0u;

        auto stage = [&](int st, auto bc) INL {
            await(have, 1u << wo_k(st), f, tok);
#pragma unroll
            for (int i = 0; i < 4; ++i)
                glds16_dev(uni((const char*)A + wo_k(st) * 128 + i * 32 * D_ * 2), aoff, lds0 + (st & 3) * ASZ + h * 1024 + i * 4096);
            if constexpr (decltype(bc)::value) wo_stage_b(lds0, B, boff, h, st);
        };
        stage(0, CT<0>{});
        stage(1, CT<0>{});
#pragma unroll
        for (int i = 0; i < 7; ++i) goff[i] = opaque(gu_bsrc((h + 4 * i) * 1024 + (int)opaque(lane) * 16, id >> 3));
        wait_vm<4>();
        BARRIER();
        stage(2, CT<0>{});
        stage(3, CT<0>{});
#pragma unroll 1
        for (int st = 4; st < WONB; ++st) {
            wait_vm<8>();
            BARRIER();
            stage(st, CT<0>{});
        }
#pragma unroll 1
        for (int st = WONB; st < NK; ++st) {
            wait_vm<8>();
            BARRIER();
            stage(st, CT<1>{});
        }
        wait_vm<12>();
        BARRIER();
        stage_g(0, 0); stage_g(1, 0); stage_g(2, 0);
        wait_vm<9>();
        BARRIER();
        stage_g(3, 0); stage_g(4, 0);
        wait_vm<5>();
        BARRIER();
        stage_g(5, 0); stage_g(6, 0);
        res_setup(rc, lds0, m0, n0, tid);

    } else {
        const int wm = wid >> 1, wn = wid & 1;
        const int l15 = lane & 15, g = lane >> 4, q = l15 >> 2, p4 = l15 & 3;
        unsigned ab[2], bb[2];
#pragma unroll
        for (int x = 0; x < 2; ++x) {
            ab[x] = opaque(lds0 + (wm * 64 + l15) * 128 + (((x * 4 + g) ^ (l15 >> 1)) * 16));
            const unsigned b = lds0 + WOB + (g * 8 + q) * 128 + p4 * 8 + (((wn * 2 + x) ^ ((q >> 1) | ((g & 1) << 1))) << 5);
            bb[x] = opaque(b);
        }
        f32x4 acc[4][2];
#pragma unroll
        for (int j = 0; j < 2; ++j) {
            const bf16x8 sel = pick(g == j * 2 + (l15 >> 3), l15 & 7);
#pragma unroll
            for (int i = 0; i < 4; ++i) {
                acc[i][j] = mma(sel, rr[i], f32x4{0.f, 0.f, 0.f, 0.f});
                asm volatile("" : "+v"(acc[i][j]));
            }
        }
        res_setup(rc, lds0, m0, n0, tid);
        const unsigned cw = opaque(lds0 + (wm * 64 + l15) * CSTR + (wn * 32 + g * 4) * 2);
        auto ldop = [&](bf16x8 (&af)[8], s16x4 (&bf)[8], auto ktc, auto oc) INL {
            constexpr int KT = decltype(ktc)::value, O = decltype(oc)::value;
            if constexpr (O == 2 || (O >= 5 && O < 12)) {
                constexpr int X = O == 2 ? 0 : O - 4;
                af[(X & 1) * 4 + X / 2] = lds_a<(KT % 4) * ASZ + (X / 2) * 2048>(ab[X & 1]);
            } else {
                constexpr int X = O < 2 ? O : (O < 5 ? O - 1 : O - 8), J = X / 4;
                bf[X] = lds_h<(KT % 4) * 8192 + ((X >> 1) & 1) * 4096 + (X & 1) * 512>(bb[J]);
            }
        };
        auto ktile = [&](bf16x8 (&af)[8], s16x4 (&bf)[8], bf16x8 (&an)[8], s16x4 (&bn)[8], auto nktc) INL {
            static_for<0, 16>([&](auto tc) INL {
                constexpr int T = decltype(tc)::value, j = T / 8, i = j ? 3 - (T / 2) % 4 : (T / 2) % 4, kk = T & 1;
                if constexpr (T == WOBAR && decltype(nktc)::value < NK) {
                    WAIT_LGKM0();
                    BARRIER();
                }
                mma_ip(cat(bf[j * 4 + kk * 2], bf[j * 4 + kk * 2 + 1]), af[kk * 4 + i], acc[i][j]);
                SCHED();
                if constexpr (decltype(nktc)::value < NK && T >= WOBAR) {
                    constexpr int U = T - WOBAR, lo_ = (U * 16 + WOLD - 1) / WOLD, hi_ = ((U + 1) * 16 + WOLD - 1) / WOLD;
                    static_for<lo_, (hi_ < 16 ? hi_ : 16)>([&](auto oc) INL { ldop(an, bn, nktc, oc); });
                }
                SCHED();
            });
        };
        if (first && wid == 0) {
            const int l = lane < 12 ? lane : 0, cb = (id >> 3) * 7 / 4 + l % 3;
            const int job = ((cb < 56 ? cb : 55) + (l >= 6 ? 56 : 0)) * 2 + (l / 3 & 1), oid = job + 8 * ((job + 32) / 56);
            unsigned have = 0u;
            await(have, ~0u, pflags + (((oid / 128) * 8 + oid % 8) * 16 + (oid % 128) / 8) * 32, tok);
        }
        bf16x8 a0[8], a1[8];
        s16x4 b0[8], b1[8];
        BARRIER();
        static_for<0, 16>([&](auto oc) INL { ldop(a0, b0, CT<0>{}, oc); });
#pragma unroll 1
        for (int it = 0; it < 3; ++it) {
            ktile(a0, b0, a1, b1, CT<1>{});
            ktile(a1, b1, a0, b0, CT<2>{});
            ktile(a0, b0, a1, b1, CT<3>{});
            const unsigned d = it == 2 ? -65536 : 32768;
            bb[0] += d; bb[1] += d;
            ktile(a1, b1, a0, b0, CT<4>{});
        }
        ktile(a0, b0, a1, b1, CT<13>{});
        ktile(a1, b1, a0, b0, CT<14>{});
        ktile(a0, b0, a1, b1, CT<15>{});
        ktile(a1, b1, a0, b0, CT<16>{});
#pragma unroll
        for (int t = 0; t < 8; ++t) {
            const int j = t / 4, i = j ? 7 - t : t;
            *(LDS_AS u32x2*)(size_t)(cw + i * 16 * CSTR + j * 32) = u32x2{pk2(acc[i][j][0], acc[i][j][1]), pk2(acc[i][j][2], acc[i][j][3])};
            SCHED();
        }
    }
    WAIT_LGKM0();
    BARRIER();
    res_tail(rc, C, sp);
    publish(oflags + (tm * 16 + tn) * 32, tok);
    if (wid >= 4) {
#pragma unroll
        for (int i = 0; i < 7; ++i) stage_g(i, 1);
    }
}

constexpr int QAS = 144 * 128, QBS = 192 * 128, QBR = 57344, QST = 159744;
constexpr int qsl(int kt) { return (kt + 2) % 3; }
__device__ __forceinline__ int qkv_k(int i) { return (int)(0xb3a2fedc98765410ull >> (4 * i)) & 15; }
__device__ __forceinline__ unsigned qkv_boff(int h, int lane) {
    return b_off(h, lane, 3 * D_, (((wg_id()) % 128) / 8) * 64);
}
__device__ __forceinline__ void qkv_stage_b(unsigned lds0, const u16* B, unsigned boff, int h, int kt, int sl) {
#pragma unroll
    for (int i = 0; i < 6; ++i)
        glds16(uni((const char*)B + ((kt * 64 + (i & 1) * 32) * 3 * D_ + (i >> 1) * D_) * 2), boff,
               lds0 + QBR + sl * QBS + h * 1024 + i * 4096);
}
__device__ __forceinline__ void qkv_phase(char* smem, const u16* A, const u16* __restrict__ B, u16* C, float* cs,
                                          const float* sp, const unsigned* pflags, unsigned* oflags,
                                          const u16* __restrict__ wo, bool first, const u16* __restrict__ wg,
                                          const u16* __restrict__ wu, u64* epoch, u64& tok, bf16x8 (&rr)[4]) {
    constexpr int NK = 16, RES = 131072;
    const int tid = (int)opaque(threadIdx.x), lane = tid & 63;
    const int wid = __builtin_amdgcn_readfirstlane(tid >> 6);
    const int id = wg_id();
    const int tm = (id / 128) * 8 + id % 8, tn = (id % 128) / 8;
    const int m0 = tm * 128;
    const unsigned lds0 = __builtin_amdgcn_readfirstlane((unsigned)(size_t)(LDS_AS char*)smem);
    float* const stat = (float*)(smem + QST);
    float* const csl = stat + 2 * 144;
    float* const cso = cs + 16 * F_ + id * 192;
    auto token = [&]() INL {
        asm volatile("s_load_dwordx2 %0, %1, 0x0\n\ts_waitcnt lgkmcnt(0)" : "=s"(tok) : "s"(epoch) : "memory");
        tok += 0x9e3779b97f4a7c15ull;
    };
    AttC atc;
    if (wid >= 4) {
        const int h = wid - 4, hl = tid - 256;
        const int row = h * 8 + (lane >> 3), hr = h * 4 + ((lane >> 3) & 3);
        const unsigned la = (unsigned)((m0 + row) * D_ + ((lane & 7) ^ ((row >> 1) & 7)) * 8) * 2u;
        const unsigned lh = (unsigned)((tm ? m0 - 16 + hr : 0) * D_ + ((lane & 7) ^ ((hr >> 1) & 7)) * 8) * 2u;
        const unsigned lb = qkv_boff(h, lane);
        const unsigned wb = wo_boff(h, lane);
        auto wo_pre = [&](int i) INL {
#pragma unroll
            for (int e = i ? 0 : 1; e < 3; ++e) wo_stage_b(lds0, wo, wb, h, i * 3 - 1 + e);
        };
        const int wtile = ((id / 64) * 4 + id % 4) * 16 + (id % 64) / 4;
        auto wo_res = [&]() INL {
#pragma unroll
            for (int i = 0; i < 4; ++i)
                glds16_dev(uni((const char*)A + (((wtile >> 4) - tm) * 128 + i * 32) * D_ * 2 + (wtile & 15) * 128), la, lds0 + RES + h * 1024 + i * 4096);
            wo_pre(2);
        };
        if (first) {
            const unsigned long long hmask = h == 0 ? 0xffffff00ull : 0xffffffffull;
            auto piece = [&](int kt, int p) INL {
                const unsigned da = lds0 + qsl(kt) * QAS;
                if (p < 4) glds16(uni((const char*)A + kt * 128 + p * 32 * D_ * 2), la, da + h * 1024 + p * 4096);
                else if (p == 4) glds16_mask(uni((const char*)A + kt * 128), lh, da + 16384 + h * 512, hmask);
                else glds16(uni((const char*)B + ((kt * 64 + ((p - 5) & 1) * 32) * 3 * D_ + ((p - 5) >> 1) * D_) * 2), lb,
                            lds0 + QBR + qsl(kt) * QBS + h * 1024 + (p - 5) * 4096);
            };
            __builtin_amdgcn_s_setprio(HPRIO);
            constexpr unsigned tw = SINK;
            const unsigned one2 = opaque(0x3f803f80u);
            if (hl < 24) *(u32x4*)(smem + (hl >> 3) * QAS + 128 * 128 + (hl & 7) * 16) = u32x4{one2, one2, one2, one2};
            const bool idle = (tn & 7) == 3;
            const int job = idle ? 0 : id - 8 * ((id + 32) >> 6);
            const int cb = job >> 1, kh = job & 1;
            const char* const wp = uni((const char*)((cb < 56 ? wg : wu) + (long long)kh * 512 * F_ + (cb % 56) * 64));
            const unsigned wpo = (unsigned)((idle ? 0 : hl >> 3) * F_ + (hl & 7) * 8) * 2u;
            const int wst = idle ? 0 : 64 * F_;
            f32x4 wv[NK];
            float sm[8];
#pragma unroll
            for (int e = 0; e < 8; ++e) sm[e] = 0.f;
            auto wadd1 = [&](int r, int e) INL {
                float t0, t1;
                asm volatile("v_lshlrev_b32 %2, 16, %4\n\tv_and_b32 %3, 0xffff0000, %4\n\tv_add_f32 %0, %0, %2\n\tv_add_f32 %1, %1, %3"
                             : "+v"(sm[2 * e]), "+v"(sm[2 * e + 1]), "=&v"(t0), "=&v"(t1) : "v"(wv[r][e]));
            };
            auto wadd = [&](int r) INL {
#pragma unroll
                for (int e = 0; e < 4; ++e) wadd1(r, e);
            };
            const unsigned bx = bid_();
            const int kq = bx >> 4;
            auto sweep = [&]() INL {
                touch1(tw, B, (unsigned)(bx * 192 + (hl < 192 ? hl : 0)) * 128u);
                touch1(tw, A, (unsigned)(((bx & 15) * 128 + (hl & 127)) * 2048 + kq * 128));
            };
            if (kq < KIN && kq >= KSKIP) {
                if (h >= 2) {
                    for (int i = (kq - KSKIP + 1) * SLP; i > 0; --i) __builtin_amdgcn_s_sleep(1);
                    touch1(tw, B, (unsigned)(bx * 192 + (hl & 127)) * 128u);
                    touch1(tw, A, (unsigned)(((bx & 15) * 128 + (hl & 127)) * 2048 + kq * 128));
                    if (h == 2) touch1(tw, B, (unsigned)(bx * 192 + hl) * 128u);
                }
            }
            token();
            static_for<0, NK>([&](auto kc) INL {
                constexpr int KT = decltype(kc)::value;
                constexpr auto ex = [](int k) { return (k >= WV0 && k < WV0 + 16 / WVN ? WVN : 0) + (k == PV0 ? 1 : 0) + (k == NK - 2 ? 4 : 0); };
                wait_vm<(KT + 1 < NK ? 11 : 0) + ex(KT - 2) + ex(KT - 1)>();
                BARRIER();
                static_for<0, 11>([&](auto pc) INL {
                    constexpr int P = decltype(pc)::value;
                    if constexpr (KT + 2 < NK) piece(KT + 2, P);
                    if constexpr (KT - 3 >= WV0 && KT - 3 < WV0 + 16 / WVN && P < 8) {
#pragma unroll
                        for (int u = 0; u < WVN / 2; ++u) wadd1((KT - 3 - WV0) * WVN + u * 2 + P / 4, P % 4);
                    }
                    SCHED();
                });
                if constexpr (KT + KIN < NK) { if (kq == KT + KIN) sweep(); }
                if constexpr (KT >= WV0 && KT < WV0 + 16 / WVN) {
#pragma unroll
                    for (int u = 0; u < WVN; ++u) gload16<0>(wv[(KT - WV0) * WVN + u], uni(wp + ((KT - WV0) * WVN + u) * wst), wpo);
                }
                if constexpr (KT == PV0) touch1(tw, wo, (unsigned)(bx * 64 + (hl & 63)) * 128u);
                if constexpr (KT == NK - 2) wo_pre(0);
                if constexpr (KT == 7) {
                    att_setup(atc, lds0, wid, lane, m0, tn);
                    if (h == 0) att_put(atc, smem, lane);
                }
                if constexpr (KT == 4) {
                    if (h == 0) touch1(tw, oflags - 9 * 8192, ((lane < 10 ? lane : 0) * 256 + bx) * 128u);
                }
                WAIT_LGKM0();
            });
            __builtin_amdgcn_s_setprio(0);
            wo_pre(1);
            BARRIER();
            {
                const int l = lane & 15, gi = h * 3 + (lane < 48 ? lane >> 4 : 0), mr = gi * 15 + l - 1;
                const float* const raw = (const float*)(smem + 131072 + gi * 1024);
                const float mean = mulf(raw[l * 4], 1.f / D_);
                const float var = fmaxf(subf(mulf(raw[((l >> 2) * 16 + l) * 4 + (l & 3)], 1.f / D_), mulf(mean, mean)), 0.f);
                const int row = l == 0 ? 0 : (gi < 9 ? 16 + (mr < 128 ? mr : 127) : l);
                const float sd = rbf(__builtin_amdgcn_sqrtf(rbf(addf(rbf(var), 1e-5f))));
                if (gi < 10) *(float2*)(stat + 2 * row) = make_float2(rbf(mean), __builtin_amdgcn_rcpf(sd));
            }
            WAIT_LGKM0();
            BARRIER();
            if (hl < 48) store16_wt(cso, (unsigned)hl * 16u, *(const u32x4*)(csl + hl * 4));
            wo_res();
            wait_vm<16>();
#pragma unroll
            for (int r = NK - 3 > WV0 ? (NK - 3 - WV0) * WVN : 0; r < NK; ++r) wadd(r);
            asm volatile("s_nop 1\n\tv_add_f32_dpp %0, %0, %0 row_ror:8 row_mask:0xf bank_mask:0xf\n\tv_add_f32_dpp %1, %1, %1 row_ror:8 row_mask:0xf bank_mask:0xf\n\t"
                         "v_add_f32_dpp %2, %2, %2 row_ror:8 row_mask:0xf bank_mask:0xf\n\tv_add_f32_dpp %3, %3, %3 row_ror:8 row_mask:0xf bank_mask:0xf\n\t"
                         "v_add_f32_dpp %4, %4, %4 row_ror:8 row_mask:0xf bank_mask:0xf\n\tv_add_f32_dpp %5, %5, %5 row_ror:8 row_mask:0xf bank_mask:0xf\n\t"
                         "v_add_f32_dpp %6, %6, %6 row_ror:8 row_mask:0xf bank_mask:0xf\n\tv_add_f32_dpp %7, %7, %7 row_ror:8 row_mask:0xf bank_mask:0xf\n\t"
                         "s_nop 1\n\tv_permlane16_swap_b32 %0, %4\n\tv_permlane16_swap_b32 %1, %5\n\tv_permlane16_swap_b32 %2, %6\n\t"
                         "v_permlane16_swap_b32 %3, %7\n\tv_add_f32 %0, %0, %4\n\tv_add_f32 %1, %1, %5\n\tv_add_f32 %2, %2, %6\n\t"
                         "v_add_f32 %3, %3, %7\n\ts_nop 1\n\tv_permlane32_swap_b32 %0, %2\n\tv_permlane32_swap_b32 %1, %3\n\t"
                         "v_add_f32 %0, %0, %2\n\tv_add_f32 %1, %1, %3"
                         : "+v"(sm[0]), "+v"(sm[1]), "+v"(sm[2]), "+v"(sm[3]), "+v"(sm[4]), "+v"(sm[5]), "+v"(sm[6]), "+v"(sm[7]));
            wait_vm<6>();
            if ((lane & 8) == 0 && !idle) {
                const __amdgpu_buffer_rsrc_t r = __builtin_amdgcn_make_buffer_rsrc(cs, 0, 0x7fffffff, 0x00020000);
                __builtin_amdgcn_raw_buffer_store_b64(u32x2{__float_as_uint(sm[0]), __float_as_uint(sm[1])}, r,
                    (unsigned)((kh * 4 + h) * 2 * F_ + cb * 64 + (lane & 7) * 8 + ((lane >> 4) & 1) * 4 + (lane >> 5) * 2) * 4u, 0, 16);
            }
        } else {
            const unsigned* const f = pflags + ((tm && (lane & 16) ? tm - 1 : tm) * 16 + (lane & 15)) * 32;
            unsigned have = 0u;
            auto stage = [&](int st, int sl, auto bc) INL {
                const int kt = qkv_k(st);
                await(have, 0x10001u << kt, f, tok);
                const unsigned da = lds0 + sl * QAS;
#pragma unroll
                for (int i = 0; i < 4; ++i) glds16_dev(uni((const char*)A + kt * 128 + i * 32 * D_ * 2), la, da + h * 1024 + i * 4096);
                glds16_part<32>(uni((const char*)A + kt * 128), lh, da + 16384 + h * 512);
                if constexpr (decltype(bc)::value) qkv_stage_b(lds0, B, lb, h, kt, sl);
            };
            {
                u64 lk;
                gload8_dev(lk, f);
                qkv_stage_b(lds0, B, lb, h, qkv_k(2), qsl(2));
                wait_vm<6>();
                asm volatile("" : "+v"(lk));
                have = (unsigned)__ballot(lk == tok);
            }
            stage(0, qsl(0), CT<0>{});
            stage(1, qsl(1), CT<0>{});
            wait_vm<5>();
            BARRIER();
            stage(2, qsl(2), CT<0>{});
            att_setup(atc, lds0, wid, lane, m0, tn);
            if (h == 0) att_put(atc, smem, lane);
            wait_vm<5>();
            BARRIER();
            stage(3, qsl(3), CT<1>{});
            int sl = qsl(4);
#pragma unroll 1
            for (int st = 4; st < NK; ++st) {
                wait_vm<11>();
                BARRIER();
                stage(st, sl, CT<1>{});
                sl = sl == 2 ? 0 : sl + 1;
            }
            f32x4 spv[8], cstv;
            u64 look;
            asm volatile("s_nop 4\n\tglobal_load_dwordx2 %0, %1, %2 sc1" : "=v"(look) : "v"(0u), "s"(pflags + wtile * 32) : "memory");
            {
                const int srow = m0 - 16 + (hl < 144 ? hl & ~1 : 0);
                const int sr = srow < 0 ? 0 : srow;
                const unsigned so = (unsigned)((sr >> 5) * 512 + (hl & 1) * 256 + (sr & 31)) * 8u;
                gload16_dev(spv[0], sp, so);
                static_for<1, 8>([&](auto ic) INL { gload16_devo<decltype(ic)::value * 256>(spv[decltype(ic)::value], sp, so); });
                gload16_dev(cstv, cso, (unsigned)(hl < 48 ? hl : 0) * 16u);
            }
            wait_vm<21>();
            BARRIER();
            wo_pre(0);
            wait_vm<14>();
            BARRIER();
            wo_pre(1);
            wait_vm<10>();
            asm volatile("" : "+v"(cstv), "+v"(look));
            have = (u64)uni((const char*)look) == tok;
            await(have, 1u, pflags + wtile * 32, tok);
            float sm[4];
#pragma unroll
            for (int i = 0; i < 8; ++i) {
                asm volatile("" : "+v"(spv[i]));
#pragma unroll
                for (int e = 0; e < 4; ++e) sm[e] = i ? addf(sm[e], spv[i][e]) : spv[i][e];
            }
#pragma unroll
            for (int e = 0; e < 4; ++e)
                sm[e] = addf(sm[e], __uint_as_float(__builtin_amdgcn_update_dpp(0u, __float_as_uint(sm[e]), 0xB1, 0xf, 0xf, false)));
            const float mean = mulf(hl & 1 ? sm[2] : sm[0], 1.f / D_);
            const float var = fmaxf(subf(mulf(hl & 1 ? sm[3] : sm[1], 1.f / D_), mulf(mean, mean)), 0.f);
            const float sd = rbf(sqrtf(rbf(addf(rbf(var), 1e-5f))));
            if (hl < 144) *(float2*)(stat + 2 * hl) = make_float2(rbf(mean), 1.f / sd);
            if (hl < 48) *(f32x4*)(csl + hl * 4) = cstv;
            WAIT_LGKM0();
            BARRIER();
            wo_res();
            wait_vm<6>();
        }
        {
            const unsigned em = opaque(~0u);
#pragma unroll
            for (int i = 0; i < 4; ++i) store16_wt(C, BOXO + bid_() * 16384u + (unsigned)(i * 256 + hl) * 16u, u32x4{em, em, em, em});
        }
    } else {
        const int wm = wid >> 1, wn = wid & 1;
        const int l15 = lane & 15, g = lane >> 4, q = l15 >> 2, p4 = l15 & 3;
        unsigned ab[2], hb[2], bj[6];
#pragma unroll
        for (int kk = 0; kk < 2; ++kk) {
            const unsigned sw = ((kk * 4 + g) ^ (l15 >> 1)) * 16;
            ab[kk] = opaque(lds0 + (wm * 64 + l15) * 128 + sw);
            hb[kk] = opaque(lds0 + (128 + l15) * 128 + sw);
        }
#pragma unroll
        for (int j = 0; j < 6; ++j) {
            const int jj = wm && j >= 2 ? 3 - (j >> 1) : j >> 1;
            bj[j] = opaque(lds0 + QBR + jj * 8192 + (g * 8 + q) * 128 + p4 * 8 + (((wn * 2 + ((j & 1) ^ (j < 2 ? wm : 0))) ^ ((q >> 1) | ((g & 1) << 1))) << 5));
        }
        f32x4 acc[5][6];
        constexpr auto qcol = [](int c) { return c == 0 ? 1 : (c < 3 ? c + 3 : (c == 3 ? 0 : c - 2)); };
        auto ldop = [&](bf16x8 (&af)[10], s16x4 (&bf)[24], auto ktc, auto oc) INL {
            constexpr int KT = decltype(ktc)::value, O = decltype(oc)::value;
            if constexpr (O == 2 || (O >= 5 && O < 12)) {
                constexpr int X = O == 2 ? 0 : O - 4;
                af[X] = lds_a<qsl(KT) * QAS + (X / 2) * 2048>(ab[X & 1]);
            } else if constexpr (O == 24 || O == 25) {
                af[O - 16] = lds_a<qsl(KT) * QAS>(hb[O & 1]);
            } else {
                constexpr int X = O < 2 ? O : (O < 5 ? O - 1 : (O < 24 ? O - 8 : O - 10));
                bf[X] = lds_h<qsl(KT) * QBS + ((X >> 1) & 1) * 4096 + (X & 1) * 512>(bj[qcol(X / 4)]);
            }
        };
        unsigned ha[3][2];
        f32x4 rg[3];
        bf16x8 gf[2];
        auto hrow = [&](int i, int l) INL {
            const int gi = wid * 3 + i, mr = gi * 15 + l - 1;
            return l == 0 ? 128 : (gi < 9 ? (mr < 128 ? mr : 127) : 128 + l);
        };
        auto unit = [&](bf16x8 (&af)[10], s16x4 (&bf)[24], bf16x8 (&an)[10], s16x4 (&bn)[24], auto ktc, auto hc, auto fic) INL {
            constexpr bool FI = decltype(fic)::value;
            constexpr int KT = decltype(ktc)::value, HF = decltype(hc)::value;
            static_for<0, 26>([&](auto tc) INL {
                constexpr int T = decltype(tc)::value, X = HF * 26 + T, kk = X & 1;
                constexpr int c = X < 32 ? X / 8 : (X < 42 ? 4 : 5), p = (X < 32 ? X % 8 : (X < 42 ? X - 32 : X - 42)) / 2;
                constexpr int i = c == 5 ? 4 - p : (c & 1 ? 3 - p : p);
                mma_ip(cat(bf[c * 4 + kk * 2], bf[c * 4 + kk * 2 + 1]), af[i * 2 + kk], acc[i][qcol(c)]);
                SCHED();
                if constexpr (HF == 1) {
                    if constexpr (KT + 1 < NK && T < QG1) ldop(an, bn, CT<KT + 1>{}, tc);
                    if constexpr (KT + 1 < NK && T >= QL1 && T < QL1 + 16 - QG1) ldop(an, bn, CT<KT + 1>{}, CT<T - QL1 + QG1>{});
                } else if constexpr (FI) {
                    if constexpr (T % 8 < 2 && T < 24) gf[T & 1] = lds_a<qsl(KT) * QAS>(ha[T / 8][T & 1]);
                    else if constexpr (T < 20) ldop(af, bf, ktc, CT<14 + T>{});
                } else {
                    if constexpr (T < 18) ldop(af, bf, ktc, CT<16 + T>{});
                }
                SCHED();
                if constexpr (FI && HF == 0 && T % 8 == 7) {
                    mma_ip(gf[0], gf[0], rg[T / 8]);
                    SCHED();
                    if constexpr (T < 16) ldop(af, bf, ktc, CT<16 + T - 1>{});
                    SCHED();
                    mma_ip(gf[1], gf[1], rg[T / 8]);
                    SCHED();
                    if constexpr (T < 16) ldop(af, bf, ktc, CT<16 + T>{});
                    SCHED();
                }
                if constexpr (FI && X == 31) {
                    mma_ip(cat(bf[12], bf[13]), af[8], acc[4][0]);
                    SCHED();
                    mma_ip(cat(bf[14], bf[15]), af[9], acc[4][0]);
                    SCHED();
                }
            });
        };
        if (first) token();
        if (!first && id == 0 && wid == 3) {
#pragma unroll 1
            for (unsigned spins = 0; spins < (1u << 22); ++spins) {
                bool ok = true;
#pragma unroll
                for (int i = 0; i < 4; ++i)
                    ok &= __hip_atomic_load((gu64*)(oflags + 8192 + (i * 64 + lane) * 32), __ATOMIC_RELAXED, __HIP_MEMORY_SCOPE_AGENT) == tok;
                if (__all(ok)) break;
            }
            if (lane == 0) __hip_atomic_store((gu64*)epoch, tok, __ATOMIC_RELAXED, __HIP_MEMORY_SCOPE_AGENT);
        }
        bf16x8 a0[10], a1[10], a2[10];
        s16x4 b0[24], b1[24], b2[24];
        auto loop = [&](auto fic) INL {
#pragma unroll
        for (int i = 0; i < 5; ++i)
#pragma unroll
            for (int j = 0; j < 6; ++j)
                if (i < 4 || (j >> 1) == 1) {
                    acc[i][j] = f32x4{0.f, 0.f, 0.f, 0.f};
                    asm volatile("" : "+v"(acc[i][j]));
                }
            if constexpr (decltype(fic)::value) wait_vm<11>();
            BARRIER();
            static_for<0, 16>([&](auto oc) INL { ldop(a0, b0, CT<0>{}, oc); });
            auto ktile = [&](bf16x8 (&af)[10], s16x4 (&bf)[24], bf16x8 (&an)[10], s16x4 (&bn)[24], auto kc) INL {
                unit(af, bf, an, bn, kc, CT<0>{}, fic);
                WAIT_LGKM0();
                if constexpr (decltype(kc)::value == 0 && decltype(fic)::value) wait_vm<0>();
                BARRIER();
                unit(af, bf, an, bn, kc, CT<1>{}, fic);
            };
#pragma unroll 1
            for (int it = 0; it < 5; ++it) {
                ktile(a0, b0, a1, b1, CT<0>{});
                ktile(a1, b1, a2, b2, CT<1>{});
                ktile(a2, b2, a0, b0, CT<2>{});
            }
            unit(a0, b0, a1, b1, CT<NK - 1>{}, CT<0>{}, fic);
            if constexpr (decltype(fic)::value) {
                asm volatile("s_nop 15" ::: "memory");
                SCHED();
#pragma unroll
                for (int i = 0; i < 3; ++i) *(f32x4*)(smem + 131072 + (wid * 3 + i) * 1024 + (opaque(threadIdx.x) & 63u) * 16) = rg[i];
                WAIT_LGKM0();
                BARRIER();
                SCHED();
            }
            unit(a0, b0, a1, b1, CT<NK - 1>{}, CT<1>{}, fic);
            asm volatile("s_nop 15\n\ts_nop 7" ::: "memory");
            SCHED();
        };
        if (first) {
#pragma unroll
            for (int i = 0; i < 3; ++i) {
                const int hr = hrow(i, l15);
#pragma unroll
                for (int kk = 0; kk < 2; ++kk) ha[i][kk] = opaque(lds0 + hr * 128 + (((kk * 4 + g) ^ ((hr >> 1) & 7)) * 16));
                rg[i] = f32x4{0.f, 0.f, 0.f, 0.f};
                asm volatile("" : "+v"(rg[i]));
            }
            acc[4][0] = f32x4{0.f, 0.f, 0.f, 0.f};
            asm volatile("" : "+v"(acc[4][0]));
            loop(CT<1>{});
            const int le = (int)opaque(threadIdx.x) & 63, l = le & 15;
            if (l == 0) {
#pragma unroll
                for (int x = 0; x < 3; ++x) {
                    const int col = (x ? (1 + wm) * 64 + (x - 1) * 16 : wm * 16) + wn * 32 + (le >> 4) * 4;
                    *(f32x4*)(csl + col) = acc[4][x ? 1 + x : 0];
                }
            }
        } else {
            loop(CT<0>{});
        }
        const int tide = (int)opaque(threadIdx.x);
        const int l15e = tide & 15, ge = (tide & 63) >> 4;
        WAIT_LGKM0();
        BARRIER();
        f32x4 ccv[6];
        float2 stv[5];
        auto colof = [&](int j) INL { return (wm && j >= 2 ? 3 - (j >> 1) : j >> 1) * 64 + wn * 32 + ((j & 1) ^ (j < 2 ? wm : 0)) * 16 + ge * 4; };
#pragma unroll
        for (int j = 0; j < 6; ++j) ccv[j] = *(const f32x4*)(csl + colof(j));
#pragma unroll
        for (int i = 0; i < 5; ++i) stv[i] = *(const float2*)(stat + 2 * ((i < 4 ? 16 + wm * 64 + i * 16 : 0) + l15e));
#pragma unroll
        for (int i = 0; i < 5; ++i) {
            const int row = (i < 4 ? 16 + wm * 64 + i * 16 : 0) + l15e;
            const float rstd = stv[i].y, mr = -stv[i].x * rstd;
#pragma unroll
            for (int j = 0; j < 6; ++j)
                if (i < 4 || (j >> 1) == 1) {
                    const f32x2 r2 = {rstd, rstd}, m2 = {mr, mr};
                    const f32x2 y0 = __builtin_elementwise_fma(acc[i][j].xy, r2, m2 * ccv[j].xy);
                    const f32x2 y1 = __builtin_elementwise_fma(acc[i][j].zw, r2, m2 * ccv[j].zw);
                    u32x2 v = {pk2(y0.x, y0.y), pk2(y1.x, y1.y)};
                    *(u32x2*)(smem + row * ACS + colof(j) * 2) = v;
                }
        }
        SCHED();
        att_get(atc, smem, wid, tide & 63, m0);
    }
    WAIT_LGKM0();
    BARRIER();
    asm volatile("" ::: "memory");
    att_run(atc, C);
    {
        const unsigned t = opaque(threadIdx.x), l15 = t & 15u;
        const unsigned ra = lds0 + RES + ((t & 128u) / 2 + l15) * 128 + ((((t >> 4) & 7u) ^ (l15 >> 1)) * 16);
        static_for<0, 4>([&](auto ic) INL { rr[decltype(ic)::value] = lds_a<decltype(ic)::value * 2048>(ra); });
        asm volatile("" : "+v"(rr[0]), "+v"(rr[1]), "+v"(rr[2]), "+v"(rr[3]));
    }
    publish(oflags + (tm * 16 + tn) * 32, tok, 65280);
    if (wid >= 4) {
        const unsigned wb = wo_boff(wid - 4, lane);
#pragma unroll
        for (int st = 8; st < WONB; ++st) wo_stage_b(lds0, wo, wb, wid - 4, st);
    }
}

__device__ __forceinline__ unsigned dn_boff(int h, int lane) { return b_off(h, lane, D_, (bid_() >> 5) * 128); }
__device__ __forceinline__ void dn_stage_b(unsigned lds0, const u16* B, unsigned boff, int kh, int h, int i, int kg, int sl) {
    glds16(uni((const char*)B + (long long)(kg * 64 + (i & 1) * 32) * D_ * 2 + ((i >> 1) ^ kh) * 128), boff,
           lds0 + h * 1024 + sl * 16384 + i * 4096);
}
constexpr int DNA = 65536, DNR = 98304;
#define BOX_LOOK(r) "global_load_dwordx4 v[" r ":" r "+3], %14, %15 sc1\n\tglobal_load_dwordx4 v[" r "+4:" r "+7], %14, %15 offset:1024 sc1\n\t" \
    "global_load_dwordx4 v[" r "+8:" r "+11], %14, %15 offset:2048 sc1\n\tglobal_load_dwordx4 v[" r "+12:" r "+15], %14, %15 offset:3072 sc1\n\t"
#define BOX_MAX(r, i) "v_max3_u32 %12, %12, v[" r "+" i "], v[" r "+" i "+1]\n\t"
#define BOX_TEST(r) "s_waitcnt vmcnt(4)\n\tv_max_u32 %12, v[" r "], v[" r "+1]\n\t" BOX_MAX(r, "2") BOX_MAX(r, "4") BOX_MAX(r, "6") \
    BOX_MAX(r, "8") BOX_MAX(r, "10") BOX_MAX(r, "12") BOX_MAX(r, "14") "v_cmp_eq_u32 vcc, -1, %12\n\t"
#define BOX_MOV(r, i) "v_mov_b64 v[200+" i ":201+" i "], v[" r "+" i ":" r "+1+" i "]\n\t"
#define BOX_TAKE(r) BOX_MOV(r, "0") BOX_MOV(r, "2") BOX_MOV(r, "4") BOX_MOV(r, "6") BOX_MOV(r, "8") BOX_MOV(r, "10") BOX_MOV(r, "12") BOX_MOV(r, "14")
__device__ __forceinline__ void box_wait(u32x4 (&a)[4], u32x4 (&b)[4], u32x4 (&c)[4], const void* box, unsigned off) {
    unsigned t, n;
    asm volatile(BOX_TEST("200") "s_cbranch_vccz 3f\n\ts_mov_b32 %13, 0x100000\n"
                 "1:\n\t" BOX_LOOK("232") BOX_TEST("216") "s_cbranch_vccz 2f\n\t" BOX_LOOK("216") BOX_TEST("232")
                 "s_cbranch_vccz 4f\n\ts_sub_u32 %13, %13, 1\n\ts_cbranch_scc0 1b\n"
                 "4:\n\t" BOX_TAKE("232") "s_branch 5f\n"
                 "2:\n\t" BOX_TAKE("216") "\n5:\n\ts_nop 1\n3:"
                 : "+{v[200:203]}"(a[0]), "+{v[204:207]}"(a[1]), "+{v[208:211]}"(a[2]), "+{v[212:215]}"(a[3]),
                   "+{v[216:219]}"(b[0]), "+{v[220:223]}"(b[1]), "+{v[224:227]}"(b[2]), "+{v[228:231]}"(b[3]),
                   "=&{v[232:235]}"(c[0]), "=&{v[236:239]}"(c[1]), "=&{v[240:243]}"(c[2]), "=&{v[244:247]}"(c[3]), "=&v"(t), "=&s"(n)
                 : "v"(off), "s"(box) : "memory", "vcc", "scc");
}
__device__ __forceinline__ void dn_phase(char* smem, const u16* A, const u16* __restrict__ B, u16* C, float* sp,
                                         float* slab, const unsigned* gflags, unsigned* oflags,
                                         const u16* pf, u64 tok) {
    constexpr int BM = 128, NS = 4, NSO = NSOLO, NSH = (F_ / 64 - NSO) / 2, NK = NSH + NSO;
    static_assert(NSO % 2 == 0, "k-tile program");
    constexpr int NRT = (NK - 1 < NSH ? NK - 1 : NSH) / NS * NS;
    constexpr int ASZ = BM * 128, SLOT = 2 * ASZ;
    constexpr int lda = F_;
    constexpr int BOX = 16384;

    const int tid = (int)opaque(threadIdx.x), lane = tid & 63;
    const int wid = __builtin_amdgcn_readfirstlane(tid >> 6);
    const unsigned bx = bid_();
    const int xcd = bx & 7, r_ = bx >> 3;
    const int kh = xcd >> 2, tm = (xcd & 3) * 4 + (r_ & 3), tn = r_ >> 2;
    const int m0 = tm * BM, n0 = tn * 128 + kh * 64;
    const unsigned lds0 = __builtin_amdgcn_readfirstlane((unsigned)(size_t)(LDS_AS char*)smem);
    ResC rc;
    u32x4 pb[4], pc[4];
    if (wid >= 4) {
        const int h = wid - 4;
        const unsigned aoff = (unsigned)((m0 / 4 + h * 2 + (lane >> 5)) * (8 * F_) + (((lane >> 3) & 3) ^ (h & 1)) * 128 + (lane & 7) * 16);
        const unsigned boff = dn_boff(h, lane);
        const unsigned* const f = gflags + ((tm >> 1) * 32 + (lane & 31)) * 32;
        unsigned have = 0u;
        auto stage = [&](int kg, int sl, auto nbc) INL {
            await(have, (1u << (kg * 4 / 7)) | (1u << ((kg * 64 + 63) / 112)), f, tok);
#pragma unroll
            for (int i = 0; i < 4; ++i)
                glds16_dev(uni((const char*)A + kg * 512 + i * 32 * lda * 2), aoff, lds0 + DNA + h * 1024 + sl * ASZ + i * 4096);
#pragma unroll
            for (int i = 0; i < decltype(nbc)::value; ++i) dn_stage_b(lds0, B, boff, kh, h, i, kg, sl);
        };
        const int kg0 = kh * NSH;
        constexpr int NB0 = 0;
        stage(kg0, 0, CT<NB0>{});
        stage(kg0 + 1, 1, CT<NB0>{});
        wait_vm<4 + NB0>();
        BARRIER();
        stage(kg0 + 2, 2, CT<NB0>{});
        stage(kg0 + 3, 3, CT<NB0>{});
        wait_vm<8 + 2 * NB0>();
        BARRIER();
        stage(kg0 + 4, 0, CT<4>{});
        wait_vm<12 + NB0>();
        BARRIER();
        stage(kg0 + 5, 1, CT<4>{});
#pragma unroll 1
        for (int kt = 2; kt < NSH - NS; ++kt) {
            wait_vm<16>();
            BARRIER();
            stage(kg0 + kt + NS, kt & 3, CT<4>{});
        }
        static_for<NSH - NS, NK - 1>([&](auto kc) INL {
            constexpr int KT = decltype(kc)::value;
            constexpr int P2 = KT + 2 < NSH ? 8 : (KT + 2 < NK ? 6 : 0), P3 = KT + 3 < NSH ? 8 : (KT + 3 < NK ? 6 : 0);
            wait_vm<P2 + P3>();
            BARRIER();
            if constexpr (KT + NS < NK) stage(NSH + KT + NS, KT & 3, CT<2>{});
        });
        static_assert(NK % NS == 2, "dead slots");
        if (pf != nullptr) {
            for (int i = QPS; i > 0; --i) __builtin_amdgcn_s_sleep(1);
            qkv_stage_b(lds0, pf, qkv_boff(h, lane), h, qkv_k(0), qsl(0));
            qkv_stage_b(lds0, pf, qkv_boff(h, lane), h, qkv_k(1), qsl(1));
        }
        res_setup(rc, lds0 + NS * SLOT, m0, n0, tid);
        if (pf != nullptr && (bx & 1) == 0 && wid >= 6) {
            const unsigned ix = (bx >> 3) * 96 + (tid < 480 ? tid - 384 : 0);
            touch1(SINK, pf, ix * 2048 + ((bx & 3) * 4 + 3) * 128);
        }
#pragma unroll
        for (int i = 0; i < 4; ++i) asm volatile("" : "=v"(pb[i]), "=v"(pc[i]));
    } else {
        const int wm = wid >> 1, wn = wid & 1;
        const int l15 = lane & 15, g = lane >> 4, q = l15 >> 2, p4 = l15 & 3;
        unsigned ab[2], bb[2];
#pragma unroll
        for (int x = 0; x < 2; ++x) {
            ab[x] = opaque(lds0 + DNA + (wm * 16 + q) * 512 + (((x * 2 + (g >> 1)) ^ (l15 >> 3)) * 128) + p4 * 32 + (g & 1) * 16);
            bb[x] = opaque(lds0 + (g * 8 + q) * 128 + p4 * 8 + (((wn * 2 + x) ^ ((q >> 1) | ((g & 1) << 1))) << 5));
        }
        f32x4 acc[4][4];
        {
            const unsigned ra = lds0 + DNR + (wm * 64 + l15) * 128 + (((wn * 4 + g) ^ (l15 >> 1)) * 16);
            const bf16x8 selr[2] = {pick(g == (l15 >> 3), l15 & 7), pick(g == 2 + (l15 >> 3), l15 & 7)};
            static_for<0, 4>([&](auto ic) INL {
                constexpr int i = decltype(ic)::value;
                const bf16x8 rr = lds_a<i * 2048>(ra);
#pragma unroll
                for (int j = 0; j < 4; ++j) {
                    acc[i][j] = f32x4{0.f, 0.f, 0.f, 0.f};
                    if (j >= 2) acc[i][j] = mma(selr[j & 1], rr, acc[i][j]);
                    asm volatile("" : "+v"(acc[i][j]));
                }
            });
        }
        auto ldop = [&](bf16x8 (&af)[8], s16x4 (&bf)[16], auto slc, auto j0c, auto oc) INL {
            constexpr int SL = decltype(slc)::value, O = decltype(oc)::value;
            if constexpr (O == 2 || (O >= 5 && O < 12)) {
                constexpr int X = O == 2 ? 0 : O - 4;
                af[(X & 1) * 4 + X / 2] = lds_a<SL * ASZ + (X / 2) * 2048>(ab[X & 1]);
            } else {
                constexpr int X = decltype(j0c)::value * 4 + (O < 2 ? O : (O < 5 ? O - 1 : O - 8)), J = X / 4;
                bf[X] = lds_h<SL * ASZ + (J < 2 ? 8192 : 0) + ((X >> 1) & 1) * 4096 + (X & 1) * 512>(bb[J & 1]);
            }
        };
        auto ktile = [&](bf16x8 (&af)[8], s16x4 (&bf)[16], bf16x8 (&an)[8], s16x4 (&bn)[16], auto nslc, auto j0c, auto jnc, auto&& gap) INL {
            constexpr int J0 = decltype(j0c)::value, JN = decltype(jnc)::value;
            constexpr int NM = 8 * (4 - J0), NL = JN < 4 ? 8 + 4 * (4 - JN) : 0, TB = J0 ? DBAR2 : DBAR;
            constexpr int NG = NM - TB < NL ? NM - TB : NL;
            static_for<0, NM>([&](auto tc) INL {
                constexpr int T = decltype(tc)::value, j = J0 + T / 8, i = j & 1 ? 3 - (T / 2) % 4 : (T / 2) % 4, kk = T & 1;
                if constexpr (T == TB && NL > 0) {
                    WAIT_LGKM0();
                    BARRIER();
                }
                mma_ip(cat(bf[j * 4 + kk * 2], bf[j * 4 + kk * 2 + 1]), af[kk * 4 + i], acc[i][j]);
                SCHED();
                if constexpr (NL > 0 && T >= TB) {
                    constexpr int U = T - TB, lo_ = (U * NL + NG - 1) / NG, hi_ = ((U + 1) * NL + NG - 1) / NG;
                    static_for<lo_, (hi_ < NL ? hi_ : NL)>([&](auto oc) INL { ldop(an, bn, nslc, jnc, oc); });
                }
                gap(tc);
                SCHED();
            });
        };
        bf16x8 a0[8], a1[8];
        s16x4 b0[16], b1[16];
        const unsigned bo = opaque((unsigned)(wid * 4096 + lane * 16));
        const bf16x8 sel[2] = {pick(g == q, p4), pick(g == q, 4 + p4)};
        res_setup(rc, lds0 + NS * SLOT, m0, n0, tid);
        const unsigned cw = opaque(lds0 + NS * SLOT + (wm * 64 + l15) * CSTR + (wn * 32 + g * 4) * 2);
        const char* const rin = sopq((const char*)slab + (bx ^ 4u) * BOX);
        const __amdgpu_buffer_rsrc_t rout = __builtin_amdgcn_make_buffer_rsrc((char*)slab + bx * BOX, 0, BOX, 0x00020000);
        BARRIER();
        static_for<0, 24>([&](auto oc) INL { ldop(a0, b0, CT<0>{}, CT<0>{}, oc); });
        auto none = [&](auto) INL {};
#pragma unroll 1
        for (int kt = 0; kt < NRT; kt += NS) {
            ktile(a0, b0, a1, b1, CT<1>{}, CT<0>{}, CT<0>{}, none);
            ktile(a1, b1, a0, b0, CT<2>{}, CT<0>{}, CT<0>{}, none);
            ktile(a0, b0, a1, b1, CT<3>{}, CT<0>{}, CT<0>{}, none);
            ktile(a1, b1, a0, b0, CT<0>{}, CT<0>{}, CT<0>{}, none);
        }
        u32x4 ow, pa[4];
        auto look = [&](u32x4& d, auto ic) INL { gload16_devo<decltype(ic)::value * 1024>(d, rin, bo); };
        static_for<NRT, NK>([&](auto sc) INL {
            constexpr int KT = decltype(sc)::value;
            constexpr int J0 = KT < NSH ? 0 : 2, JN = KT + 1 < NSH ? 0 : (KT + 1 < NK ? 2 : 4);
            auto gap = [&](auto tc) INL {
                constexpr int T = decltype(tc)::value;
                if constexpr (KT == NSH - 1 && T >= XO && T < XO + 16) {
                    constexpr int U = T - XO, j = U / 8, i = j ? 3 - (U / 2) % 4 : (U / 2) % 4;
                    asm volatile("v_cvt_pk_bf16_f32 %0, %1, %2\n\tv_min_u32 %0, -2, %0"
                                 : "=v"(ow[(i & 1) * 2 + (U & 1)]) : "v"(acc[i][j][(U & 1) * 2]), "v"(acc[i][j][(U & 1) * 2 + 1]));
                    if constexpr (U % 4 == 3) __builtin_amdgcn_raw_buffer_store_b128(ow, rout, bo + (j * 2 + i / 2) * 1024, 0, 16);
                }
                constexpr int X = KT < NSH ? -999 : (KT - NK) * 16 + T;
                if constexpr (X >= -XA && X < -XA + 4) look(pa[X + XA], CT<X + XA>{});
                if constexpr (X >= -XB && X < -XB + 4) look(pb[X + XB], CT<X + XB>{});
            };
            if constexpr (KT & 1) ktile(a1, b1, a0, b0, CT<(KT + 1) % NS>{}, CT<J0>{}, CT<JN>{}, gap);
            else ktile(a0, b0, a1, b1, CT<(KT + 1) % NS>{}, CT<J0>{}, CT<JN>{}, gap);
        });
        box_wait(pa, pb, pc, rin, bo);
#pragma unroll
        for (int t = 0; t < 8; ++t) mma_ip(sel[(t >> 1) & 1], __builtin_bit_cast(bf16x8, pa[(t & 1) * 2 + t / 4]), acc[t >> 1][2 + (t & 1)]);
        asm volatile("s_nop 1" ::: "memory");
        SCHED();
#pragma unroll
        for (int i = 0; i < 4; ++i) {
#pragma unroll
            for (int x = 0; x < 2; ++x) {
                const f32x4 a = acc[i][2 + x];
                *(LDS_AS u32x2*)(size_t)(cw + i * 16 * CSTR + x * 32) = u32x2{pk2(a[0], a[1]), pk2(a[2], a[3])};
            }
            SCHED();
        }
    }
    WAIT_LGKM0();
    BARRIER();
    res_tail(rc, C, sp);
    if (pf != nullptr) publish(oflags + (tm * 16 + (n0 >> 6)) * 32, tok); else wait_vm<0>();
#pragma unroll
    for (int i = 0; i < 4; ++i) asm volatile("" :: "v"(pb[i]), "v"(pc[i]));
}

__device__ __forceinline__ void gu_phase(char* smem, const u16* A, const u16* B0, const u16* B1, u16* C, const float* cs,
                                         const float* sp, const u16* wd, bool first, unsigned* flags, u64 tok) {
    constexpr int BM = 256, BN = 224, TM = 64, MI = 4, NI = 7, NK = D_ / 64;
    constexpr int ASZ = BM * 128, BSZ = BN * 128, NSA = 3, NSB = 2;
    constexpr int BRING = NSA * ASZ, RING = BRING + NSB * BSZ, STL = RING + BN * 4, MIX = DNR + 16384;
    constexpr auto asl = [](int kt) { return (kt + 2) % NSA; };

    const int tid = (int)opaque(threadIdx.x), lane = tid & 63;
    const int wid = __builtin_amdgcn_readfirstlane(tid >> 6);
    const int wm = wid >> 1, wn = wid & 1;
    const int id = wg_id();
    const int tm = id & 7, tn = id >> 3, m0 = tm * BM;
    float* const csl = (float*)(smem + RING);
    const unsigned lds0 = __builtin_amdgcn_readfirstlane((unsigned)(size_t)(LDS_AS char*)smem);
    const unsigned ldsw = lds0 + wid * 1024;

    unsigned aoff[4], boff[4];
#pragma unroll
    for (int i = 0; i < 4; ++i) {
        const int row = tid >> 3, o = i * 8192 + tid * 16;
        aoff[i] = opaque((unsigned)((m0 + i * 64 + row) * D_ + ((tid & 7) ^ ((row >> 1) & 7)) * 8) * 2u);
        boff[i] = opaque(gu_bsrc(o < BSZ ? o : 0, tn));
    }
    const __amdgpu_buffer_rsrc_t ra = __builtin_amdgcn_make_buffer_rsrc((void*)A, 0, 0x7fffffff, 0x00020000);
    const __amdgpu_buffer_rsrc_t rb[4] = {__builtin_amdgcn_make_buffer_rsrc((void*)B0, 0, 0x7fffffff, 0x00020000),
                                          __builtin_amdgcn_make_buffer_rsrc((void*)(wid >= 6 ? B1 : B0), 0, 0x7fffffff, 0x00020000),
                                          __builtin_amdgcn_make_buffer_rsrc((void*)B1, 0, 0x7fffffff, 0x00020000),
                                          __builtin_amdgcn_make_buffer_rsrc((void*)B1, 0, wid < 4 ? 0x7fffffff : 0, 0x00020000)};
    const unsigned b3d[2] = {wid < 4 ? ldsw + BRING + 24576 : lds0 + 161792, wid < 4 ? ldsw + BRING + BSZ + 24576 : lds0 + 161792};
    unsigned soff = 0u;
    auto prep = [&](auto bc, auto pc, auto ktc) INL {
        constexpr int P = decltype(pc)::value, KT = decltype(ktc)::value;
        constexpr bool ISB = decltype(bc)::value;
        if constexpr (P == 0) s_set<(ISB ? KT * 64 * F_ * 2 : KT * 128)>(soff);
        if constexpr (ISB && P == 3) m0_set<0>(b3d[KT % NSB]);
        else m0_set<(ISB ? BRING + (KT % NSB) * BSZ : asl(KT) * ASZ) + P * 8192>(ldsw);
    };
    const int dtm = ((id >> 5) & 3) * 4 + (id & 3), dtn = ((id >> 2) & 7) * 2 + (id >> 7);
    const unsigned dkh = (id >> 7) * 128;
    const __amdgpu_buffer_rsrc_t rd = __builtin_amdgcn_make_buffer_rsrc((char*)wd + dkh * ((F_ / 64 - NSOLO) / 2 * D_) + (dtn >> 1) * 256, 0,
        0x7fffffff, 0x00020000);
    const __amdgpu_buffer_rsrc_t rr = __builtin_amdgcn_make_buffer_rsrc((char*)A + (dtm * 128 - m0) * D_ * 2 + dtn * 128, 0, 0x7fffffff, 0x00020000);
    unsigned doff;
    constexpr auto dreq = [](int sl) { return sl < 0 ? -1 : (int)((DSCH >> (4 * sl)) & 15) - 1; };
    constexpr int DV = (dreq(0) >= 0) + (dreq(1) >= 0) + (dreq(2) >= 0) + (dreq(3) >= 0) + (TCU <= 0);
    auto dprep = [&](auto xc) INL {
        constexpr int X = decltype(xc)::value;
        if constexpr (X == 0) {
            const int t = (int)opaque(threadIdx.x);
            doff = opaque(b_off(t >> 6, t & 63, D_, 0));
        }
        if constexpr (X < 8) asm volatile("s_xor_b32 %0, %1, %2" : "=s"(soff) : "s"(dkh), "n"((X >> 1) * 64 * D_ * 2 + (X & 1) * 128) : "scc");
        m0_set<(X < 8 ? 0 : DNR - 65536) + X * 8192>(ldsw);
    };
    auto dgo = [&](auto xc) INL {
        constexpr int X = decltype(xc)::value;
        if constexpr (X < 8) blds16<false>(rd, doff, soff);
        else blds16<true>(rr, aoff[X - 8], 0u);
    };
    auto go = [&](auto bc, auto pc) INL {
        if constexpr (decltype(bc)::value) blds16<false>(rb[decltype(pc)::value], boff[decltype(pc)::value], soff);
        else blds16<true>(ra, aoff[decltype(pc)::value], soff);
    };
    auto stage_a = [&](auto ktc) INL {
        static_for<0, 4>([&](auto pc) INL { prep(CT<0>{}, pc, ktc); asm volatile("s_nop 0"); go(CT<0>{}, pc); });
    };
    unsigned tof;
    {
        const unsigned t = tid & 255, kr = ((id >> 7) & 1) * 1664, j = (id & 31) * 256 + t;
        unsigned rw = j >> 2;
        rw = rw < 1664 ? kr + rw : (rw < 1920 ? rw + 1664 : kr);
        tof = opaque(wid < 4 ? rw * 2048 + 256 + (j & 1) * 128 + (j & 2) * 512 : (first ? (id * 224 + (t < 224 ? t : 0u)) * 128u : 0u));
    }
    __builtin_amdgcn_s_setprio(2);

    const int l15 = lane & 15, g = lane >> 4;
    const int q = l15 >> 2, p4 = l15 & 3;
    unsigned ab[2][2], bb[2][2];
#pragma unroll
    for (int kk = 0; kk < 2; ++kk) {
        const unsigned sw = ((kk * 4 + g) ^ (l15 >> 1)) * 16;
        ab[kk][0] = opaque(lds0 + (wm * TM + l15) * 128 + sw);
        ab[kk][1] = opaque(lds0 + (wm * TM + l15) * 128 + sw + 65536);
        const unsigned r = lds0 + BRING + ((g * 8 + kk * 4 + q) ^ (4 * (g & 1))) * BN;
        bb[kk][0] = opaque(r + wn * 128 + p4 * 8);
        bb[kk][1] = opaque(r + 96 + wn * 16 + (p4 & 1) * 8 + (p4 >> 1) * (BSZ / 2));
    }
    f32x4 acc[MI][NI];
#pragma unroll
    for (int i = 0; i < MI; ++i)
#pragma unroll
        for (int j = 0; j < NI; ++j) {
            acc[i][j] = f32x4{0.f, 0.f, 0.f, 0.f};
            asm volatile("" : "+v"(acc[i][j]));
        }
    bf16x8 af[2][2 * MI];
    s16x4 bf[2][4 * NI];
    auto ldop = [&](auto ktc, auto oc) INL {
        constexpr int KT = decltype(ktc)::value, O = decltype(oc)::value;
        if constexpr (O == 2 || (O >= 5 && O < 12)) {
            constexpr int X = O == 2 ? 0 : O - 4, off = asl(KT) * ASZ + (X / 2) * 2048;
            af[KT & 1][X] = lds_a<(off & 65535)>(ab[X & 1][off >> 16]);
        } else {
            constexpr int X = O < 2 ? O : (O < 5 ? O - 1 : O - 8), J = X / 4;
            bf[KT & 1][X] = lds_h<(KT % NSB) * BSZ + ((X >> 1) & 1) * 32 * BN + (J == 6 ? 0 : (J / 3) * (BSZ / 2) + (J % 3) * 32)>(bb[X & 1][J == 6]);
        }
    };
    f32x4 stn[4];
    float sx[8];
    const unsigned sto = opaque(wid * 4096 + g * 1024 + l15 * 16);
    const char* const spw = uni((const char*)sp + m0 * 128);
    auto stats = [&](auto zc) INL {
        constexpr int Z = decltype(zc)::value;
        if constexpr (Z == 0) {
#pragma unroll
            for (int i = 0; i < 4; ++i) asm volatile("" : "+v"(stn[i]));
        }
        if constexpr (Z < 8) asm volatile("v_add_f32 %0, %1, %2" : "=v"(sx[Z]) : "v"(stn[Z / 4 * 2][Z % 4]), "v"(stn[Z / 4 * 2 + 1][Z % 4]));
        else if constexpr (Z < 12) asm volatile("v_add_f32 %0, %0, %1" : "+v"(sx[(Z & 1) * 2 + (Z / 2 & 1)]) : "v"(sx[4 + (Z & 1) * 2 + (Z / 2 & 1)]));
        else if constexpr (Z < 14) asm volatile("v_permlane16_swap_b32 %0, %1" : "+v"(sx[Z - 12]), "+v"(sx[Z - 10]));
        else if constexpr (Z < 16) asm volatile("v_add_f32 %0, %0, %1" : "+v"(sx[Z - 14]) : "v"(sx[Z - 12]));
        else if constexpr (Z == 17 || Z == 22) asm volatile("v_permlane32_swap_b32 %0, %1" : "+v"(sx[0]), "+v"(sx[1]));
        else if constexpr (Z == 18) asm volatile("v_add_f32 %0, %0, %1" : "+v"(sx[0]) : "v"(sx[1]));
        else if constexpr (Z == 19) asm volatile("v_mul_f32 %0, 0x3a800000, %1" : "=v"(sx[1]) : "v"(sx[0]));
        else if constexpr (Z == 20) asm volatile("v_mul_f32 %0, 0x3a800000, %0" : "+v"(sx[0]));
        else if constexpr (Z == 23) asm volatile("v_mul_f32 %0, %1, %1" : "=v"(sx[2]) : "v"(sx[0]));
        else if constexpr (Z == 24) asm volatile("v_sub_f32 %0, %0, %1" : "+v"(sx[1]) : "v"(sx[2]));
        else if constexpr (Z == 25) asm volatile("v_max_f32 %0, 0, %0" : "+v"(sx[1]));
        else if constexpr (Z == 26) asm volatile("v_cvt_pk_bf16_f32 %0, %0, -%1" : "+v"(sx[1]) : "v"(sx[0]));
        else if constexpr (Z == 27) asm volatile("v_and_b32 %0, 0xffff0000, %1" : "=v"(sx[0]) : "v"(sx[1]));
        else if constexpr (Z == 28 || Z == 31) asm volatile("v_lshlrev_b32 %0, 16, %0" : "+v"(sx[1]));
        else if constexpr (Z == 29) asm volatile("v_add_f32 %0, 0x3727c5ac, %0" : "+v"(sx[1]));
        else if constexpr (Z == 30 || Z == 33) asm volatile("v_cvt_pk_bf16_f32 %0, %0, %0" : "+v"(sx[1]));
        else if constexpr (Z == 32) asm volatile("v_sqrt_f32 %0, %0" : "+v"(sx[1]));
        else if constexpr (Z == 34) asm volatile("v_lshlrev_b32 %0, 16, %1" : "=v"(sx[2]) : "v"(sx[1]));
        else if constexpr (Z == 35) asm volatile("v_rcp_f32 %0, %1" : "=v"(sx[1]) : "v"(sx[2]));
        else if constexpr (Z == 36) asm volatile("v_mul_f32 %0, %0, %0" : "+v"(sx[2]));
        else if constexpr (Z == 37) asm volatile("v_mul_f32 %0, 0xbfb8aa3b, %0" : "+v"(sx[1]));
        else if constexpr (Z == 16) asm volatile("v_and_b32 %0, 31, %1" : "=v"(sx[4]) : "v"(threadIdx.x));
        else if constexpr (Z == 21) asm volatile("v_lshl_add_u32 %0, %0, 4, %1" : "+v"(sx[4]) : "s"(lds0 + STL + wid * 512));
        else if constexpr (Z == 38) *(LDS_AS f32x4*)(size_t)(__float_as_uint(sx[4])) = f32x4{sx[0], sx[1], sx[2], sx[3]};
    };
    static_assert((2 * (NK - 2) + 1 - (2 * STK + 5)) * SPU >= 39, "statistics in time");
    f32x4 ccv[NI], stv[MI];
    const unsigned cca = opaque(lds0 + RING + (wn * 64 + g * 4) * 4), cc6 = opaque(lds0 + RING + (48 + wn * 8 + (g & 1) * 4 + (g >> 1) * 112) * 4);
    const unsigned sta = opaque(lds0 + STL + wm * 1024 + (l15 & 1) * 256 + (l15 >> 1) * 16);
    const unsigned gof = opaque(((m0 + wm * TM) / 4 + (g & 1) * 4 + q) * (8 * F_) + (tn * 7 + wn * 4) * 128 + p4 * 32 + (g >> 1) * 16);
    const unsigned gofm = opaque(((m0 + wm * TM + wn * 32) / 4 + (lane >> 3)) * (8 * F_) + (tn * 7 + 3) * 128 + (lane & 7) * 16);
    const unsigned hsw = opaque(lds0 + MIX + (wm * TM + (g >> 1) * 16 + l15) * 32 + wn * 16 + (g & 1) * 8);
    const unsigned hsr = opaque(lds0 + MIX + wid * 1024 + lane * 16);
    auto eld = [&](auto oc) INL {
        constexpr int O = decltype(oc)::value;
        constexpr int SI = O == 0 ? 0 : (O == 3 ? 1 : (O == 7 ? 2 : (O == 8 ? 3 : -1)));
        constexpr int J = O < 3 ? O * 3 - 3 : (O < 6 ? O * 3 - 11 : (O == 6 ? 6 : O * 3 - 25));
        if constexpr (SI >= 0) stv[SI] = *(const LDS_AS f32x4*)(size_t)(sta + (SI >> 1) * 512 + (SI & 1) * 128);
        else if constexpr (J == 6) ccv[6] = *(const LDS_AS f32x4*)(size_t)(cc6);
        else ccv[J] = *(const LDS_AS f32x4*)(size_t)(cca + ((J / 3) * 112 + (J % 3) * 16) * 4);
    };
    auto unit = [&](auto ktc, auto kkc) INL {
        constexpr int KT = decltype(ktc)::value, KK = decltype(kkc)::value;
        constexpr int NKT = KK ? KT + 1 : KT;
        static_for<0, MI * NI>([&](auto tc) INL {
            constexpr int T = decltype(tc)::value, X = KK * MI * NI + T, j = X / 8, i = j & 1 ? MI - 1 - (X / 2) % MI : (X / 2) % MI;
            mma_ip(cat(bf[KT & 1][4 * j + 2 * (X & 1)], bf[KT & 1][4 * j + 2 * (X & 1) + 1]), af[KT & 1][2 * i + (X & 1)], acc[i][j]);
            SCHED();
            constexpr bool RQ = KT + 2 < NK && T < 4 * GGS;
            if constexpr (RQ && T % GGS == 0) prep(kkc, CT<T / GGS>{}, CT<KT + 2>{});
            if constexpr (RQ && T % GGS == 1) go(kkc, CT<T / GGS>{});
            constexpr int DX = T < 4 * GGS ? dreq((2 * (KT - NK + 2) + KK) * 4 + T / GGS) : -1;
            if constexpr (DX >= 0 && T % GGS == 0) dprep(CT<DX>{});
            if constexpr (DX >= 0 && T % GGS == 1) dgo(CT<DX>{});
            if constexpr (2 * (KT - NK + 2) + KK == TCU && T == TCT) touch1(SINK, wd, tof);
            constexpr int O = T - (T + GGS - 2) / GGS;
            if constexpr (NKT < NK && T % GGS != 1 && O < (KK ? 12 : 36 - GL2)) ldop(CT<NKT>{}, CT<(KK ? O : O + GL2)>{});
            if constexpr (NKT < NK && KK == 1 && T >= 20 && T < 8 + GL2) ldop(CT<NKT>{}, CT<T - 8>{});
            if constexpr (KT == STK && KK == 1 && T >= STT && T < STT + 4) gload16_devo<(T - STT) * 256>(stn[T - STT], spw, sto);
            constexpr int Z = (2 * KT + KK - 2 * STK - 5) * SPU + T - (MI * NI - SPU);
            if constexpr (Z >= 0 && Z < 39 && T >= MI * NI - SPU) stats(CT<Z>{});
            if constexpr (NKT == NK && T < 11) eld(tc);
            if constexpr (KK == 0 && T == PRT && KT < NK - 1) __builtin_amdgcn_s_setprio(0);
            SCHED();
        });
    };

    if (wid == 0) {
        const int c = (lane < BN / 4 ? lane : 0) * 4;
        const unsigned co = (unsigned)((c >= BN / 2 ? F_ + c - BN / 2 : c) + tn * (BN / 2)) * 4u;
        f32x4 cp[8];
#pragma unroll
        for (int i = 0; i < 8; ++i) gload16_dev(cp[i], cs + i * 2 * F_, co);
        wait_vm<0>();
#pragma unroll
        for (int i = 0; i < 8; ++i) {
            asm volatile("" : "+v"(cp[i]));
            if (i) cp[0] += cp[i];
        }
        if (lane < BN / 4) *(f32x4*)(csl + lane * 4) = cp[0];
    }
    if (wid == 1) {
        const int pid = id ^ 128;
        const int w = lane < 32 ? tm * 32 + lane : (lane < 48 ? dtm * 16 + dtn : ((pid / 64) * 4 + pid % 4) * 16 + (pid % 64) / 4);
#pragma unroll 1
        for (unsigned spins = 0; spins < (1u << 22); ++spins) {
            if (__all(__hip_atomic_load((gu64*)(flags + w * 32), __ATOMIC_RELAXED, __HIP_MEMORY_SCOPE_AGENT) == tok)) break;
            __builtin_amdgcn_s_sleep(1);
        }
    }
    BARRIER();
    stage_a(CT<0>{});
    wait_vm<0>();
    BARRIER();
    stage_a(CT<1>{});
    static_for<0, GL2>([&](auto oc) INL { ldop(CT<0>{}, oc); });
    static_for<0, NK>([&](auto kc) INL {
        constexpr int KT = decltype(kc)::value;
        unit(CT<KT>{}, CT<0>{});
        if constexpr (KT < NK - 1) {
            WAIT_LGKM0();
            wait_vm<(KT == STK + 1 ? 8 : (KT == NK - 2 ? DV : 4))>();
            BARRIER();
            if constexpr (PRT < 28) __builtin_amdgcn_s_setprio(2);
        }
        unit(CT<KT>{}, CT<1>{});
    });
    asm volatile("s_nop 15\n\ts_nop 7" ::: "memory");
    SCHED();
    const int tide = (int)opaque(threadIdx.x);
    {
        __builtin_amdgcn_s_setprio(0);
        auto center = [&](f32x4 a, f32x4 c, float nm) INL {
            float a0, a1, a2, a3;
            asm("v_fma_f32 %0, %4, %5, %9\n\tv_fma_f32 %1, %4, %6, %10\n\tv_fma_f32 %2, %4, %7, %11\n\tv_fma_f32 %3, %4, %8, %12"
                : "=&v"(a0), "=&v"(a1), "=&v"(a2), "=&v"(a3)
                : "v"(nm), "v"(c[0]), "v"(c[1]), "v"(c[2]), "v"(c[3]), "v"(a[0]), "v"(a[1]), "v"(a[2]), "v"(a[3]));
            return f32x4{a0, a1, a2, a3};
        };
        auto glu = [&](f32x4 gp, f32x4 up, float c, float k) INL {
            float t0, t1, t2, t3, u0 = up[0], u1 = up[1], u2 = up[2], u3 = up[3];
            asm("v_mul_f32 %0, %12, %8\n\tv_mul_f32 %1, %12, %9\n\tv_mul_f32 %2, %12, %10\n\tv_mul_f32 %3, %12, %11\n\t"
                "v_exp_f32 %0, %0\n\tv_exp_f32 %1, %1\n\tv_exp_f32 %2, %2\n\tv_exp_f32 %3, %3\n\t"
                "v_mul_f32 %4, %4, %8\n\tv_mul_f32 %5, %5, %9\n\t"
                "v_fma_f32 %0, %0, %13, %13\n\tv_fma_f32 %1, %1, %13, %13\n\t"
                "v_fma_f32 %2, %2, %13, %13\n\tv_fma_f32 %3, %3, %13, %13\n\t"
                "v_rcp_f32 %0, %0\n\tv_rcp_f32 %1, %1\n\tv_rcp_f32 %2, %2\n\tv_rcp_f32 %3, %3\n\t"
                "v_mul_f32 %6, %6, %10\n\tv_mul_f32 %7, %7, %11\n\t"
                "v_mul_f32 %4, %4, %0\n\tv_mul_f32 %5, %5, %1\n\tv_mul_f32 %6, %6, %2\n\tv_mul_f32 %7, %7, %3"
                : "=&v"(t0), "=&v"(t1), "=&v"(t2), "=&v"(t3), "+v"(u0), "+v"(u1), "+v"(u2), "+v"(u3)
                : "v"(gp[0]), "v"(gp[1]), "v"(gp[2]), "v"(gp[3]), "v"(c), "v"(k));
            return u32x2{pk2(u0, u1), pk2(u2, u3)};
        };
        auto centerp = [&](f32x4 a, f32x4 c, float nm) INL {
            const f32x2 n2 = {nm, nm};
            const f32x2 lo = __builtin_elementwise_fma(n2, c.xy, a.xy), hi = __builtin_elementwise_fma(n2, c.zw, a.zw);
            return f32x4{lo.x, lo.y, hi.x, hi.y};
        };
        auto glu2 = [&](f32x2 gp, f32x2 up, float c, float k) INL {
            const f32x2 ea = gp * f32x2{c, c}, k2 = {k, k};
            const f32x2 d = __builtin_elementwise_fma(f32x2{__builtin_amdgcn_exp2f(ea.x), __builtin_amdgcn_exp2f(ea.y)}, k2, k2);
            const f32x2 hv = (gp * up) * f32x2{__builtin_amdgcn_rcpf(d.x), __builtin_amdgcn_rcpf(d.y)};
            return pk2(hv.x, hv.y);
        };
        auto glup = [&](f32x4 gp, f32x4 up, float c, float k) INL {
            return u32x2{glu2(gp.xy, up.xy, c, k), glu2(gp.zw, up.zw, c, k)};
        };
        u32x2 hp[2];
        auto blk = [&](int i, int j, auto pkc) INL {
            if constexpr (decltype(pkc)::value)
                hp[i & 1] = glup(centerp(acc[i][j], ccv[j], stv[i][0]), centerp(acc[i][j + 3], ccv[j + 3], stv[i][0]), stv[i][1], stv[i][2]);
            else
                hp[i & 1] = glu(center(acc[i][j], ccv[j], stv[i][0]), center(acc[i][j + 3], ccv[j + 3], stv[i][0]), stv[i][1], stv[i][2]);
        };
        auto blk2 = [&](int i, int j) INL {
            f32x2 gp[4], up[4], ea[4];
#pragma unroll
            for (int x = 0; x < 4; ++x) {
                const f32x4 a = acc[i + (x >> 1)][j], c = ccv[j];
                const float nm = stv[i + (x >> 1)][0];
                gp[x] = __builtin_elementwise_fma(f32x2{nm, nm}, x & 1 ? c.zw : c.xy, x & 1 ? a.zw : a.xy);
            }
            SCHED();
#pragma unroll
            for (int x = 0; x < 4; ++x) { const float c = stv[i + (x >> 1)][1]; ea[x] = gp[x] * f32x2{c, c}; }
            SCHED();
#pragma unroll
            for (int x = 0; x < 4; ++x) {
                const f32x4 a = acc[i + (x >> 1)][j + 3], c = ccv[j + 3];
                const float nm = stv[i + (x >> 1)][0];
                ea[x] = f32x2{__builtin_amdgcn_exp2f(ea[x].x), __builtin_amdgcn_exp2f(ea[x].y)};
                up[x] = __builtin_elementwise_fma(f32x2{nm, nm}, x & 1 ? c.zw : c.xy, x & 1 ? a.zw : a.xy);
                SCHED();
            }
#pragma unroll
            for (int x = 0; x < 4; ++x) { const float k = stv[i + (x >> 1)][2]; ea[x] = __builtin_elementwise_fma(ea[x], f32x2{k, k}, f32x2{k, k}); }
            SCHED();
#pragma unroll
            for (int x = 0; x < 4; ++x) {
                ea[x] = f32x2{__builtin_amdgcn_rcpf(ea[x].x), __builtin_amdgcn_rcpf(ea[x].y)};
                up[x] = gp[x] * up[x];
                SCHED();
            }
#pragma unroll
            for (int x = 0; x < 4; ++x) up[x] = up[x] * ea[x];
            SCHED();
#pragma unroll
            for (int x = 0; x < 4; ++x) hp[x & 1][x >> 1] = pk2(up[(x & 1) * 2 + (x >> 1)].x, up[(x & 1) * 2 + (x >> 1)].y);
        };
        auto flush = [&](int hf, int j) INL {
            const auto t0 = __builtin_amdgcn_permlane16_swap(hp[0][0], hp[1][0], false, false);
            const auto t1 = __builtin_amdgcn_permlane16_swap(hp[0][1], hp[1][1], false, false);
            store16_wt(C, gof + hf * 8 * (8 * F_) + j * 128, u32x4{t0[0], t1[0], t0[1], t1[1]});
            SCHED();
        };
        auto head = [&](auto pkc) INL { blk(0, 0, pkc); blk(1, 0, pkc); flush(0, 0); blk(0, 1, pkc); };
        auto sync = [&]() INL {
            WAIT_LGKM0();
            BARRIER();
            asm volatile("" ::: "memory");
            SCHED();
        };
        const bool late = wid >= 4;
        if (!late) head(CT<0>{});
        sync();
#pragma unroll
        for (int i = 0; i < MI; ++i)
#pragma unroll
            for (int j = 0; j < NI; ++j) asm volatile("" : "+v"(acc[i][j]));
#pragma unroll
        for (int i = 0; i < MI; i += 2) {
            const f32x4 x = centerp(acc[i][6], ccv[6], stv[i][0]), y = centerp(acc[i + 1][6], ccv[6], stv[i + 1][0]);
            f32x4 gm, um;
#pragma unroll
            for (int r = 0; r < 4; ++r) {
                const auto t = __builtin_amdgcn_permlane32_swap(__float_as_uint(x[r]), __float_as_uint(y[r]), false, false);
                gm[r] = __uint_as_float(t[0]); um[r] = __uint_as_float(t[1]);
            }
            const bool hi = (tide & 32) != 0;
            *(LDS_AS u32x2*)(size_t)(hsw + i * 512) =
                glup(gm, um, hi ? stv[i + 1][1] : stv[i][1], hi ? stv[i + 1][2] : stv[i][2]);
        }
        u32x4 mv;
        auto mixed = [&]() INL {
            sync();
            mv = *(const LDS_AS u32x4*)(size_t)(hsr);
            SCHED();
        };
        if constexpr (MXP == 0) { mixed(); store16_wt(C, gofm, mv); }
        if (late) {
            __builtin_amdgcn_s_setprio(LPR);
            blk2(0, 0);
            flush(0, 0);
            blk2(0, 1);
        } else {
            blk(1, 1, CT<1>{});
        }
        flush(0, 1);
        if constexpr (MXP == 1) mixed();
        blk2(0, 2);
        if constexpr (MXP == 1) store16_wt(C, gofm, mv);
        flush(0, 2);
#pragma unroll
        for (int j = 0; j < 3; ++j) {
            blk2(2, j);
            flush(1, j);
        }
        __builtin_amdgcn_s_setprio(0);
        publish(flags + (512 + tm * 32 + tn) * 32, tok);
    }
}

constexpr long long W_OB = 12, W_H = 16, W_XA = 32, W_XB = 36, W_CS = 40, W_SPA = 42, W_SPB = 43, W_FL = 44, W_EP = 45, W_SL = 48, W_L1 = 48;
extern "C" __global__ __launch_bounds__(512, 2) void body_k(const u16* __restrict__ x0, const u16* __restrict__ wqkv,
                                                 const u16* __restrict__ wo, const u16* __restrict__ wg,
                                                 const u16* __restrict__ wu, const u16* __restrict__ wd, u16* out,
                                                 char* ws) {
    extern __shared__ __attribute__((aligned(16))) char smem[];
    {
        const int tid = threadIdx.x, lane = tid & 63, wid = __builtin_amdgcn_readfirstlane(tid >> 6);
        if (wid >= 2 && wid < 6) {
            const int id = (blockIdx.x & 7) * 32 + (blockIdx.x >> 3);
            const int tm = (id / 128) * 8 + id % 8, tn = (id % 128) / 8, h = wid - 2;
            const int row = h * 8 + (lane >> 3), hr = h * 4 + ((lane >> 3) & 3);
            const unsigned la = (unsigned)((tm * 128 + row) * D_ + ((lane & 7) ^ ((row >> 1) & 7)) * 8) * 2u;
            const unsigned lh = (unsigned)((tm ? tm * 128 - 16 + hr : 0) * D_ + ((lane & 7) ^ ((hr >> 1) & 7)) * 8) * 2u;
            const unsigned lb = b_off(h, lane, 3 * D_, tn * 64);
#pragma unroll 1
            for (int kt = 0; kt < 2; ++kt) {
#pragma unroll
                for (int i = 0; i < 4; ++i) glds16(uni((const char*)x0 + kt * 128 + i * 32 * D_ * 2), la, (2 - 2 * kt) * QAS + h * 1024 + i * 4096);
                glds16_mask(uni((const char*)x0 + kt * 128), lh, (2 - 2 * kt) * QAS + 16384 + h * 512, h == 0 ? 0xffffff00ull : 0xffffffffull);
                qkv_stage_b(0, wqkv, lb, h, kt, 2 - 2 * kt);
            }
        } else if (wid == 1) {
            code_touch(SINK, lane);
        }
    }
    u64 tok = 0;
#pragma unroll 1
    for (int L = 0; L < 2; ++L) {
        auto at = [&](long long mb, int l) INL { return sopq(ws) + ((mb + l * W_L1) << 20); };
        bf16x8 rr[4];
        auto fl = [&](int ph) INL { return (unsigned*)at(W_FL, 0) + 16384 + ph * 8192; };
        qkv_phase(smem, L ? (u16*)at(W_XB, 0) : x0, sopq(wqkv), (u16*)at(W_OB, L), (float*)at(W_CS, 0), (float*)at(W_SPB, 0), fl(4),
                  fl(7 - L), sopq(wo), L == 0, sopq(wg), sopq(wu), (u64*)at(W_EP, 0), tok, rr);
        wo_phase(smem, (u16*)at(W_OB, L), sopq(wo), (u16*)at(W_XA, L), rr, (float*)at(W_SPA, L), fl(7 - L),
                 fl(L), L == 0, sopq(wg), sopq(wu), tok);
        gu_phase(smem, (u16*)at(W_XA, L), sopq(wg), sopq(wu), (u16*)at(W_H, L), (float*)at(W_CS, 0), (float*)at(W_SPA, L), sopq(wd),
                 L == 0, fl(L), tok);
        dn_phase(smem, (u16*)at(W_H, L), sopq(wd), L ? out : (u16*)at(W_XB, 0), L ? nullptr : (float*)at(W_SPB, 0),
                 (float*)at(W_SL, L), fl(2 + L), fl(4 + L), L ? nullptr : sopq(wqkv), tok);
    }
    asm volatile(".Lbody_end:");
}

extern "C" void kernel_launch(void* const* d_in, const int* in_sizes, int n_in, void* d_out, int out_size,
                              void* d_ws, size_t ws_size, hipStream_t stream) {
    const u16* x0 = (const u16*)d_in[0];
    const u16* wqkv = (const u16*)d_in[1];
    const u16* wo = (const u16*)d_in[2];
    const u16* wg = (const u16*)d_in[4];
    const u16* wu = (const u16*)d_in[5];
    const u16* wd = (const u16*)d_in[6];
    constexpr int smem = 163840;
    (void)hipFuncSetAttribute((const void*)body_k, hipFuncAttributeMaxDynamicSharedMemorySize, smem);
    body_k<<<256, 512, smem, stream>>>(x0, wqkv, wo, wg, wu, wd, (u16*)d_out, (char*)d_ws);
}
```

```cpp
#include <hip/hip_runtime.h>
#include <hip/hip_bf16.h>
#include <stdint.h>

#pragma clang diagnostic ignored "-Winline-asm"
typedef unsigned short u16;
using bf16x8 = __attribute__((ext_vector_type(8))) short;
using s16x4  = __attribute__((ext_vector_type(4))) short;
using f32x4  = __attribute__((ext_vector_type(4))) float;
using f32x2  = __attribute__((ext_vector_type(2))) float;
using u32x2  = __attribute__((ext_vector_type(2))) unsigned;
using u32x4  = __attribute__((ext_vector_type(4))) unsigned;
typedef __bf16 bf2_t __attribute__((ext_vector_type(2)));
#define LDS_AS __attribute__((address_space(3)))
#define INL __attribute__((always_inline))

constexpr int TCU = -1, TCT = 25, STT = 22, STK = 10, MXP = 1, LPR = 1, SPU = 10, PRT = 20, WOLD = 12, WOBAR = 4,
              WVN = 2, GL2 = 16, QL1 = 16, QG1 = 10, GGS = 6, WV0 = 5, PV0 = 6, HPRIO = 2, SLP = 3, KIN = 9, KSKIP = 2,
              CSP = 1, QBT = 12, GBT = 6, GW2 = 0, QSO = 0, QSP = 2, QNY = 1, WN4 = 2, MKP = 0, WN1 = 3, WN2 = 2,
              WN3 = 3, NSOLO = 4, QPS = 0, XO = 5, XA = 28, XB = 8, DBAR = 4, DBAR2 = 4;
constexpr unsigned long long DSCH = 0x87654321a9000000ull;
constexpr int S_ = 2048, D_ = 1024, F_ = 3584;

__device__ __forceinline__ float bf2f(u16 v) { return __uint_as_float(((unsigned)v) << 16); }
__device__ __forceinline__ u16 f2bf(float f) {
    __hip_bfloat16 h = __float2bfloat16(f);
    return *reinterpret_cast<u16*>(&h);
}
__device__ __forceinline__ float rbf(float f) { return bf2f(f2bf(f)); }
__device__ __forceinline__ unsigned pk2(float a, float b) {
    const f32x2 v = {a, b};
    return __builtin_bit_cast(unsigned, __builtin_convertvector(v, bf2_t));
}
__device__ __forceinline__ float addf(float a, float b) {
    float r;
    asm("v_add_f32 %0, %1, %2" : "=v"(r) : "v"(a), "v"(b));
    return r;
}
__device__ __forceinline__ float mulf(float a, float b) {
    float r;
    asm("v_mul_f32 %0, %1, %2" : "=v"(r) : "v"(a), "v"(b));
    return r;
}
__device__ __forceinline__ float subf(float a, float b) {
    float r;
    asm("v_sub_f32 %0, %1, %2" : "=v"(r) : "v"(a), "v"(b));
    return r;
}

template <int V> struct CT { static constexpr int value = V; };
template <int I, int N, class F> __device__ __forceinline__ void static_for(F&& f) {
    if constexpr (I < N) { f(CT<I>{}); static_for<I + 1, N>(f); }
}

#define WAIT_LGKM0() asm volatile("s_waitcnt lgkmcnt(0)" ::: "memory")
#define BARRIER() do { asm volatile("" ::: "memory"); __builtin_amdgcn_s_barrier(); asm volatile("" ::: "memory"); } while (0)
#define SCHED() __builtin_amdgcn_sched_barrier(0)
template <int N> __device__ __forceinline__ void wait_vm() {
    static_assert(N >= 0 && N < 64, "vmcnt range");
    asm volatile("s_waitcnt vmcnt(%0)" :: "n"(N) : "memory");
}

__device__ __forceinline__ void glds16(const void* sbase, unsigned voff, unsigned lds_dst) {
    asm volatile("s_mov_b32 m0, %2\n\ts_nop 3\n\tglobal_load_lds_dwordx4 %0, %1" :: "v"(voff), "s"(sbase), "s"(__builtin_amdgcn_readfirstlane(lds_dst)) : "memory", "m0");
}
template <int NLANE>
__device__ __forceinline__ void glds16_part(const void* sbase, unsigned voff, unsigned lds_dst) {
    unsigned long long ex;
    asm volatile("s_mov_b64 %0, exec\n\ts_mov_b32 m0, %3\n\ts_mov_b64 exec, %4\n\ts_nop 1\n\t"
                 "global_load_lds_dwordx4 %1, %2 sc1\n\ts_mov_b64 exec, %0"
                 : "=&s"(ex) : "v"(voff), "s"(sbase), "s"(__builtin_amdgcn_readfirstlane(lds_dst)), "n"((1ull << NLANE) - 1) : "memory", "m0");
}
__device__ __forceinline__ void glds16_mask(const void* sbase, unsigned voff, unsigned lds_dst, unsigned long long mask) {
    unsigned long long ex;
    asm volatile("s_mov_b64 %0, exec\n\ts_mov_b32 m0, %3\n\ts_mov_b64 exec, %4\n\ts_nop 1\n\t"
                 "global_load_lds_dwordx4 %1, %2\n\ts_mov_b64 exec, %0"
                 : "=&s"(ex) : "v"(voff), "s"(sbase), "s"(__builtin_amdgcn_readfirstlane(lds_dst)), "s"(mask) : "memory", "m0");
}
__device__ __forceinline__ void glds16_dev(const void* sbase, unsigned voff, unsigned lds_dst) {
    asm volatile("s_mov_b32 m0, %2\n\ts_nop 3\n\tglobal_load_lds_dwordx4 %0, %1 sc1" :: "v"(voff), "s"(sbase), "s"(__builtin_amdgcn_readfirstlane(lds_dst)) : "memory", "m0");
}
template <int V> __device__ __forceinline__ void s_set(unsigned& d) { asm volatile("s_mov_b32 %0, %1" : "=s"(d) : "n"(V)); }
template <int V> __device__ __forceinline__ void m0_set(unsigned base) { asm volatile("s_add_i32 m0, %0, %1" :: "s"(base), "n"(V) : "m0", "scc"); }
template <bool DEV> __device__ __forceinline__ void blds16(__amdgpu_buffer_rsrc_t r, unsigned voff, unsigned soff) {
    if constexpr (DEV) asm volatile("buffer_load_dwordx4 %0, %1, %2 offen sc1 lds" :: "v"(voff), "s"(r), "s"(soff) : "memory");
    else asm volatile("buffer_load_dwordx4 %0, %1, %2 offen lds" :: "v"(voff), "s"(r), "s"(soff) : "memory");
}
__device__ __forceinline__ void gload16_dev(f32x4& d, const void* sbase, unsigned voff) {
    asm volatile("s_nop 4\n\tglobal_load_dwordx4 %0, %1, %2 sc1" : "=v"(d) : "v"(voff), "s"(sbase) : "memory");
}
__device__ __forceinline__ void gload8_dev(unsigned long long& d, const void* p) {
    asm volatile("global_load_dwordx2 %0, %1, off sc1" : "=v"(d) : "v"(p) : "memory");
}
template <int OFF, class T> __device__ __forceinline__ void gload16_devo(T& d, const void* sbase, unsigned voff) {
    asm volatile("global_load_dwordx4 %0, %1, %2 offset:%3 sc1" : "=v"(d) : "v"(voff), "s"(sbase), "n"(OFF) : "memory");
}
template <int OFF> __device__ __forceinline__ void gload16(f32x4& d, const void* sbase, unsigned voff) {
    asm volatile("s_nop 4\n\tglobal_load_dwordx4 %0, %1, %2 offset:%3" : "=v"(d) : "v"(voff), "s"(sbase), "n"(OFF) : "memory");
}
constexpr unsigned SINK = 163584;
__device__ __forceinline__ void touch1(unsigned sink, const void* sbase, unsigned voff) {
    asm volatile("s_mov_b32 m0, %2\n\ts_nop 3\n\tglobal_load_lds_dword %0, %1" :: "v"(voff), "s"(sbase), "s"(sink) : "memory", "m0");
}
__device__ __forceinline__ const char* uni(const char* p) {
    const unsigned long long v = (unsigned long long)p;
    const unsigned lo = __builtin_amdgcn_readfirstlane((unsigned)v), hi = __builtin_amdgcn_readfirstlane((unsigned)(v >> 32));
    return (const char*)(((unsigned long long)hi << 32) | lo);
}
__device__ __forceinline__ void store16_wt(void* base, unsigned off, u32x4 v) {
    const __amdgpu_buffer_rsrc_t r = __builtin_amdgcn_make_buffer_rsrc(base, 0, 0x7fffffff, 0x00020000);
    __builtin_amdgcn_raw_buffer_store_b128(v, r, off, 0, 16);
}
__device__ __forceinline__ unsigned opaque(unsigned v) { asm volatile("" : "+v"(v)); return v; }
__device__ __forceinline__ unsigned bid_() { unsigned b = blockIdx.x; asm volatile("" : "+s"(b)); return b; }
__device__ __forceinline__ int wg_id() { const unsigned b = bid_(); return (b & 7) * 32 + (b >> 3); }
template <class T> __device__ __forceinline__ T* sopq(T* p) { asm volatile("" : "+s"(p)); return p; }
__device__ __forceinline__ void code_touch(unsigned sink, unsigned lane) {
    const char* pc;
    int off;
    unsigned len;
    asm volatile("s_getpc_b64 %0\n.Lct%=:\n\ts_mov_b32 %1, body_k-.Lct%=\n\ts_mov_b32 %2, .Lbody_end-body_k"
                 : "=s"(pc), "=s"(off), "=s"(len));
    const unsigned o = ((bid_() >> 3) + lane * 32u) * 128u;
    touch1(sink, pc + off, o < len ? o : 0u);
}
template <int OFF> __device__ __forceinline__ bf16x8 lds_a(unsigned base) {
    static_assert(OFF >= 0 && OFF < 65536, "ds offset");
    return *(const LDS_AS bf16x8*)(size_t)(base + OFF);
}
template <int OFF> __device__ __forceinline__ s16x4 lds_h(unsigned base) {
    static_assert(OFF >= 0 && OFF < 65536, "ds offset");
    return __builtin_amdgcn_ds_read_tr16_b64_v4i16((LDS_AS s16x4*)(size_t)(base + OFF));
}
__device__ __forceinline__ bf16x8 cat(s16x4 lo, s16x4 hi) { return __builtin_shufflevector(lo, hi, 0, 1, 2, 3, 4, 5, 6, 7); }
__device__ __forceinline__ f32x4 mma(bf16x8 a, bf16x8 b, f32x4 c) {
    return __builtin_amdgcn_mfma_f32_16x16x32_bf16(a, b, c, 0, 0, 0);
}

__device__ __forceinline__ void mma_ip(bf16x8 a, bf16x8 b, f32x4& c) {
    asm volatile("v_mfma_f32_16x16x32_bf16 %0, %1, %2, %0" : "+v"(c) : "v"(a), "v"(b));
}

constexpr int CSTR = 64 * 2 + 16;
struct ResC { unsigned rd, co, so; };
__device__ __forceinline__ void res_setup(ResC& c, unsigned lds, int m0, int n0, int tid) {
    const int row = (tid >> 6) * 16 + ((tid >> 3) & 7), ch = tid & 7;
    c.rd = opaque(lds + row * CSTR + ch * 16);
    c.co = opaque((unsigned)((m0 + row) * D_ + n0 + ch * 8) * 2u);
    c.so = opaque((unsigned)(((m0 + row) >> 5) * 512 + (n0 >> 6) * 32 + (row & 31) + (ch >> 2) * 8) * 8u);
}
__device__ __forceinline__ void res_tail(const ResC& c, u16* C, float* sp) {
    const u32x4 v = *(const LDS_AS u32x4*)(size_t)c.rd, w = *(const LDS_AS u32x4*)(size_t)(c.rd + 8 * CSTR);
    const __amdgpu_buffer_rsrc_t rc = __builtin_amdgcn_make_buffer_rsrc(C, 0, 0x7fffffff, 0x00020000);
    __builtin_amdgcn_raw_buffer_store_b128(v, rc, c.co, 0, 16);
    __builtin_amdgcn_raw_buffer_store_b128(w, rc, c.co, 8 * D_ * 2, 16);
    SCHED();
    if (sp == nullptr) return;
    unsigned s0, q0, s1, q1;
    asm("v_dot2_f32_bf16 %0, %4, %12, 0\n\tv_dot2_f32_bf16 %1, %4, %4, 0\n\tv_dot2_f32_bf16 %0, %5, %12, %0\n\tv_dot2_f32_bf16 %1, %5, %5, %1\n\t"
        "v_dot2_f32_bf16 %0, %6, %12, %0\n\tv_dot2_f32_bf16 %1, %6, %6, %1\n\tv_dot2_f32_bf16 %0, %7, %12, %0\n\tv_dot2_f32_bf16 %1, %7, %7, %1\n\t"
        "v_dot2_f32_bf16 %2, %8, %12, 0\n\tv_dot2_f32_bf16 %3, %8, %8, 0\n\tv_dot2_f32_bf16 %2, %9, %12, %2\n\tv_dot2_f32_bf16 %3, %9, %9, %3\n\t"
        "v_dot2_f32_bf16 %2, %10, %12, %2\n\tv_dot2_f32_bf16 %3, %10, %10, %3\n\tv_dot2_f32_bf16 %2, %11, %12, %2\n\tv_dot2_f32_bf16 %3, %11, %11, %3\n\t"
        "v_add_f32_dpp %0, %0, %0 row_half_mirror row_mask:0xf bank_mask:0x5\n\tv_add_f32_dpp %1, %1, %1 row_half_mirror row_mask:0xf bank_mask:0x5\n\t"
        "v_add_f32_dpp %0, %2, %2 row_half_mirror row_mask:0xf bank_mask:0xa\n\tv_add_f32_dpp %1, %3, %3 row_half_mirror row_mask:0xf bank_mask:0xa\n\t"
        "s_nop 0\n\tv_add_f32_dpp %0, %0, %0 quad_perm:[1,0,3,2] row_mask:0xf bank_mask:0xf\n\t"
        "v_add_f32_dpp %1, %1, %1 quad_perm:[1,0,3,2] row_mask:0xf bank_mask:0xf\n\t"
        "s_nop 0\n\tv_add_f32_dpp %0, %0, %0 quad_perm:[2,3,0,1] row_mask:0xf bank_mask:0xf\n\t"
        "v_add_f32_dpp %1, %1, %1 quad_perm:[2,3,0,1] row_mask:0xf bank_mask:0xf"
        : "=&v"(s0), "=&v"(q0), "=&v"(s1), "=&v"(q1)
        : "v"(v[0]), "v"(v[1]), "v"(v[2]), "v"(v[3]), "v"(w[0]), "v"(w[1]), "v"(w[2]), "v"(w[3]), "s"(0x3f803f80u));
    const __amdgpu_buffer_rsrc_t r = __builtin_amdgcn_make_buffer_rsrc(sp, 0, 0x7fffffff, 0x00020000);
    __builtin_amdgcn_raw_buffer_store_b64(u32x2{s0, q0}, r, c.so, 0, 16);
}
__device__ __forceinline__ bf16x8 pick(bool on, int e) {
    const unsigned one = on ? 0x3f80u << (e & 1) * 16 : 0u;
    bf16x8 m = __builtin_bit_cast(bf16x8, u32x4{(e >> 1) == 0 ? one : 0u, (e >> 1) == 1 ? one : 0u, (e >> 1) == 2 ? one : 0u, (e >> 1) == 3 ? one : 0u});
    asm volatile("" : "+v"(m));
    return m;
}

constexpr int ATX = 155648;
__device__ __forceinline__ void att_put(char* smem, int lane, int m0) {
    const int l15 = lane & 15, g = lane >> 4;
    u32x4 cx, cy, c0, ps;
#pragma unroll
    for (int r = 0; r < 4; ++r) {
        const int e = l15 - g * 4 - r, eh = ((l15 - g * 4) & ~1) - (r & 1) * 2;
        cx[r] = (e & 1) == 0 && e < 0 ? 0u : 0xff800000u;
        cy[r] = (e & 1) == 0 && e >= 0 ? 0u : 0xff800000u;
        c0[r] = m0 ? cx[r] : 0xff800000u;
        const unsigned hs = r & 1 ? 0x0302u : 0x0100u;
        ps[r] = (eh < 0) == (r < 2) ? (l15 & 1 ? hs << 16 | 0x0c0cu : 0x0c0c0000u | hs) : 0x0c0c0c0cu;
    }
    u32x4* const p = (u32x4*)(smem + ATX) + lane;
    p[0] = cx; p[64] = cy; p[128] = c0; p[192] = ps;
}
using u32x8 = __attribute__((ext_vector_type(8))) unsigned;
#define AMM "v_mfma_f32_16x16x32_bf16 "
#define ACV "v_cvt_pk_bf16_f32 "
#define AX1(s) "v_max3_f32 v[" s "], v[" s "], v[" s "+1], v[" s "+4]\n\t"
#define AX2(s) "v_max3_f32 v[" s "+2], v[" s "+2], v[" s "+3], v[" s "+6]\n\t"
#define AX3(s) "v_max_f32 v[" s "], v[" s "], v[" s "+5]\n\t"
#define AX4(s) "v_max_f32 v[" s "+2], v[" s "+2], v[" s "+7]\n\t"
#define AOP(op, d, s) op " v" d ", v[" s "], v[" s "+2]\n\t"
#define AO2(op, d, e, a, b) op " v" d ", v" a ", v" b "\n\t" op " v" e ", v" a ", v" b "\n\t"
#define ASB(s, n) "v_sub_f32 v[" s "], v[" s "], v" n "\n\tv_sub_f32 v[" s "+2], v[" s "+2], v" n "\n\t"
#define AEX(s) "v_exp_f32 v[" s "], v[" s "]\n\t"
#define AMU(s, n) "v_mul_f32 v[" s "], v[" s "], v" n "\n\t"
#define APK(s) ACV "v[" s "+1], v[" s "], v[" s "+2]\n\t"
#define APM(s, i) "v_perm_b32 v[" s "+4+" i "], v[" s "+1], v[" s "+1], v[240+" i "]\n\t"
#define APV(o, v, s) AMM "v[" o ":" o "+3], v[" v ":" v "+3], v[" s "+4:" s "+7], 0\n\t"
#define AOC(s, o) ACV "v[" s "], v[" o "], v[" o "+1]\n\t" ACV "v[" s "+1], v[" o "+2], v[" o "+3]\n\t"
#define AST(s, off) "buffer_store_dwordx4 v[" s ":" s "+3], %[oo], %[rc], 0 offen offset:" off " sc1\n\t"
#define S16(a, b) "v_permlane16_swap_b32 v" a ", v" b "\n\t"
#define S32(a, b) "v_permlane32_swap_b32 v" a ", v" b "\n\t"
__device__ __forceinline__ void att_run(const u32x4 (&q)[4], const u32x4 (&k)[4], const f32x4 (&v)[3][2], const u32x2 (&v3)[2],
                                        const u32x4 (&am)[4], const f32x2 (&rk)[4], const f32x2 (&sc)[4], unsigned xa, unsigned oo, u16* C) {
    u32x4 kx;
    u32x8 s3, s2, w0, w1;
    asm volatile("ds_read_b128 %2, %3\n\tds_read_b64 v[164:165], %3 offset:1024\n\t"
                 AMM "v[228:231], %4, %6, %8\n\t" ACV "v166, %9, %10\n\t" ACV "v167, %11, %12\n\t" ACV "v168, %13, %14\n\t"
                 AMM "v[224:227], %5, %6, %7\n\t" ACV "v169, %15, %16\n\t" ACV "v170, %17, %18\n\t" ACV "v171, %19, %20"
                 : "=&{v[224:231]}"(s3), "=&{v[164:171]}"(w0), "=&v"(kx)
                 : "v"(xa), "v"(k[3]), "v"(k[2]), "v"(q[3]), "v"(am[0]), "v"(am[1]),
                   "v"(v[0][0][0]), "v"(v[0][0][1]), "v"(v[0][0][2]), "v"(v[0][0][3]), "v"(v[1][0][0]), "v"(v[1][0][1]),
                   "v"(v[1][0][2]), "v"(v[1][0][3]), "v"(v[2][0][0]), "v"(v[2][0][1]), "v"(v[2][0][2]), "v"(v[2][0][3]) : "memory");
    asm volatile("ds_read_b64 v[174:175], %2 offset:1032\n\t"
                 AMM "v[220:223], %3, %5, %7\n\t" ACV "v176, %8, %9\n\t" ACV "v177, %10, %11\n\t" ACV "v178, %12, %13\n\t"
                 AMM "v[216:219], %4, %5, %6\n\t" ACV "v179, %14, %15\n\t" ACV "v180, %16, %17\n\t" ACV "v181, %18, %19"
                 : "=&{v[216:223]}"(s2), "=&{v[174:181]}"(w1)
                 : "v"(xa), "v"(k[2]), "v"(k[1]), "v"(q[2]), "v"(am[0]), "v"(am[1]),
                   "v"(v[0][1][0]), "v"(v[0][1][1]), "v"(v[0][1][2]), "v"(v[0][1][3]), "v"(v[1][1][0]), "v"(v[1][1][1]),
                   "v"(v[1][1][2]), "v"(v[1][1][3]), "v"(v[2][1][0]), "v"(v[2][1][1]), "v"(v[2][1][2]), "v"(v[2][1][3]) : "memory");
    const __amdgpu_buffer_rsrc_t rc = __builtin_amdgcn_make_buffer_rsrc(C, 0, 0x7fffffff, 0x00020000);
    asm volatile(AMM "v[212:215], %[k1], %[q1], %[cy]\n\t" AX1("224") AX2("224") AX3("224")
                 AMM "v[208:211], %[k0], %[q1], %[cx]\n\t" AX4("224") AMU("224", "195") AMU("226", "199")
                 AMM "v[204:207], %[k0], %[q0], %[cy]\n\t" AOP("v_max_f32", "232", "224") AX1("216") AX2("216") "s_waitcnt lgkmcnt(0)\n\t"
                 AMM "v[200:203], %[kx], %[q0], %[c0]\n\t" AX3("216") AX4("216") AMU("216", "194") AMU("218", "198")
                 AOP("v_max_f32", "233", "216") AX1("208") AX2("208")
                 S16("232", "233") AX3("208") AO2("v_max_f32", "236", "237", "232", "233") AX4("208") AMU("208", "193")
                 S32("236", "237") AMU("210", "197") AO2("v_max_f32", "232", "233", "236", "237") AOP("v_max_f32", "234", "208") AX1("200")
                 S16("232", "233") AX2("200") AX3("200") AX4("200") ASB("224", "232") AMU("200", "192") AMU("202", "196")
                 AOP("v_max_f32", "235", "200") ASB("216", "233")
                 S16("234", "235") AEX("224") AEX("226") AO2("v_max_f32", "238", "239", "234", "235") AEX("216") AEX("218")
                 S32("238", "239") AOP("v_add_f32", "232", "224") AO2("v_max_f32", "234", "235", "238", "239")
                 AOP("v_add_f32", "233", "216") AMU("224", "187")
                 S16("234", "235") AMU("226", "191")
                 S16("232", "233") ASB("208", "234") AO2("v_add_f32", "236", "237", "232", "233") ASB("200", "235")
                 S32("236", "237") AEX("208") AEX("210") "v_add_f32 v232, v236, v237\n\t" AEX("200") "v_rcp_f32 v232, v232\n\t" AEX("202")
                 "v_mov_b32 v233, v232\n\t" AOP("v_add_f32", "234", "208") AOP("v_add_f32", "235", "200")
                 S16("232", "233") AMU("224", "232")
                 S16("234", "235") AMU("226", "232") AMU("216", "186") AO2("v_add_f32", "238", "239", "234", "235") AMU("218", "190") APK("224")
                 S32("238", "239") AMU("216", "233") "v_add_f32 v234, v238, v239\n\t" AMU("218", "233") "v_rcp_f32 v234, v234\n\t"
                 APM("224", "0") "v_mov_b32 v235, v234\n\t" APM("224", "1") APM("224", "2")
                 S16("234", "235") APM("224", "3") APK("216") AMU("208", "185")
                 APV("132", "170", "224") APM("216", "0") APM("216", "1") APM("216", "2")
                 APV("136", "180", "224") APM("216", "3") AMU("210", "189") AMU("208", "234")
                 APV("140", "168", "216") AMU("210", "234") AMU("200", "184") APK("208")
                 APV("144", "178", "216") APM("208", "0") APM("208", "1") APM("208", "2") APM("208", "3") AMU("202", "188") AMU("200", "235")
                 APV("148", "166", "208") AMU("202", "235") AOC("224", "132")
                 APV("152", "176", "208") APK("200") AOC("226", "136") AST("224", "3072")
                 APM("200", "0") APM("200", "1") APM("200", "2") APM("200", "3") AOC("216", "140")
                 APV("156", "164", "200") AOC("218", "144") AST("216", "2048")
                 APV("160", "174", "200") AOC("208", "148") AOC("210", "152") AST("208", "1024")
                 AOC("200", "156") "s_nop 0\n\t" AOC("202", "160") AST("200", "0") "s_nop 1"
                 : "+{v[224:231]}"(s3), "+{v[216:223]}"(s2)
                 : "{v[164:171]}"(w0), "{v[174:181]}"(w1), "{v[172:173]}"(v3[0]), "{v[182:183]}"(v3[1]),
                   [k1] "v"(k[1]), [k0] "v"(k[0]), [kx] "v"(kx), [q1] "v"(q[1]), [q0] "v"(q[0]),
                   [cx] "v"(am[0]), [cy] "v"(am[1]), [c0] "v"(am[2]), "{v[240:243]}"(am[3]),
                   "{v[184:185]}"(rk[0]), "{v[186:187]}"(rk[1]), "{v[188:189]}"(rk[2]), "{v[190:191]}"(rk[3]),
                   "{v[192:193]}"(sc[0]), "{v[194:195]}"(sc[1]), "{v[196:197]}"(sc[2]), "{v[198:199]}"(sc[3]), [oo] "v"(oo), [rc] "s"(rc)
                 : "memory", "v132", "v133", "v134", "v135", "v136", "v137", "v138", "v139", "v140", "v141", "v142", "v143", "v144", "v145",
                   "v146", "v147", "v148", "v149", "v150", "v151", "v152", "v153", "v154", "v155", "v156", "v157", "v158", "v159", "v160",
                   "v161", "v162", "v163", "v200", "v201", "v202", "v203", "v204", "v205", "v206", "v207", "v208", "v209", "v210",
                   "v211", "v212", "v213", "v214", "v215", "v232", "v233", "v234", "v235", "v236", "v237", "v238", "v239");
}

typedef unsigned long long u64;
typedef __attribute__((address_space(1))) u64 gu64;
__device__ __forceinline__ void publish(unsigned* f, u64 tok, int wid, unsigned sink = SINK) {
    asm volatile("s_cmp_eq_u32 %0, 0\n\ts_cselect_b64 exec, 1, 0\n\ts_waitcnt vmcnt(0)\n\ts_barrier\n\t"
                 "global_store_dwordx2 %1, %2, %3 sc1\n\ts_mov_b64 exec, -1" :: "s"(wid), "v"(0u), "v"(tok), "s"(f) : "memory", "scc");
    if (wid == 2) {
        code_touch(sink, opaque(threadIdx.x) & 63u);
        wait_vm<0>();
    }
}
template <class M> __device__ __forceinline__ void await(M& have, M need, const unsigned* f, u64 tok) {
    if (__builtin_expect((have & need) != need, 0)) {
#pragma unroll 1
        for (unsigned spins = 0; spins < (1u << 22); ++spins) {
            have = (M)__ballot(__hip_atomic_load((gu64*)f, __ATOMIC_RELAXED, __HIP_MEMORY_SCOPE_AGENT) == tok);
            if ((have & need) == need) break;
            __builtin_amdgcn_s_sleep(1);
        }
        asm volatile("" ::: "memory");
    }
}
__device__ __forceinline__ unsigned gu_bsrc(int o, int tn) {
    const int rem = (o >> 4) % (64 * 14);
    const int rowp = rem / 14, chn = rem % 14;
    const int kr = rowp ^ (4 * ((rowp >> 3) & 1));
    return (unsigned)(kr * F_ + tn * 112 + chn * 8) * 2u;
}
template <bool PERM = false>
__device__ __forceinline__ unsigned b_off(int h, int lane, int ld, int col) {
    const int row = h * 8 + (lane >> 3), cp = lane & 7;
    const int jj = (cp >> 1) ^ (((row >> 1) & 1) | ((row >> 2) & 2));
    return (unsigned)((PERM ? (lane >> 5) * 16 + h * 4 + (row & 3) : row) * ld + col + jj * 16 + (cp & 1) * 8) * 2u;
}

constexpr int WOB = 65536, WONB = 12;
constexpr unsigned BOXO = 36u << 20;
__device__ __forceinline__ int wo_k(int i) { return (int)(0xb3fedca987654210ull >> (4 * i)) & 15; }
__device__ __forceinline__ unsigned wo_boff(int h, int lane) {
    return b_off<true>(h, lane, D_, (((wg_id()) % 64) / 4) * 64);
}
__device__ __forceinline__ void wo_stage_b(unsigned lds0, const u16* B, unsigned boff, int h, int step) {
#pragma unroll
    for (int i = 0; i < 2; ++i)
        glds16(uni((const char*)B + (wo_k(step) * 64 + i * 32) * D_ * 2), boff,
               lds0 + WOB + (step < WONB ? step : step - WONB) * 8192 + h * 1024 + i * 4096);
}
__device__ __forceinline__ void wo_phase(char* smem, const u16* A, const u16* __restrict__ B, u16* C, float* sp,
                                         const unsigned* pflags, unsigned* oflags, bool first, const u16* __restrict__ wg,
                                         const u16* __restrict__ wu, u64 tok) {
    constexpr int NK = 16, ASZ = 16384;
    const int tid = (int)opaque(threadIdx.x), lane = tid & 63;
    const int wid = __builtin_amdgcn_readfirstlane(tid >> 6);
    const int id = wg_id();
    const int tm = (id / 64) * 4 + id % 4, tn = (id % 64) / 4;
    const int m0 = tm * 128, n0 = tn * 64;
    const unsigned lds0 = __builtin_amdgcn_readfirstlane((unsigned)(size_t)(LDS_AS char*)smem);
    ResC rc;
    unsigned goff[7] = {0u, 0u, 0u, 0u, 0u, 0u, 0u};
    auto stage_g = [&](int i, int kt) INL {
        const int p = wid - 4 + 4 * i;
        glds16(uni((const char*)(p >= 14 ? wu : wg) + kt * 64 * F_ * 2), goff[i], lds0 + 98304 + kt * 28672 + p * 1024);
    };
    if (wid >= 4) {
        const int h = wid - 4;
        const unsigned aoff = (unsigned)(m0 * D_ * 2 + (h & 1) * 4096 + (h >> 1) * 1024 + lane * 16);
        const unsigned boff = wo_boff(h, lane);
        const unsigned* const f = pflags + (tm * 16 + (lane & 15)) * 32;
        unsigned have = 0u;

        auto stage = [&](int st, auto bc) INL {
            await(have, 1u << wo_k(st), f, tok);
#pragma unroll
            for (int i = 0; i < 4; ++i)
                glds16_dev(uni((const char*)A + wo_k(st) * 8192 + (i >> 1) * 64 * D_ * 2 + (i & 1) * 2048), aoff,
                           lds0 + ((st + 1) & 3) * ASZ + h * 1024 + i * 4096);
            if constexpr (decltype(bc)::value) wo_stage_b(lds0, B, boff, h, st);
        };
        stage(0, CT<0>{});
        stage(1, CT<0>{});
#pragma unroll
        for (int i = 0; i < 7; ++i) goff[i] = opaque(gu_bsrc((h + 4 * i) * 1024 + (int)opaque(lane) * 16, id >> 3));
        wait_vm<4>();
        BARRIER();
        stage(2, CT<0>{});
        stage(3, CT<0>{});
#pragma unroll 1
        for (int st = 4; st < WONB; ++st) {
            wait_vm<8>();
            BARRIER();
            stage(st, CT<0>{});
        }
#pragma unroll 1
        for (int st = WONB; st < NK; ++st) {
            wait_vm<8>();
            BARRIER();
            stage(st, CT<1>{});
        }
        wait_vm<12>();
        BARRIER();
        stage_g(0, 0); stage_g(1, 0); stage_g(2, 0);
        wait_vm<9>();
        BARRIER();
        stage_g(3, 0); stage_g(4, 0);
        wait_vm<5>();
        BARRIER();
        stage_g(5, 0); stage_g(6, 0);
        res_setup(rc, lds0 + ASZ, m0, n0, tid);

    } else {
        const int wm = wid >> 1, wn = wid & 1;
        const int l15 = lane & 15, g = lane >> 4, q = l15 >> 2, p4 = l15 & 3;
        const unsigned ab = opaque(lds0 + wm * 8192 + lane * 16);
        unsigned bb[4];
#pragma unroll
        for (int x = 0; x < 4; ++x)
            bb[x] = opaque(lds0 + WOB + (g * 8 + q + ((x & 1) ^ wm) * 4) * 128 + p4 * 8 + (((wn * 2 + x / 2) ^ ((q >> 1) | ((g & 1) << 1))) << 5));
        f32x4 acc[4][2];
        {
            const unsigned ra = lds0 + (wm * 64 + l15) * 128 + (((wn * 4 + g) ^ (l15 >> 1)) * 16);
            const bf16x8 sel[2] = {pick(g == (l15 >> 3), l15 & 7), pick(g == 2 + (l15 >> 3), l15 & 7)};
            static_for<0, 4>([&](auto ic) INL {
                constexpr int i = decltype(ic)::value;
                const bf16x8 rr = lds_a<i * 2048>(ra);
#pragma unroll
                for (int j = 0; j < 2; ++j) {
                    acc[i][j] = mma(sel[j], rr, f32x4{0.f, 0.f, 0.f, 0.f});
                    asm volatile("" : "+v"(acc[i][j]));
                }
            });
        }
        res_setup(rc, lds0 + ASZ, m0, n0, tid);
        const unsigned cw = opaque(lds0 + ASZ + (wm * 64 + l15) * CSTR + (wn * 32 + g * 4) * 2);
        auto ldop = [&](bf16x8 (&af)[8], s16x4 (&bf)[8], auto ktc, auto oc) INL {
            constexpr int KT = decltype(ktc)::value, O = decltype(oc)::value;
            if constexpr (O == 2 || (O >= 5 && O < 12)) {
                constexpr int X = O == 2 ? 0 : O - 4;
                af[(X & 1) * 4 + X / 2] = lds_a<((KT + 1) % 4) * ASZ + X * 1024>(ab);
            } else {
                constexpr int X = O < 2 ? O : (O < 5 ? O - 1 : O - 8), J = X / 4;
                bf[X] = lds_h<(KT % 4) * 8192 + ((X >> 1) & 1) * 4096>(bb[J * 2 + (X & 1)]);
            }
        };
        auto ktile = [&](bf16x8 (&af)[8], s16x4 (&bf)[8], bf16x8 (&an)[8], s16x4 (&bn)[8], auto nktc) INL {
            static_for<0, 16>([&](auto tc) INL {
                constexpr int T = decltype(tc)::value, j = T / 8, i = j ? 3 - (T / 2) % 4 : (T / 2) % 4, kk = T & 1;
                if constexpr (T == WOBAR && decltype(nktc)::value < NK) {
                    WAIT_LGKM0();
                    BARRIER();
                }
                mma_ip(cat(bf[j * 4 + kk * 2], bf[j * 4 + kk * 2 + 1]), af[kk * 4 + i], acc[i][j]);
                SCHED();
                if constexpr (decltype(nktc)::value < NK && T >= WOBAR) {
                    constexpr int U = T - WOBAR, lo_ = (U * 16 + WOLD - 1) / WOLD, hi_ = ((U + 1) * 16 + WOLD - 1) / WOLD;
                    static_for<lo_, (hi_ < 16 ? hi_ : 16)>([&](auto oc) INL { ldop(an, bn, nktc, oc); });
                }
                SCHED();
            });
        };
        if (first && wid == 0) {
            const int l = lane < 12 ? lane : 0, cb = (id >> 3) * 7 / 4 + l % 3;
            const int job = ((cb < 56 ? cb : 55) + (l >= 6 ? 56 : 0)) * 2 + (l / 3 & 1), oid = job + 8 * ((job + 32) / 56);
            unsigned have = 0u;
            await(have, ~0u, pflags + (((oid / 128) * 8 + oid % 8) * 16 + (oid % 128) / 8) * 32, tok);
        }
        bf16x8 a0[8], a1[8];
        s16x4 b0[8], b1[8];
        BARRIER();
        static_for<0, 16>([&](auto oc) INL { ldop(a0, b0, CT<0>{}, oc); });
#pragma unroll 1
        for (int it = 0; it < 3; ++it) {
            ktile(a0, b0, a1, b1, CT<1>{});
            ktile(a1, b1, a0, b0, CT<2>{});
            ktile(a0, b0, a1, b1, CT<3>{});
            const unsigned d = it == 2 ? -65536 : 32768;
            bb[0] += d; bb[1] += d; bb[2] += d; bb[3] += d;
            ktile(a1, b1, a0, b0, CT<4>{});
        }
        ktile(a0, b0, a1, b1, CT<13>{});
        ktile(a1, b1, a0, b0, CT<14>{});
        ktile(a0, b0, a1, b1, CT<15>{});
        ktile(a1, b1, a0, b0, CT<16>{});
#pragma unroll
        for (int t = 0; t < 8; ++t) {
            const int j = t / 4, i = j ? 7 - t : t;
            *(LDS_AS u32x2*)(size_t)(cw + i * 16 * CSTR + j * 32) = u32x2{pk2(acc[i][j][0], acc[i][j][1]), pk2(acc[i][j][2], acc[i][j][3])};
            SCHED();
        }
    }
    WAIT_LGKM0();
    BARRIER();
    res_tail(rc, C, sp);
    publish(oflags + (tm * 16 + tn) * 32, tok, wid);
    if (wid >= 4) {
#pragma unroll
        for (int i = 0; i < 7; ++i) stage_g(i, 1);
    }
}

constexpr int QAS = 144 * 128, QBS = 192 * 128, QBR = 57344;
constexpr int QSK = 3 * QAS, QCS = 159744, QGR = QAS, QXC = 32768;
constexpr int qsl(int kt) { return (kt + 2) % 3; }
__device__ __forceinline__ int qkv_k(int i) { return (int)(0xb3a2fedc98765410ull >> (4 * i)) & 15; }
__device__ __forceinline__ unsigned qkv_boff(int h, int lane) {
    return b_off(h, lane, 3 * D_, (((wg_id()) % 128) / 8) * 64);
}
__device__ __forceinline__ void qkv_stage_b(unsigned lds0, const u16* B, unsigned boff, int h, int kt, int sl) {
#pragma unroll
    for (int i = 0; i < 6; ++i)
        glds16(uni((const char*)B + ((kt * 64 + (i & 1) * 32) * 3 * D_ + (i >> 1) * D_) * 2), boff,
               lds0 + QBR + sl * QBS + h * 1024 + i * 4096);
}
__device__ __forceinline__ void qkv_phase(char* smem, const u16* A, const u16* __restrict__ B, u16* C, float* cs,
                                          const float* sp, const unsigned* pflags, unsigned* oflags,
                                          const u16* __restrict__ wo, bool first, const u16* __restrict__ wg,
                                          const u16* __restrict__ wu, u64* epoch, u64& tok) {
    constexpr int NK = 16;
    const int tid = (int)opaque(threadIdx.x), lane = tid & 63;
    const int wid = __builtin_amdgcn_readfirstlane(tid >> 6);
    const int id = wg_id();
    const int tm = (id / 128) * 8 + id % 8, tn = (id % 128) / 8;
    const int m0 = tm * 128;
    const unsigned lds0 = __builtin_amdgcn_readfirstlane((unsigned)(size_t)(LDS_AS char*)smem);
    float* const csl = (float*)(smem + QCS);
    float* const cso = cs + 16 * F_ + id * 192;
    auto token = [&]() INL {
        asm volatile("s_load_dwordx2 %0, %1, 0x0\n\ts_waitcnt lgkmcnt(0)" : "=s"(tok) : "s"(epoch) : "memory");
        tok += 0x9e3779b97f4a7c15ull;
    };
    if (wid >= 4) {
        const int h = wid - 4, hl = tid - 256;
        const int row = h * 8 + (lane >> 3), hr = h * 4 + ((lane >> 3) & 3);
        const unsigned la = (unsigned)((m0 + row) * D_ + ((lane & 7) ^ ((row >> 1) & 7)) * 8) * 2u;
        const unsigned lh = (unsigned)((tm ? m0 - 16 + hr : 0) * D_ + ((lane & 7) ^ ((hr >> 1) & 7)) * 8) * 2u;
        const unsigned lb = qkv_boff(h, lane);
        const unsigned wb = wo_boff(h, lane);
        constexpr auto wn = [](int i) { return i == 0 ? 2 : (i == 1 ? WN1 : (i == 2 ? WN2 : (i == 3 ? WN3 : (i == 4 ? WN4 : 10 - WN1 - WN2 - WN3 - WN4)))); };
        static_assert(WN1 + WN2 <= 6 && WN1 + WN2 + WN3 <= 9, "free slots");
        auto wo_pre = [&](int i) INL {
            int e0 = 0;
#pragma unroll
            for (int x = 0; x < i; ++x) e0 += wn(x);
#pragma unroll
            for (int e = e0; e < e0 + wn(i); ++e) wo_stage_b(lds0, wo, wb, h, (int)(0xb765a9843210ull >> (4 * e)) & 15);
        };
        const int wtile = ((id / 64) * 4 + id % 4) * 16 + (id % 64) / 4;
        auto wo_res = [&]() INL {
#pragma unroll
            for (int i = 0; i < 4; ++i)
                glds16_dev(uni((const char*)A + (((wtile >> 4) - tm) * 128 + i * 32) * D_ * 2 + (wtile & 15) * 128), la, lds0 + h * 1024 + i * 4096);
        };
        auto marks = [&]() INL {
            const unsigned em = opaque(~0u), bo = BOXO + bid_() * 16384u + (unsigned)hl * 16u;
            const __amdgpu_buffer_rsrc_t r = __builtin_amdgcn_make_buffer_rsrc(C, 0, 0x7fffffff, 0x00020000);
#pragma unroll
            for (int i = 0; i < 4; ++i) __builtin_amdgcn_raw_buffer_store_b128(u32x4{em, em, em, em}, r, bo, i * 4096, 16);
        };
        auto put_stat = [&](float sum, float sq, int row, bool on) INL {
            const float mean = mulf(sum, 1.f / D_);
            const float var = fmaxf(subf(mulf(sq, 1.f / D_), mulf(mean, mean)), 0.f);
            const float rs = __builtin_amdgcn_rcpf(rbf(__builtin_amdgcn_sqrtf(rbf(addf(rbf(var), 1e-5f))))), nm = -rbf(mean);
            if (on) {
                *(float*)(smem + QSK + row * 4) = nm;
                *(float*)(smem + QSK + 576 + row * 4) = rs * (0.17677669529663687f * 1.44269504088896341f);
                *(float*)(smem + QSK + 1152 + row * 4) = rs;
            }
        };
        if (first) {
            const unsigned long long hmask = h == 0 ? 0xffffff00ull : 0xffffffffull;
            auto piece = [&](int kt, int p) INL {
                const unsigned da = lds0 + qsl(kt) * QAS;
                if (p < 4) glds16(uni((const char*)A + kt * 128 + p * 32 * D_ * 2), la, da + h * 1024 + p * 4096);
                else if (p == 4) glds16_mask(uni((const char*)A + kt * 128), lh, da + 16384 + h * 512, hmask);
                else glds16(uni((const char*)B + ((kt * 64 + ((p - 5) & 1) * 32) * 3 * D_ + ((p - 5) >> 1) * D_) * 2), lb,
                            lds0 + QBR + qsl(kt) * QBS + h * 1024 + (p - 5) * 4096);
            };
            __builtin_amdgcn_s_setprio(HPRIO);
            constexpr unsigned tw = SINK;
            const unsigned one2 = opaque(0x3f803f80u);
            if (hl < 24) *(u32x4*)(smem + (hl >> 3) * QAS + 128 * 128 + (hl & 7) * 16) = u32x4{one2, one2, one2, one2};
            const bool idle = (tn & 7) == 3;
            const int job = idle ? 0 : id - 8 * ((id + 32) >> 6);
            const int cb = job >> 1, kh = job & 1;
            const char* const wp = uni((const char*)((cb < 56 ? wg : wu) + (long long)kh * 512 * F_ + (cb % 56) * 64));
            const unsigned wpo = (unsigned)((idle ? 0 : hl >> 3) * F_ + (hl & 7) * 8) * 2u;
            const int wst = idle ? 0 : 64 * F_;
            f32x4 wv[NK];
            float sm[8];
#pragma unroll
            for (int e = 0; e < 8; ++e) sm[e] = 0.f;
            auto wadd1 = [&](int r, int e) INL {
                float t0, t1;
                asm volatile("v_lshlrev_b32 %2, 16, %4\n\tv_and_b32 %3, 0xffff0000, %4\n\tv_add_f32 %0, %0, %2\n\tv_add_f32 %1, %1, %3"
                             : "+v"(sm[2 * e]), "+v"(sm[2 * e + 1]), "=&v"(t0), "=&v"(t1) : "v"(wv[r][e]));
            };
            auto wadd = [&](int r) INL {
#pragma unroll
                for (int e = 0; e < 4; ++e) wadd1(r, e);
            };
            const unsigned bx = bid_();
            const int kq = bx >> 4;
            auto sweep = [&]() INL {
                touch1(tw, B, (unsigned)(bx * 192 + (hl < 192 ? hl : 0)) * 128u);
                touch1(tw, A, (unsigned)(((bx & 15) * 128 + (hl & 127)) * 2048 + kq * 128));
            };
            if (kq < KIN && kq >= KSKIP) {
                if (h >= 2) {
                    for (int i = (kq - KSKIP + 1) * SLP; i > 0; --i) __builtin_amdgcn_s_sleep(1);
                    touch1(tw, B, (unsigned)(bx * 192 + (hl & 127)) * 128u);
                    touch1(tw, A, (unsigned)(((bx & 15) * 128 + (hl & 127)) * 2048 + kq * 128));
                    if (h == 2) touch1(tw, B, (unsigned)(bx * 192 + hl) * 128u);
                }
            }
            token();
            static_for<0, NK>([&](auto kc) INL {
                constexpr int KT = decltype(kc)::value;
                constexpr auto ex = [](int k) { return (k >= WV0 && k < WV0 + 16 / WVN ? WVN : 0) + (k == PV0 ? 1 : 0) + (k == NK - 2 ? 8 : 0); };
                wait_vm<(KT + 1 < NK ? 11 : 0) + ex(KT - 2) + ex(KT - 1)>();
                BARRIER();
                static_for<0, 11>([&](auto pc) INL {
                    constexpr int P = decltype(pc)::value;
                    if constexpr (KT + 2 < NK) piece(KT + 2, P);
                    if constexpr (KT - 3 >= WV0 && KT - 3 < WV0 + 16 / WVN && P < 8) {
#pragma unroll
                        for (int u = 0; u < WVN / 2; ++u) wadd1((KT - 3 - WV0) * WVN + u * 2 + P / 4, P % 4);
                    }
                    SCHED();
                });
                if constexpr (KT + KIN < NK) { if (kq == KT + KIN) sweep(); }
                if constexpr (KT >= WV0 && KT < WV0 + 16 / WVN) {
#pragma unroll
                    for (int u = 0; u < WVN; ++u) gload16<0>(wv[(KT - WV0) * WVN + u], uni(wp + ((KT - WV0) * WVN + u) * wst), wpo);
                }
                if constexpr (KT == PV0) touch1(tw, wo, (unsigned)(bx * 64 + (hl & 63)) * 128u);
                if constexpr (KT == NK - 2) { wo_res(); wo_pre(0); }
                if constexpr (KT == 7) { if (h == 0) att_put(smem, lane, m0); }
                if constexpr (KT == 4) {
                    if (h == 0) touch1(tw, oflags - 9 * 8192, ((lane < 10 ? lane : 0) * 256 + bx) * 128u);
                }
                WAIT_LGKM0();
            });
            auto colsum = [&]() INL {
#pragma unroll
            for (int r = NK - 3 > WV0 ? (NK - 3 - WV0) * WVN : 0; r < NK; ++r) wadd(r);
            asm volatile("s_nop 1\n\tv_add_f32_dpp %0, %0, %0 row_ror:8 row_mask:0xf bank_mask:0xf\n\tv_add_f32_dpp %1, %1, %1 row_ror:8 row_mask:0xf bank_mask:0xf\n\t"
                         "v_add_f32_dpp %2, %2, %2 row_ror:8 row_mask:0xf bank_mask:0xf\n\tv_add_f32_dpp %3, %3, %3 row_ror:8 row_mask:0xf bank_mask:0xf\n\t"
                         "v_add_f32_dpp %4, %4, %4 row_ror:8 row_mask:0xf bank_mask:0xf\n\tv_add_f32_dpp %5, %5, %5 row_ror:8 row_mask:0xf bank_mask:0xf\n\t"
                         "v_add_f32_dpp %6, %6, %6 row_ror:8 row_mask:0xf bank_mask:0xf\n\tv_add_f32_dpp %7, %7, %7 row_ror:8 row_mask:0xf bank_mask:0xf\n\t"
                         "s_nop 1\n\tv_permlane16_swap_b32 %0, %4\n\tv_permlane16_swap_b32 %1, %5\n\tv_permlane16_swap_b32 %2, %6\n\t"
                         "v_permlane16_swap_b32 %3, %7\n\tv_add_f32 %0, %0, %4\n\tv_add_f32 %1, %1, %5\n\tv_add_f32 %2, %2, %6\n\t"
                         "v_add_f32 %3, %3, %7\n\ts_nop 1\n\tv_permlane32_swap_b32 %0, %2\n\tv_permlane32_swap_b32 %1, %3\n\t"
                         "v_add_f32 %0, %0, %2\n\tv_add_f32 %1, %1, %3"
                         : "+v"(sm[0]), "+v"(sm[1]), "+v"(sm[2]), "+v"(sm[3]), "+v"(sm[4]), "+v"(sm[5]), "+v"(sm[6]), "+v"(sm[7]));
            if ((lane & 8) == 0 && !idle) {
                const __amdgpu_buffer_rsrc_t r = __builtin_amdgcn_make_buffer_rsrc(cs, 0, 0x7fffffff, 0x00020000);
                __builtin_amdgcn_raw_buffer_store_b64(u32x2{__float_as_uint(sm[0]), __float_as_uint(sm[1])}, r,
                    (unsigned)((kh * 4 + h) * 2 * F_ + cb * 64 + (lane & 7) * 8 + ((lane >> 4) & 1) * 4 + (lane >> 5) * 2) * 4u, 0, 16);
            }
            };
            if constexpr (CSP == 1) { if constexpr (WV0 > 5) wait_vm<(WV0 > 7 ? 0 : 8)>(); colsum(); }
            wo_pre(1);
            BARRIER();
            {
                const int l = lane & 15, gi = h * 3 + (lane < 48 ? lane >> 4 : 0), mr = gi * 15 + l - 1;
                const float* const raw = (const float*)(smem + QGR + gi * 1024);
                put_stat(raw[l * 4], raw[((l >> 2) * 16 + l) * 4 + (l & 3)], l == 0 ? 0 : (gi < 9 ? 16 + (mr < 128 ? mr : 127) : l), gi < 10);
            }
            __builtin_amdgcn_s_setprio(0);
            if constexpr (GW2 == 0) wo_pre(2);
            WAIT_LGKM0();
            BARRIER();
            if constexpr (GW2 == 1) wo_pre(2);
            if (hl < 48) store16_wt(cso, (unsigned)hl * 16u, *(const u32x4*)(csl + hl * 4));
            wo_pre(3);
            if constexpr (CSP == 2) { wait_vm<2 * (WN1 + WN2 + WN3)>(); colsum(); }
            if constexpr (MKP == 0) marks();
            BARRIER();
            if constexpr (CSP == 3) { wait_vm<2 * (WN1 + WN2 + WN3) + (MKP == 0 ? 4 : 0)>(); colsum(); }
        } else {
            const unsigned* const f = pflags + (lane < 32 ? (tm && (lane & 16) ? tm - 1 : tm) * 16 + (lane & 15) : wtile) * 32;
            u64 have = 0;
            auto stage = [&](int st, int sl, auto bc) INL {
                const int kt = qkv_k(st);
                await(have, 0x10001ull << kt, f, tok);
                const unsigned da = lds0 + sl * QAS;
#pragma unroll
                for (int i = 0; i < 4; ++i) glds16_dev(uni((const char*)A + kt * 128 + i * 32 * D_ * 2), la, da + h * 1024 + i * 4096);
                glds16_part<32>(uni((const char*)A + kt * 128), lh, da + 16384 + h * 512);
                if constexpr (decltype(bc)::value) qkv_stage_b(lds0, B, lb, h, kt, sl);
            };
            {
                u64 lk;
                gload8_dev(lk, f);
                qkv_stage_b(lds0, B, lb, h, qkv_k(2), qsl(2));
                wait_vm<6>();
                asm volatile("" : "+v"(lk));
                have = __ballot(lk == tok);
            }
            stage(0, qsl(0), CT<0>{});
            stage(1, qsl(1), CT<0>{});
            wait_vm<5>();
            BARRIER();
            stage(2, qsl(2), CT<0>{});
            if (h == 0) att_put(smem, lane, m0);
            wait_vm<5>();
            BARRIER();
            stage(3, qsl(3), CT<1>{});
            if (h == 0) glds16_dev(uni((const char*)cso), lane * 16u, lds0 + QCS);
            int sl = qsl(4);
#pragma unroll 1
            for (int st = 4; st < NK; ++st) {
                wait_vm<11>();
                BARRIER();
                stage(st, sl, CT<1>{});
                sl = sl == 2 ? 0 : sl + 1;
            }
            f32x4 spv[8];
            await(have, 1ull << 32, f, tok);
            {
                const int srow = m0 - 16 + (hl < 144 ? hl & ~1 : 0);
                const int sr = srow < 0 ? 0 : srow;
                const unsigned so = (unsigned)((sr >> 5) * 512 + (hl & 1) * 256 + (sr & 31)) * 8u;
                gload16_dev(spv[0], sp, so);
                static_for<1, 8>([&](auto ic) INL { gload16_devo<decltype(ic)::value * 256>(spv[decltype(ic)::value], sp, so); });
            }
            wait_vm<19>();
            BARRIER();
            wo_res();
            wo_pre(0);
            wait_vm<16>();
            BARRIER();
            if constexpr (QSP) __builtin_amdgcn_s_setprio(QSP);
            wait_vm<8>();
            float sm[4];
#pragma unroll
            for (int i = 0; i < 8; ++i) {
                asm volatile("" : "+v"(spv[i]));
#pragma unroll
                for (int e = 0; e < 4; ++e) sm[e] = i ? addf(sm[e], spv[i][e]) : spv[i][e];
            }
#pragma unroll
            for (int e = 0; e < 4; ++e)
                sm[e] = addf(sm[e], __uint_as_float(__builtin_amdgcn_update_dpp(0u, __float_as_uint(sm[e]), 0xB1, 0xf, 0xf, false)));
            put_stat(hl & 1 ? sm[2] : sm[0], hl & 1 ? sm[3] : sm[1], hl, hl < 144);
            if constexpr (QSP) __builtin_amdgcn_s_setprio(0);
            if constexpr (QSO == 0) wo_pre(1);
            WAIT_LGKM0();
            BARRIER();
            if constexpr (QSO == 1) wo_pre(1); else wo_pre(2);
            BARRIER();
            if constexpr (QSO == 1) wo_pre(2);
            wo_pre(3);
            if constexpr (MKP == 0) marks();
            BARRIER();
        }
        wo_pre(4);
        wait_vm<(QNY ? 2 * wn(4) : 0)>();
        BARRIER();
        if constexpr (MKP == 1) marks();
        wo_pre(5);
        if constexpr (MKP == 2) marks();
    } else {
        const int wm = wid >> 1, wn = wid & 1;
        const int l15 = lane & 15, g = lane >> 4, q = l15 >> 2, p4 = l15 & 3;
        unsigned ab[2], hb[2], bj[6];
#pragma unroll
        for (int kk = 0; kk < 2; ++kk) {
            const unsigned sw = ((kk * 4 + g) ^ (l15 >> 1)) * 16;
            ab[kk] = opaque(lds0 + (wm * 64 + l15) * 128 + sw);
            hb[kk] = opaque(lds0 + (128 + l15) * 128 + sw);
        }
#pragma unroll
        for (int j = 0; j < 6; ++j)
            bj[j] = opaque(lds0 + QBR + (j >> 1) * 8192 + (g * 8 + q) * 128 + p4 * 8 + (((wn * 2 + ((j & 1) ^ wm)) ^ ((q >> 1) | ((g & 1) << 1))) << 5));
        f32x4 acc[5][6];
        constexpr auto qcol = [](int c) { return c < 3 ? 2 * c + 1 : 2 * c - 6; };
        auto ldop = [&](bf16x8 (&af)[10], s16x4 (&bf)[24], auto ktc, auto oc) INL {
            constexpr int KT = decltype(ktc)::value, O = decltype(oc)::value;
            if constexpr (O == 2 || (O >= 5 && O < 12)) {
                constexpr int X = O == 2 ? 0 : O - 4;
                af[X] = lds_a<qsl(KT) * QAS + (X / 2) * 2048>(ab[X & 1]);
            } else if constexpr (O == 24 || O == 25) {
                af[O - 16] = lds_a<qsl(KT) * QAS>(hb[O & 1]);
            } else {
                constexpr int X = O < 2 ? O : (O < 5 ? O - 1 : (O < 24 ? O - 8 : O - 10));
                bf[X] = lds_h<qsl(KT) * QBS + ((X >> 1) & 1) * 4096 + (X & 1) * 512>(bj[qcol(X / 4)]);
            }
        };
        unsigned ha[3][2];
        f32x4 rg[3];
        bf16x8 gf[2];
        auto hrow = [&](int i, int l) INL {
            const int gi = wid * 3 + i, mr = gi * 15 + l - 1;
            return l == 0 ? 128 : (gi < 9 ? (mr < 128 ? mr : 127) : 128 + l);
        };
        unsigned ea[10], ma[2], xw[2], xa, oo, l16, p4_, g16, cwa, cwb;
        f32x4 vnm[5], ccv[4];
        f32x2 rnm[3], rsc[2], rk[4];
        float ccw[2];
        u32x4 am[4];
        constexpr int NEA = 15;
        auto eadr = [&](auto tc) INL {
            constexpr int T = decltype(tc)::value;
            if constexpr (T == 0) asm volatile("v_and_b32 %0, 63, %1\n\tv_lshlrev_b32 %0, 4, %0" : "=v"(l16) : "v"(threadIdx.x));
            if constexpr (T == 1) { p4_ = opaque(l16 >> 2 & 60u); ma[0] = opaque(l16 + lds0 + ATX); }
            if constexpr (T == 2) ma[1] = opaque(ma[0] + (1 - wm) * 2048);
            if constexpr (T == 3) { asm volatile("v_and_b32 %0, 48, %1" : "=v"(g16) : "v"(threadIdx.x)); ea[0] = opaque(p4_ + lds0 + QSK); ea[1] = opaque(g16 + lds0 + QSK); }
            if constexpr (T == 4) { ea[2] = opaque(ea[0] + wm * 256); ea[3] = opaque(ea[1] + wm * 256); }
            if constexpr (T == 5) ea[4] = opaque(g16 + lds0 + QCS + (wn * 2 + wm) * 64);
            if constexpr (T == 6) { ea[6] = opaque(ea[4] ^ 64u); ea[5] = opaque(ea[4] - g16 + p4_); }
            if constexpr (T == 7) { ea[7] = opaque(ea[5] ^ 64u); ea[9] = opaque((p4_ & 56u) - g16); }
            if constexpr (T == 8) ea[8] = opaque(ea[2] - ((ea[9] & 63u) | -1216u));
            if constexpr (T == 9) ea[9] = opaque(ea[2] - (((ea[9] - 8u) & 63u) | -1216u));
            if constexpr (T == 10) xa = opaque(l16 + lds0 + QXC + wn * 4096);
            if constexpr (T == 11) xw[1] = opaque(xa + (wm * 4 + 1) * 2048);
            if constexpr (T == 12) { xw[0] = opaque(xa + wm * 8); xa = opaque(opaque(xa) + wm * 2048); }
            if constexpr (T == 13) oo = opaque(l16 + (tm * 32 + tn) * 8192);
            if constexpr (T == 14) oo = opaque(oo + (wm * 32 + wn) * 4096);
            if constexpr (T == 15) cwa = opaque(p4_ == 0 ? ea[4] : xa);
            if constexpr (T == 16) cwb = opaque(g16 == 0 ? ea[5] : xa);
        };
        auto ld2 = [&](unsigned a) INL { return f32x2{*(const LDS_AS float*)(size_t)a, *(const LDS_AS float*)(size_t)(a + 64)}; };
        auto eld = [&](auto oc) INL {
            constexpr int O = decltype(oc)::value;
            if constexpr (O < 4) am[O] = *(const LDS_AS u32x4*)(size_t)(O == 2 ? ma[1] : ma[0] + O * 1024);
            else if constexpr (O == 5 || O == 7) ccw[O / 2 - 2] = *(const LDS_AS float*)(size_t)(ea[O] + 512);
            else if constexpr (O < 8) ccv[O / 2] = *(const LDS_AS f32x4*)(size_t)(ea[O] + 256);
            else if constexpr (O < 10) ccv[O - 8] = *(const LDS_AS f32x4*)(size_t)(ea[2 * O - 12]);
            else if constexpr (O == 10) rnm[2].x = *(const LDS_AS float*)(size_t)ea[0];
            else if constexpr (O == 11) vnm[4] = *(const LDS_AS f32x4*)(size_t)ea[1];
            else if constexpr (O == 12 || O == 15) rnm[O == 12] = ld2(ea[2] + 64 + (O == 12) * 128);
            else if constexpr (O < 18) { constexpr int I = O < 15 ? 16 - O : 17 - O; vnm[I] = *(const LDS_AS f32x4*)(size_t)(ea[3] + (I + 1) * 64); }
            else if constexpr (O < 20) rsc[O - 18] = ld2(ea[2] + 576 + 64 + (O - 18) * 128);
            else rk[O - 20] = ld2(ea[8 + (O - 20) / 2] + (O & 1) * 128);
        };
        auto unit = [&](bf16x8 (&af)[10], s16x4 (&bf)[24], bf16x8 (&an)[10], s16x4 (&bn)[24], auto ktc, auto hc, auto fic) INL {
            constexpr bool FI = decltype(fic)::value;
            constexpr int KT = decltype(ktc)::value, HF = decltype(hc)::value;
            static_for<0, 26>([&](auto tc) INL {
                constexpr int T = decltype(tc)::value, X = HF * 26 + T, kk = X & 1;
                constexpr int c = X < 32 ? X / 8 : (X < 42 ? 4 : 5), p = (X < 32 ? X % 8 : (X < 42 ? X - 32 : X - 42)) / 2;
                constexpr int i = c == 5 ? 4 - p : (c & 1 ? 3 - p : p);
                if constexpr (qcol(c) >= 4) mma_ip(af[i * 2 + kk], cat(bf[c * 4 + kk * 2], bf[c * 4 + kk * 2 + 1]), acc[i][qcol(c)]);
                else mma_ip(cat(bf[c * 4 + kk * 2], bf[c * 4 + kk * 2 + 1]), af[i * 2 + kk], acc[i][qcol(c)]);
                SCHED();
                if constexpr (HF == 1) {
                    if constexpr (KT + 1 == NK && FI) {
                        if constexpr (T == GBT - 4) *(LDS_AS f32x4*)(size_t)(cwa + 2048) = rg[2];
                        else if constexpr (T == GBT) { WAIT_LGKM0(); BARRIER(); }
                        else if constexpr (T < 17) eadr(CT<T + 2 - (T > GBT - 4) - (T > GBT)>{});
                        else if constexpr (T < 21) eld(CT<T - 17>{});
                        else if constexpr (T == 21) *(LDS_AS f32x4*)(size_t)cwa = acc[4][0];
                        else if constexpr (T == 22) *(LDS_AS f32x4*)(size_t)(cwa + 256) = acc[4][2];
                        else if constexpr (T == 24) *(LDS_AS float*)(size_t)(cwb + 512) = acc[4][4][0];
                    }
                    if constexpr (KT + 1 == NK && !FI) {
                        static_assert(QBT >= 10, "order");
                        constexpr int EN = 26 - QBT < 14 ? 26 - QBT : 14;
                        if constexpr (T < 10) eld(tc);
                        if constexpr (T == QBT - 1) BARRIER();
                        if constexpr (T >= QBT)
                            static_for<(T - QBT) * 14 / EN, ((T + 1 - QBT) * 14 / EN < 14 ? (T + 1 - QBT) * 14 / EN : 14)>([&](auto oc) INL { eld(CT<10 + decltype(oc)::value>{}); });
                    }
                    if constexpr (KT + 1 < NK && T < QG1) ldop(an, bn, CT<KT + 1>{}, tc);
                    if constexpr (KT + 1 < NK && T >= QL1 && T < QL1 + 16 - QG1) ldop(an, bn, CT<KT + 1>{}, CT<T - QL1 + QG1>{});
                } else if constexpr (FI) {
                    if constexpr (T % 8 < 2 && T < 24) gf[T & 1] = lds_a<qsl(KT) * QAS>(ha[T / 8][T & 1]);
                    else if constexpr (T < 20) ldop(af, bf, ktc, CT<14 + T>{});
                    else if constexpr (KT + 1 == NK) {
                        if constexpr (T == 20) eadr(CT<0>{});
                        if constexpr (T == 25) eadr(CT<1>{});
                        if constexpr (T == 21) cwa = opaque(l16 + lds0 + QGR + wid * 3072);
                        if constexpr (T == 22 || T == 24) *(LDS_AS f32x4*)(size_t)(cwa + (T - 22) * 512) = rg[T / 2 - 11];
                    }
                } else {
                    if constexpr (T < 18) ldop(af, bf, ktc, CT<16 + T>{});
                }
                SCHED();
                if constexpr (FI && HF == 0 && T % 8 == 7) {
                    mma_ip(gf[0], gf[0], rg[T / 8]);
                    SCHED();
                    if constexpr (T < 16) ldop(af, bf, ktc, CT<16 + T - 1>{});
                    SCHED();
                    mma_ip(gf[1], gf[1], rg[T / 8]);
                    SCHED();
                    if constexpr (T < 16) ldop(af, bf, ktc, CT<16 + T>{});
                    SCHED();
                }
                if constexpr (FI && X == 31) {
                    mma_ip(cat(bf[12], bf[13]), af[8], acc[4][0]);
                    SCHED();
                    mma_ip(cat(bf[14], bf[15]), af[9], acc[4][0]);
                    SCHED();
                }
            });
        };
        if (first) token();
        if (!first && id == 0 && wid == 3) {
#pragma unroll 1
            for (unsigned spins = 0; spins < (1u << 22); ++spins) {
                bool ok = true;
#pragma unroll
                for (int i = 0; i < 4; ++i)
                    ok &= __hip_atomic_load((gu64*)(oflags + 8192 + (i * 64 + lane) * 32), __ATOMIC_RELAXED, __HIP_MEMORY_SCOPE_AGENT) == tok;
                if (__all(ok)) break;
            }
            if (lane == 0) __hip_atomic_store((gu64*)epoch, tok, __ATOMIC_RELAXED, __HIP_MEMORY_SCOPE_AGENT);
        }
        bf16x8 a0[10], a1[10], a2[10];
        s16x4 b0[24], b1[24], b2[24];
        auto loop = [&](auto fic) INL {
#pragma unroll
        for (int i = 0; i < 5; ++i)
#pragma unroll
            for (int j = 0; j < 6; ++j)
                if (i < 4 || j == 2 || j == 4) {
                    acc[i][j] = f32x4{0.f, 0.f, 0.f, 0.f};
                    asm volatile("" : "+v"(acc[i][j]));
                }
            if constexpr (decltype(fic)::value) wait_vm<11>();
            BARRIER();
            static_for<0, 16>([&](auto oc) INL { ldop(a0, b0, CT<0>{}, oc); });
            auto ktile = [&](bf16x8 (&af)[10], s16x4 (&bf)[24], bf16x8 (&an)[10], s16x4 (&bn)[24], auto kc) INL {
                unit(af, bf, an, bn, kc, CT<0>{}, fic);
                WAIT_LGKM0();
                if constexpr (decltype(kc)::value == 0 && decltype(fic)::value) wait_vm<0>();
                BARRIER();
                unit(af, bf, an, bn, kc, CT<1>{}, fic);
            };
#pragma unroll 1
            for (int it = 0; it < 5; ++it) {
                ktile(a0, b0, a1, b1, CT<0>{});
                ktile(a1, b1, a2, b2, CT<1>{});
                ktile(a2, b2, a0, b0, CT<2>{});
            }
            unit(a0, b0, a1, b1, CT<NK - 1>{}, CT<0>{}, fic);
            unit(a0, b0, a1, b1, CT<NK - 1>{}, CT<1>{}, fic);
        };
        if (first) {
#pragma unroll
            for (int i = 0; i < 3; ++i) {
                const int hr = hrow(i, l15);
#pragma unroll
                for (int kk = 0; kk < 2; ++kk) ha[i][kk] = opaque(lds0 + hr * 128 + (((kk * 4 + g) ^ ((hr >> 1) & 7)) * 16));
                rg[i] = f32x4{0.f, 0.f, 0.f, 0.f};
                asm volatile("" : "+v"(rg[i]));
            }
            acc[4][0] = f32x4{0.f, 0.f, 0.f, 0.f};
            asm volatile("" : "+v"(acc[4][0]));
            loop(CT<1>{});
            WAIT_LGKM0();
            BARRIER();
            static_for<0, 20>([&](auto oc) INL {
                constexpr int O = decltype(oc)::value;
                eld(CT<(O < 10 ? (int)(0x98d7c6b5a4ull >> (4 * O)) & 15 : O + 4)>{});
                SCHED();
            });
        } else {
            static_for<0, NEA>([&](auto tc) INL { eadr(tc); });
            loop(CT<0>{});
            BARRIER();
        }
        SCHED();
        auto ln2 = [&](int i, int j0, int j1) INL {
            f32x2 y[4];
#pragma unroll
            for (int x = 0; x < 4; ++x) {
                const int j = x < 2 ? j0 : j1;
                const f32x2 a = x & 1 ? acc[i][j].zw : acc[i][j].xy;
                if (j < 4) y[x] = __builtin_elementwise_fma(i & 1 ? rnm[i / 2].yy : rnm[i / 2].xx, x & 1 ? ccv[j].zw : ccv[j].xy, a);
                else y[x] = __builtin_elementwise_fma(x & 1 ? vnm[i].zw : vnm[i].xy, f32x2{ccw[j - 4], ccw[j - 4]}, a);
            }
            SCHED();
            acc[i][j0] = f32x4{y[0].x, y[0].y, y[1].x, y[1].y};
            acc[i][j1] = f32x4{y[2].x, y[2].y, y[3].x, y[3].y};
        };
        auto rnd = [&](f32x4 a, f32x4 b) INL {
            u32x4 r = {pk2(a[0], a[1]), pk2(a[2], a[3]), pk2(b[0], b[1]), pk2(b[2], b[3])};
            asm volatile("" : "+v"(r));
            SCHED();
            return r;
        };
        u32x4 qf[4], kf[4];
        u32x2 v3[2];
        ln2(4, 2, 4);
        {
            const u32x4 hv = rnd(acc[4][2], acc[4][4]);
            *(LDS_AS u32x2*)(size_t)(xw[0]) = hv.xy;
            *(LDS_AS u32x2*)(size_t)(xw[0] + 1024) = hv.zw;
        }
        ln2(3, 2, 3);
        kf[3] = rnd(acc[3][2], acc[3][3]);
        *(LDS_AS u32x2*)(size_t)(xw[1] + 8) = kf[3].xy;
        *(LDS_AS u32x2*)(size_t)(xw[1]) = kf[3].zw;
        ln2(3, 4, 5);
        v3[0] = u32x2{pk2(acc[3][4][0], acc[3][4][1]), pk2(acc[3][4][2], acc[3][4][3])};
        v3[1] = u32x2{pk2(acc[3][5][0], acc[3][5][1]), pk2(acc[3][5][2], acc[3][5][3])};
        *(LDS_AS u32x2*)(size_t)(xw[1] + 1032) = v3[0];
        *(LDS_AS u32x2*)(size_t)(xw[1] + 1024) = v3[1];
        SCHED();
        f32x4 vf[3][2];
        f32x2 sc[4];
#pragma unroll
        for (int i = 3; i >= 0; --i) {
            ln2(i, 0, 1);
            qf[i] = rnd(acc[i][0], acc[i][1]);
            if (i < 3) {
                ln2(i, 2, 3);
                kf[i] = rnd(acc[i][2], acc[i][3]);
                ln2(i, 4, 5);
                vf[i][0] = acc[i][4];
                vf[i][1] = acc[i][5];
                asm volatile("" : "+v"(vf[i][0]), "+v"(vf[i][1]));
            }
        }
#pragma unroll
        for (int i = 0; i < 4; ++i) {
            sc[i] = rk[i] * rsc[i & 1];
            asm volatile("" : "+v"(sc[i]));
        }
        WAIT_LGKM0();
        BARRIER();
        SCHED();
        att_run(qf, kf, vf, v3, am, rk, sc, xa, oo, C);
        SCHED();
        publish(oflags + (tm * 16 + tn) * 32, tok, wid, 65280);
    }
}

__device__ __forceinline__ unsigned dn_boff(int h, int lane) { return b_off(h, lane, D_, (bid_() >> 5) * 128); }
__device__ __forceinline__ void dn_stage_b(unsigned lds0, const u16* B, unsigned boff, int kh, int h, int i, int kg, int sl) {
    glds16(uni((const char*)B + (long long)(kg * 64 + (i & 1) * 32) * D_ * 2 + ((i >> 1) ^ kh) * 128), boff,
           lds0 + h * 1024 + sl * 16384 + i * 4096);
}
constexpr int DNA = 65536, DNR = 98304;
#define BOX_LOOK(r) "global_load_dwordx4 v[" r ":" r "+3], %14, %15 sc1\n\tglobal_load_dwordx4 v[" r "+4:" r "+7], %14, %15 offset:1024 sc1\n\t" \
    "global_load_dwordx4 v[" r "+8:" r "+11], %14, %15 offset:2048 sc1\n\tglobal_load_dwordx4 v[" r "+12:" r "+15], %14, %15 offset:3072 sc1\n\t"
#define BOX_MAX(r, i) "v_max3_u32 %12, %12, v[" r "+" i "], v[" r "+" i "+1]\n\t"
#define BOX_TEST(r) "s_waitcnt vmcnt(4)\n\tv_max_u32 %12, v[" r "], v[" r "+1]\n\t" BOX_MAX(r, "2") BOX_MAX(r, "4") BOX_MAX(r, "6") \
    BOX_MAX(r, "8") BOX_MAX(r, "10") BOX_MAX(r, "12") BOX_MAX(r, "14") "v_cmp_eq_u32 vcc, -1, %12\n\t"
#define BOX_MOV(r, i) "v_mov_b64 v[200+" i ":201+" i "], v[" r "+" i ":" r "+1+" i "]\n\t"
#define BOX_TAKE(r) BOX_MOV(r, "0") BOX_MOV(r, "2") BOX_MOV(r, "4") BOX_MOV(r, "6") BOX_MOV(r, "8") BOX_MOV(r, "10") BOX_MOV(r, "12") BOX_MOV(r, "14")
__device__ __forceinline__ void box_wait(u32x4 (&a)[4], u32x4 (&b)[4], u32x4 (&c)[4], const void* box, unsigned off) {
    unsigned t, n;
    asm volatile(BOX_TEST("200") "s_cbranch_vccz 3f\n\ts_mov_b32 %13, 0x100000\n"
                 "1:\n\t" BOX_LOOK("232") BOX_TEST("216") "s_cbranch_vccz 2f\n\t" BOX_LOOK("216") BOX_TEST("232")
                 "s_cbranch_vccz 4f\n\ts_sub_u32 %13, %13, 1\n\ts_cbranch_scc0 1b\n"
                 "4:\n\t" BOX_TAKE("232") "s_branch 5f\n"
                 "2:\n\t" BOX_TAKE("216") "\n5:\n\ts_nop 1\n3:"
                 : "+{v[200:203]}"(a[0]), "+{v[204:207]}"(a[1]), "+{v[208:211]}"(a[2]), "+{v[212:215]}"(a[3]),
                   "+{v[216:219]}"(b[0]), "+{v[220:223]}"(b[1]), "+{v[224:227]}"(b[2]), "+{v[228:231]}"(b[3]),
                   "=&{v[232:235]}"(c[0]), "=&{v[236:239]}"(c[1]), "=&{v[240:243]}"(c[2]), "=&{v[244:247]}"(c[3]), "=&v"(t), "=&s"(n)
                 : "v"(off), "s"(box) : "memory", "vcc", "scc");
}
__device__ __forceinline__ void dn_phase(char* smem, const u16* A, const u16* __restrict__ B, u16* C, float* sp,
                                         float* slab, const unsigned* gflags, unsigned* oflags,
                                         const u16* pf, u64 tok) {
    constexpr int BM = 128, NS = 4, NSO = NSOLO, NSH = (F_ / 64 - NSO) / 2, NK = NSH + NSO;
    static_assert(NSO % 2 == 0, "k-tile program");
    constexpr int NRT = (NK - 1 < NSH ? NK - 1 : NSH) / NS * NS;
    constexpr int ASZ = BM * 128, SLOT = 2 * ASZ;
    constexpr int lda = F_;
    constexpr int BOX = 16384;

    const int tid = (int)opaque(threadIdx.x), lane = tid & 63;
    const int wid = __builtin_amdgcn_readfirstlane(tid >> 6);
    const unsigned bx = bid_();
    const int xcd = bx & 7, r_ = bx >> 3;
    const int kh = xcd >> 2, tm = (xcd & 3) * 4 + (r_ & 3), tn = r_ >> 2;
    const int m0 = tm * BM, n0 = tn * 128 + kh * 64;
    const unsigned lds0 = __builtin_amdgcn_readfirstlane((unsigned)(size_t)(LDS_AS char*)smem);
    ResC rc;
    u32x4 pb[4], pc[4];
    if (wid >= 4) {
        const int h = wid - 4;
        const unsigned aoff = (unsigned)((m0 / 4 + h * 2 + (lane >> 5)) * (8 * F_) + (((lane >> 3) & 3) ^ (h & 1)) * 128 + (lane & 7) * 16);
        const unsigned boff = dn_boff(h, lane);
        const unsigned* const f = gflags + ((tm >> 1) * 32 + (lane & 31)) * 32;
        unsigned have = 0u;
        auto stage = [&](int kg, int sl, auto nbc) INL {
            await(have, (1u << (kg * 4 / 7)) | (1u << ((kg * 64 + 63) / 112)), f, tok);
#pragma unroll
            for (int i = 0; i < 4; ++i)
                glds16_dev(uni((const char*)A + kg * 512 + i * 32 * lda * 2), aoff, lds0 + DNA + h * 1024 + sl * ASZ + i * 4096);
#pragma unroll
            for (int i = 0; i < decltype(nbc)::value; ++i) dn_stage_b(lds0, B, boff, kh, h, i, kg, sl);
        };
        const int kg0 = kh * NSH;
        constexpr int NB0 = 0;
        stage(kg0, 0, CT<NB0>{});
        stage(kg0 + 1, 1, CT<NB0>{});
        wait_vm<4 + NB0>();
        BARRIER();
        stage(kg0 + 2, 2, CT<NB0>{});
        stage(kg0 + 3, 3, CT<NB0>{});
        wait_vm<8 + 2 * NB0>();
        BARRIER();
        stage(kg0 + 4, 0, CT<4>{});
        wait_vm<12 + NB0>();
        BARRIER();
        stage(kg0 + 5, 1, CT<4>{});
#pragma unroll 1
        for (int kt = 2; kt < NSH - NS; ++kt) {
            wait_vm<16>();
            BARRIER();
            stage(kg0 + kt + NS, kt & 3, CT<4>{});
        }
        static_for<NSH - NS, NK - 1>([&](auto kc) INL {
            constexpr int KT = decltype(kc)::value;
            constexpr int P2 = KT + 2 < NSH ? 8 : (KT + 2 < NK ? 6 : 0), P3 = KT + 3 < NSH ? 8 : (KT + 3 < NK ? 6 : 0);
            wait_vm<P2 + P3>();
            BARRIER();
            if constexpr (KT + NS < NK) stage(NSH + KT + NS, KT & 3, CT<2>{});
        });
        static_assert(NK % NS == 2, "dead slots");
        if (pf != nullptr) {
            for (int i = QPS; i > 0; --i) __builtin_amdgcn_s_sleep(1);
            qkv_stage_b(lds0, pf, qkv_boff(h, lane), h, qkv_k(0), qsl(0));
            qkv_stage_b(lds0, pf, qkv_boff(h, lane), h, qkv_k(1), qsl(1));
        }
        res_setup(rc, lds0 + NS * SLOT, m0, n0, tid);
        if (pf != nullptr && (bx & 1) == 0 && wid >= 6) {
            const unsigned ix = (bx >> 3) * 96 + (tid < 480 ? tid - 384 : 0);
            touch1(SINK, pf, ix * 2048 + ((bx & 3) * 4 + 3) * 128);
        }
#pragma unroll
        for (int i = 0; i < 4; ++i) asm volatile("" : "=v"(pb[i]), "=v"(pc[i]));
    } else {
        const int wm = wid >> 1, wn = wid & 1;
        const int l15 = lane & 15, g = lane >> 4, q = l15 >> 2, p4 = l15 & 3;
        unsigned ab[2], bb[2];
#pragma unroll
        for (int x = 0; x < 2; ++x) {
            ab[x] = opaque(lds0 + DNA + (wm * 16 + q) * 512 + (((x * 2 + (g >> 1)) ^ (l15 >> 3)) * 128) + p4 * 32 + (g & 1) * 16);
            bb[x] = opaque(lds0 + (g * 8 + q) * 128 + p4 * 8 + (((wn * 2 + x) ^ ((q >> 1) | ((g & 1) << 1))) << 5));
        }
        f32x4 acc[4][4];
        {
            const unsigned ra = lds0 + DNR + (wm * 64 + l15) * 128 + (((wn * 4 + g) ^ (l15 >> 1)) * 16);
            const bf16x8 selr[2] = {pick(g == (l15 >> 3), l15 & 7), pick(g == 2 + (l15 >> 3), l15 & 7)};
            static_for<0, 4>([&](auto ic) INL {
                constexpr int i = decltype(ic)::value;
                const bf16x8 rr = lds_a<i * 2048>(ra);
#pragma unroll
                for (int j = 0; j < 4; ++j) {
                    acc[i][j] = f32x4{0.f, 0.f, 0.f, 0.f};
                    if (j >= 2) acc[i][j] = mma(selr[j & 1], rr, acc[i][j]);
                    asm volatile("" : "+v"(acc[i][j]));
                }
            });
        }
        auto ldop = [&](bf16x8 (&af)[8], s16x4 (&bf)[16], auto slc, auto j0c, auto oc) INL {
            constexpr int SL = decltype(slc)::value, O = decltype(oc)::value;
            if constexpr (O == 2 || (O >= 5 && O < 12)) {
                constexpr int X = O == 2 ? 0 : O - 4;
                af[(X & 1) * 4 + X / 2] = lds_a<SL * ASZ + (X / 2) * 2048>(ab[X & 1]);
            } else {
                constexpr int X = decltype(j0c)::value * 4 + (O < 2 ? O : (O < 5 ? O - 1 : O - 8)), J = X / 4;
                bf[X] = lds_h<SL * ASZ + (J < 2 ? 8192 : 0) + ((X >> 1) & 1) * 4096 + (X & 1) * 512>(bb[J & 1]);
            }
        };
        auto ktile = [&](bf16x8 (&af)[8], s16x4 (&bf)[16], bf16x8 (&an)[8], s16x4 (&bn)[16], auto nslc, auto j0c, auto jnc, auto&& gap) INL {
            constexpr int J0 = decltype(j0c)::value, JN = decltype(jnc)::value;
            constexpr int NM = 8 * (4 - J0), NL = JN < 4 ? 8 + 4 * (4 - JN) : 0, TB = J0 ? DBAR2 : DBAR;
            constexpr int NG = NM - TB < NL ? NM - TB : NL;
            static_for<0, NM>([&](auto tc) INL {
                constexpr int T = decltype(tc)::value, j = J0 + T / 8, i = j & 1 ? 3 - (T / 2) % 4 : (T / 2) % 4, kk = T & 1;
                if constexpr (T == TB && NL > 0) {
                    WAIT_LGKM0();
                    BARRIER();
                }
                mma_ip(cat(bf[j * 4 + kk * 2], bf[j * 4 + kk * 2 + 1]), af[kk * 4 + i], acc[i][j]);
                SCHED();
                if constexpr (NL > 0 && T >= TB) {
                    constexpr int U = T - TB, lo_ = (U * NL + NG - 1) / NG, hi_ = ((U + 1) * NL + NG - 1) / NG;
                    static_for<lo_, (hi_ < NL ? hi_ : NL)>([&](auto oc) INL { ldop(an, bn, nslc, jnc, oc); });
                }
                gap(tc);
                SCHED();
            });
        };
        bf16x8 a0[8], a1[8];
        s16x4 b0[16], b1[16];
        const unsigned bo = opaque((unsigned)(wid * 4096 + lane * 16));
        const bf16x8 sel[2] = {pick(g == q, p4), pick(g == q, 4 + p4)};
        res_setup(rc, lds0 + NS * SLOT, m0, n0, tid);
        const unsigned cw = opaque(lds0 + NS * SLOT + (wm * 64 + l15) * CSTR + (wn * 32 + g * 4) * 2);
        const char* const rin = sopq((const char*)slab + (bx ^ 4u) * BOX);
        const __amdgpu_buffer_rsrc_t rout = __builtin_amdgcn_make_buffer_rsrc((char*)slab + bx * BOX, 0, BOX, 0x00020000);
        BARRIER();
        static_for<0, 24>([&](auto oc) INL { ldop(a0, b0, CT<0>{}, CT<0>{}, oc); });
        auto none = [&](auto) INL {};
#pragma unroll 1
        for (int kt = 0; kt < NRT; kt += NS) {
            ktile(a0, b0, a1, b1, CT<1>{}, CT<0>{}, CT<0>{}, none);
            ktile(a1, b1, a0, b0, CT<2>{}, CT<0>{}, CT<0>{}, none);
            ktile(a0, b0, a1, b1, CT<3>{}, CT<0>{}, CT<0>{}, none);
            ktile(a1, b1, a0, b0, CT<0>{}, CT<0>{}, CT<0>{}, none);
        }
        u32x4 ow, pa[4];
        auto look = [&](u32x4& d, auto ic) INL { gload16_devo<decltype(ic)::value * 1024>(d, rin, bo); };
        static_for<NRT, NK>([&](auto sc) INL {
            constexpr int KT = decltype(sc)::value;
            constexpr int J0 = KT < NSH ? 0 : 2, JN = KT + 1 < NSH ? 0 : (KT + 1 < NK ? 2 : 4);
            auto gap = [&](auto tc) INL {
                constexpr int T = decltype(tc)::value;
                if constexpr (KT == NSH - 1 && T >= XO && T < XO + 16) {
                    constexpr int U = T - XO, j = U / 8, i = j ? 3 - (U / 2) % 4 : (U / 2) % 4;
                    asm volatile("v_cvt_pk_bf16_f32 %0, %1, %2\n\tv_min_u32 %0, -2, %0"
                                 : "=v"(ow[(i & 1) * 2 + (U & 1)]) : "v"(acc[i][j][(U & 1) * 2]), "v"(acc[i][j][(U & 1) * 2 + 1]));
                    if constexpr (U % 4 == 3) __builtin_amdgcn_raw_buffer_store_b128(ow, rout, bo + (j * 2 + i / 2) * 1024, 0, 16);
                }
                constexpr int X = KT < NSH ? -999 : (KT - NK) * 16 + T;
                if constexpr (X >= -XA && X < -XA + 4) look(pa[X + XA], CT<X + XA>{});
                if constexpr (X >= -XB && X < -XB + 4) look(pb[X + XB], CT<X + XB>{});
            };
            if constexpr (KT & 1) ktile(a1, b1, a0, b0, CT<(KT + 1) % NS>{}, CT<J0>{}, CT<JN>{}, gap);
            else ktile(a0, b0, a1, b1, CT<(KT + 1) % NS>{}, CT<J0>{}, CT<JN>{}, gap);
        });
        box_wait(pa, pb, pc, rin, bo);
#pragma unroll
        for (int t = 0; t < 8; ++t) mma_ip(sel[(t >> 1) & 1], __builtin_bit_cast(bf16x8, pa[(t & 1) * 2 + t / 4]), acc[t >> 1][2 + (t & 1)]);
        asm volatile("s_nop 1" ::: "memory");
        SCHED();
#pragma unroll
        for (int i = 0; i < 4; ++i) {
#pragma unroll
            for (int x = 0; x < 2; ++x) {
                const f32x4 a = acc[i][2 + x];
                *(LDS_AS u32x2*)(size_t)(cw + i * 16 * CSTR + x * 32) = u32x2{pk2(a[0], a[1]), pk2(a[2], a[3])};
            }
            SCHED();
        }
    }
    WAIT_LGKM0();
    BARRIER();
    res_tail(rc, C, sp);
    if (pf != nullptr) publish(oflags + (tm * 16 + (n0 >> 6)) * 32, tok, wid); else wait_vm<0>();
#pragma unroll
    for (int i = 0; i < 4; ++i) asm volatile("" :: "v"(pb[i]), "v"(pc[i]));
}

__device__ __forceinline__ void gu_phase(char* smem, const u16* A, const u16* B0, const u16* B1, u16* C, const float* cs,
                                         const float* sp, const u16* wd, bool first, unsigned* flags, u64 tok) {
    constexpr int BM = 256, BN = 224, TM = 64, MI = 4, NI = 7, NK = D_ / 64;
    constexpr int ASZ = BM * 128, BSZ = BN * 128, NSA = 3, NSB = 2;
    constexpr int BRING = NSA * ASZ, RING = BRING + NSB * BSZ, STL = RING + BN * 4, MIX = DNR + 16384;
    constexpr auto asl = [](int kt) { return (kt + 2) % NSA; };

    const int tid = (int)opaque(threadIdx.x), lane = tid & 63;
    const int wid = __builtin_amdgcn_readfirstlane(tid >> 6);
    const int wm = wid >> 1, wn = wid & 1;
    const int id = wg_id();
    const int tm = id & 7, tn = id >> 3, m0 = tm * BM;
    float* const csl = (float*)(smem + RING);
    const unsigned lds0 = __builtin_amdgcn_readfirstlane((unsigned)(size_t)(LDS_AS char*)smem);
    const unsigned ldsw = lds0 + wid * 1024;

    unsigned aoff[4], boff[4];
#pragma unroll
    for (int i = 0; i < 4; ++i) {
        const int row = tid >> 3, o = i * 8192 + tid * 16;
        aoff[i] = opaque((unsigned)((m0 + i * 64 + row) * D_ + ((tid & 7) ^ ((row >> 1) & 7)) * 8) * 2u);
        boff[i] = opaque(gu_bsrc(o < BSZ ? o : 0, tn));
    }
    const __amdgpu_buffer_rsrc_t ra = __builtin_amdgcn_make_buffer_rsrc((void*)A, 0, 0x7fffffff, 0x00020000);
    const __amdgpu_buffer_rsrc_t rb[4] = {__builtin_amdgcn_make_buffer_rsrc((void*)B0, 0, 0x7fffffff, 0x00020000),
                                          __builtin_amdgcn_make_buffer_rsrc((void*)(wid >= 6 ? B1 : B0), 0, 0x7fffffff, 0x00020000),
                                          __builtin_amdgcn_make_buffer_rsrc((void*)B1, 0, 0x7fffffff, 0x00020000),
                                          __builtin_amdgcn_make_buffer_rsrc((void*)B1, 0, wid < 4 ? 0x7fffffff : 0, 0x00020000)};
    const unsigned b3d[2] = {wid < 4 ? ldsw + BRING + 24576 : lds0 + 161792, wid < 4 ? ldsw + BRING + BSZ + 24576 : lds0 + 161792};
    unsigned soff = 0u;
    auto prep = [&](auto bc, auto pc, auto ktc) INL {
        constexpr int P = decltype(pc)::value, KT = decltype(ktc)::value;
        constexpr bool ISB = decltype(bc)::value;
        if constexpr (P == 0) s_set<(ISB ? KT * 64 * F_ * 2 : KT * 128)>(soff);
        if constexpr (ISB && P == 3) m0_set<0>(b3d[KT % NSB]);
        else m0_set<(ISB ? BRING + (KT % NSB) * BSZ : asl(KT) * ASZ) + P * 8192>(ldsw);
    };
    const int dtm = ((id >> 5) & 3) * 4 + (id & 3), dtn = ((id >> 2) & 7) * 2 + (id >> 7);
    const unsigned dkh = (id >> 7) * 128;
    const __amdgpu_buffer_rsrc_t rd = __builtin_amdgcn_make_buffer_rsrc((char*)wd + dkh * ((F_ / 64 - NSOLO) / 2 * D_) + (dtn >> 1) * 256, 0,
        0x7fffffff, 0x00020000);
    const __amdgpu_buffer_rsrc_t rr = __builtin_amdgcn_make_buffer_rsrc((char*)A + (dtm * 128 - m0) * D_ * 2 + dtn * 128, 0, 0x7fffffff, 0x00020000);
    unsigned doff;
    constexpr auto dreq = [](int sl) { return sl < 0 ? -1 : (int)((DSCH >> (4 * sl)) & 15) - 1; };
    constexpr int DV = (dreq(0) >= 0) + (dreq(1) >= 0) + (dreq(2) >= 0) + (dreq(3) >= 0) + (TCU <= 0);
    auto dprep = [&](auto xc) INL {
        constexpr int X = decltype(xc)::value;
        if constexpr (X == 0) {
            const int t = (int)opaque(threadIdx.x);
            doff = opaque(b_off(t >> 6, t & 63, D_, 0));
        }
        if constexpr (X < 8) asm volatile("s_xor_b32 %0, %1, %2" : "=s"(soff) : "s"(dkh), "n"((X >> 1) * 64 * D_ * 2 + (X & 1) * 128) : "scc");
        m0_set<(X < 8 ? 0 : DNR - 65536) + X * 8192>(ldsw);
    };
    auto dgo = [&](auto xc) INL {
        constexpr int X = decltype(xc)::value;
        if constexpr (X < 8) blds16<false>(rd, doff, soff);
        else blds16<true>(rr, aoff[X - 8], 0u);
    };
    auto go = [&](auto bc, auto pc) INL {
        if constexpr (decltype(bc)::value) blds16<false>(rb[decltype(pc)::value], boff[decltype(pc)::value], soff);
        else blds16<true>(ra, aoff[decltype(pc)::value], soff);
    };
    auto stage_a = [&](auto ktc) INL {
        static_for<0, 4>([&](auto pc) INL { prep(CT<0>{}, pc, ktc); asm volatile("s_nop 0"); go(CT<0>{}, pc); });
    };
    unsigned tof;
    {
        const unsigned t = tid & 255, kr = ((id >> 7) & 1) * 1664, j = (id & 31) * 256 + t;
        unsigned rw = j >> 2;
        rw = rw < 1664 ? kr + rw : (rw < 1920 ? rw + 1664 : kr);
        tof = opaque(wid < 4 ? rw * 2048 + 256 + (j & 1) * 128 + (j & 2) * 512 : (first ? (id * 224 + (t < 224 ? t : 0u)) * 128u : 0u));
    }
    __builtin_amdgcn_s_setprio(2);

    const int l15 = lane & 15, g = lane >> 4;
    const int q = l15 >> 2, p4 = l15 & 3;
    unsigned ab[2][2], bb[2][2];
#pragma unroll
    for (int kk = 0; kk < 2; ++kk) {
        const unsigned sw = ((kk * 4 + g) ^ (l15 >> 1)) * 16;
        ab[kk][0] = opaque(lds0 + (wm * TM + l15) * 128 + sw);
        ab[kk][1] = opaque(lds0 + (wm * TM + l15) * 128 + sw + 65536);
        const unsigned r = lds0 + BRING + ((g * 8 + kk * 4 + q) ^ (4 * (g & 1))) * BN;
        bb[kk][0] = opaque(r + wn * 128 + p4 * 8);
        bb[kk][1] = opaque(r + 96 + wn * 16 + (p4 & 1) * 8 + (p4 >> 1) * (BSZ / 2));
    }
    f32x4 acc[MI][NI];
#pragma unroll
    for (int i = 0; i < MI; ++i)
#pragma unroll
        for (int j = 0; j < NI; ++j) {
            acc[i][j] = f32x4{0.f, 0.f, 0.f, 0.f};
            asm volatile("" : "+v"(acc[i][j]));
        }
    bf16x8 af[2][2 * MI];
    s16x4 bf[2][4 * NI];
    auto ldop = [&](auto ktc, auto oc) INL {
        constexpr int KT = decltype(ktc)::value, O = decltype(oc)::value;
        if constexpr (O == 2 || (O >= 5 && O < 12)) {
            constexpr int X = O == 2 ? 0 : O - 4, off = asl(KT) * ASZ + (X / 2) * 2048;
            af[KT & 1][X] = lds_a<(off & 65535)>(ab[X & 1][off >> 16]);
        } else {
            constexpr int X = O < 2 ? O : (O < 5 ? O - 1 : O - 8), J = X / 4;
            bf[KT & 1][X] = lds_h<(KT % NSB) * BSZ + ((X >> 1) & 1) * 32 * BN + (J == 6 ? 0 : (J / 3) * (BSZ / 2) + (J % 3) * 32)>(bb[X & 1][J == 6]);
        }
    };
    f32x4 stn[4];
    float sx[8];
    const unsigned sto = opaque(wid * 4096 + g * 1024 + l15 * 16);
    const char* const spw = uni((const char*)sp + m0 * 128);
    auto stats = [&](auto zc) INL {
        constexpr int Z = decltype(zc)::value;
        if constexpr (Z == 0) {
#pragma unroll
            for (int i = 0; i < 4; ++i) asm volatile("" : "+v"(stn[i]));
        }
        if constexpr (Z < 8) asm volatile("v_add_f32 %0, %1, %2" : "=v"(sx[Z]) : "v"(stn[Z / 4 * 2][Z % 4]), "v"(stn[Z / 4 * 2 + 1][Z % 4]));
        else if constexpr (Z < 12) asm volatile("v_add_f32 %0, %0, %1" : "+v"(sx[(Z & 1) * 2 + (Z / 2 & 1)]) : "v"(sx[4 + (Z & 1) * 2 + (Z / 2 & 1)]));
        else if constexpr (Z < 14) asm volatile("v_permlane16_swap_b32 %0, %1" : "+v"(sx[Z - 12]), "+v"(sx[Z - 10]));
        else if constexpr (Z < 16) asm volatile("v_add_f32 %0, %0, %1" : "+v"(sx[Z - 14]) : "v"(sx[Z - 12]));
        else if constexpr (Z == 17 || Z == 22) asm volatile("v_permlane32_swap_b32 %0, %1" : "+v"(sx[0]), "+v"(sx[1]));
        else if constexpr (Z == 18) asm volatile("v_add_f32 %0, %0, %1" : "+v"(sx[0]) : "v"(sx[1]));
        else if constexpr (Z == 19) asm volatile("v_mul_f32 %0, 0x3a800000, %1" : "=v"(sx[1]) : "v"(sx[0]));
        else if constexpr (Z == 20) asm volatile("v_mul_f32 %0, 0x3a800000, %0" : "+v"(sx[0]));
        else if constexpr (Z == 23) asm volatile("v_mul_f32 %0, %1, %1" : "=v"(sx[2]) : "v"(sx[0]));
        else if constexpr (Z == 24) asm volatile("v_sub_f32 %0, %0, %1" : "+v"(sx[1]) : "v"(sx[2]));
        else if constexpr (Z == 25) asm volatile("v_max_f32 %0, 0, %0" : "+v"(sx[1]));
        else if constexpr (Z == 26) asm volatile("v_cvt_pk_bf16_f32 %0, %0, -%1" : "+v"(sx[1]) : "v"(sx[0]));
        else if constexpr (Z == 27) asm volatile("v_and_b32 %0, 0xffff0000, %1" : "=v"(sx[0]) : "v"(sx[1]));
        else if constexpr (Z == 28 || Z == 31) asm volatile("v_lshlrev_b32 %0, 16, %0" : "+v"(sx[1]));
        else if constexpr (Z == 29) asm volatile("v_add_f32 %0, 0x3727c5ac, %0" : "+v"(sx[1]));
        else if constexpr (Z == 30 || Z == 33) asm volatile("v_cvt_pk_bf16_f32 %0, %0, %0" : "+v"(sx[1]));
        else if constexpr (Z == 32) asm volatile("v_sqrt_f32 %0, %0" : "+v"(sx[1]));
        else if constexpr (Z == 34) asm volatile("v_lshlrev_b32 %0, 16, %1" : "=v"(sx[2]) : "v"(sx[1]));
        else if constexpr (Z == 35) asm volatile("v_rcp_f32 %0, %1" : "=v"(sx[1]) : "v"(sx[2]));
        else if constexpr (Z == 36) asm volatile("v_mul_f32 %0, %0, %0" : "+v"(sx[2]));
        else if constexpr (Z == 37) asm volatile("v_mul_f32 %0, 0xbfb8aa3b, %0" : "+v"(sx[1]));
        else if constexpr (Z == 16) asm volatile("v_and_b32 %0, 31, %1" : "=v"(sx[4]) : "v"(threadIdx.x));
        else if constexpr (Z == 21) asm volatile("v_lshl_add_u32 %0, %0, 4, %1" : "+v"(sx[4]) : "s"(lds0 + STL + wid * 512));
        else if constexpr (Z == 38) *(LDS_AS f32x4*)(size_t)(__float_as_uint(sx[4])) = f32x4{sx[0], sx[1], sx[2], sx[3]};
    };
    static_assert((2 * (NK - 2) + 1 - (2 * STK + 5)) * SPU >= 39, "statistics in time");
    f32x4 ccv[NI], stv[MI];
    const unsigned cca = opaque(lds0 + RING + (wn * 64 + g * 4) * 4), cc6 = opaque(lds0 + RING + (48 + wn * 8 + (g & 1) * 4 + (g >> 1) * 112) * 4);
    const unsigned sta = opaque(lds0 + STL + wm * 1024 + (l15 & 1) * 256 + (l15 >> 1) * 16);
    const unsigned gof = opaque(((m0 + wm * TM) / 4 + (g & 1) * 4 + q) * (8 * F_) + (tn * 7 + wn * 4) * 128 + p4 * 32 + (g >> 1) * 16);
    const unsigned gofm = opaque(((m0 + wm * TM + wn * 32) / 4 + (lane >> 3)) * (8 * F_) + (tn * 7 + 3) * 128 + (lane & 7) * 16);
    const unsigned hsw = opaque(lds0 + MIX + (wm * TM + (g >> 1) * 16 + l15) * 32 + wn * 16 + (g & 1) * 8);
    const unsigned hsr = opaque(lds0 + MIX + wid * 1024 + lane * 16);
    auto eld = [&](auto oc) INL {
        constexpr int O = decltype(oc)::value;
        constexpr int SI = O == 0 ? 0 : (O == 3 ? 1 : (O == 7 ? 2 : (O == 8 ? 3 : -1)));
        constexpr int J = O < 3 ? O * 3 - 3 : (O < 6 ? O * 3 - 11 : (O == 6 ? 6 : O * 3 - 25));
        if constexpr (SI >= 0) stv[SI] = *(const LDS_AS f32x4*)(size_t)(sta + (SI >> 1) * 512 + (SI & 1) * 128);
        else if constexpr (J == 6) ccv[6] = *(const LDS_AS f32x4*)(size_t)(cc6);
        else ccv[J] = *(const LDS_AS f32x4*)(size_t)(cca + ((J / 3) * 112 + (J % 3) * 16) * 4);
    };
    auto unit = [&](auto ktc, auto kkc) INL {
        constexpr int KT = decltype(ktc)::value, KK = decltype(kkc)::value;
        constexpr int NKT = KK ? KT + 1 : KT;
        static_for<0, MI * NI>([&](auto tc) INL {
            constexpr int T = decltype(tc)::value, X = KK * MI * NI + T, j = X / 8, i = j & 1 ? MI - 1 - (X / 2) % MI : (X / 2) % MI;
            mma_ip(cat(bf[KT & 1][4 * j + 2 * (X & 1)], bf[KT & 1][4 * j + 2 * (X & 1) + 1]), af[KT & 1][2 * i + (X & 1)], acc[i][j]);
            SCHED();
            constexpr bool RQ = KT + 2 < NK && T < 4 * GGS;
            if constexpr (RQ && T % GGS == 0) prep(kkc, CT<T / GGS>{}, CT<KT + 2>{});
            if constexpr (RQ && T % GGS == 1) go(kkc, CT<T / GGS>{});
            constexpr int DX = T < 4 * GGS ? dreq((2 * (KT - NK + 2) + KK) * 4 + T / GGS) : -1;
            if constexpr (DX >= 0 && T % GGS == 0) dprep(CT<DX>{});
            if constexpr (DX >= 0 && T % GGS == 1) dgo(CT<DX>{});
            if constexpr (2 * (KT - NK + 2) + KK == TCU && T == TCT) touch1(SINK, wd, tof);
            constexpr int O = T - (T + GGS - 2) / GGS;
            if constexpr (NKT < NK && T % GGS != 1 && O < (KK ? 12 : 36 - GL2)) ldop(CT<NKT>{}, CT<(KK ? O : O + GL2)>{});
            if constexpr (NKT < NK && KK == 1 && T >= 20 && T < 8 + GL2) ldop(CT<NKT>{}, CT<T - 8>{});
            if constexpr (KT == STK && KK == 1 && T >= STT && T < STT + 4) gload16_devo<(T - STT) * 256>(stn[T - STT], spw, sto);
            constexpr int Z = (2 * KT + KK - 2 * STK - 5) * SPU + T - (MI * NI - SPU);
            if constexpr (Z >= 0 && Z < 39 && T >= MI * NI - SPU) stats(CT<Z>{});
            if constexpr (NKT == NK && T < 11) eld(tc);
            if constexpr (KK == 0 && T == PRT && KT < NK - 1) __builtin_amdgcn_s_setprio(0);
            SCHED();
        });
    };

    if (wid == 0) {
        const int c = (lane < BN / 4 ? lane : 0) * 4;
        const unsigned co = (unsigned)((c >= BN / 2 ? F_ + c - BN / 2 : c) + tn * (BN / 2)) * 4u;
        f32x4 cp[8];
#pragma unroll
        for (int i = 0; i < 8; ++i) gload16_dev(cp[i], cs + i * 2 * F_, co);
        wait_vm<0>();
#pragma unroll
        for (int i = 0; i < 8; ++i) {
            asm volatile("" : "+v"(cp[i]));
            if (i) cp[0] += cp[i];
        }
        if (lane < BN / 4) *(f32x4*)(csl + lane * 4) = cp[0];
    }
    if (wid == 1) {
        const int pid = id ^ 128;
        const int w = lane < 32 ? tm * 32 + lane : (lane < 48 ? dtm * 16 + dtn : ((pid / 64) * 4 + pid % 4) * 16 + (pid % 64) / 4);
#pragma unroll 1
        for (unsigned spins = 0; spins < (1u << 22); ++spins) {
            if (__all(__hip_atomic_load((gu64*)(flags + w * 32), __ATOMIC_RELAXED, __HIP_MEMORY_SCOPE_AGENT) == tok)) break;
            __builtin_amdgcn_s_sleep(1);
        }
    }
    BARRIER();
    stage_a(CT<0>{});
    wait_vm<0>();
    BARRIER();
    stage_a(CT<1>{});
    static_for<0, GL2>([&](auto oc) INL { ldop(CT<0>{}, oc); });
    static_for<0, NK>([&](auto kc) INL {
        constexpr int KT = decltype(kc)::value;
        unit(CT<KT>{}, CT<0>{});
        if constexpr (KT < NK - 1) {
            WAIT_LGKM0();
            wait_vm<(KT == STK + 1 ? 8 : (KT == NK - 2 ? DV : 4))>();
            BARRIER();
            if constexpr (PRT < 28) __builtin_amdgcn_s_setprio(2);
        }
        unit(CT<KT>{}, CT<1>{});
    });
    asm volatile("s_nop 15\n\ts_nop 7" ::: "memory");
    SCHED();
    const int tide = (int)opaque(threadIdx.x);
    {
        __builtin_amdgcn_s_setprio(0);
        auto center = [&](f32x4 a, f32x4 c, float nm) INL {
            float a0, a1, a2, a3;
            asm("v_fma_f32 %0, %4, %5, %9\n\tv_fma_f32 %1, %4, %6, %10\n\tv_fma_f32 %2, %4, %7, %11\n\tv_fma_f32 %3, %4, %8, %12"
                : "=&v"(a0), "=&v"(a1), "=&v"(a2), "=&v"(a3)
                : "v"(nm), "v"(c[0]), "v"(c[1]), "v"(c[2]), "v"(c[3]), "v"(a[0]), "v"(a[1]), "v"(a[2]), "v"(a[3]));
            return f32x4{a0, a1, a2, a3};
        };
        auto glu = [&](f32x4 gp, f32x4 up, float c, float k) INL {
            float t0, t1, t2, t3, u0 = up[0], u1 = up[1], u2 = up[2], u3 = up[3];
            asm("v_mul_f32 %0, %12, %8\n\tv_mul_f32 %1, %12, %9\n\tv_mul_f32 %2, %12, %10\n\tv_mul_f32 %3, %12, %11\n\t"
                "v_exp_f32 %0, %0\n\tv_exp_f32 %1, %1\n\tv_exp_f32 %2, %2\n\tv_exp_f32 %3, %3\n\t"
                "v_mul_f32 %4, %4, %8\n\tv_mul_f32 %5, %5, %9\n\t"
                "v_fma_f32 %0, %0, %13, %13\n\tv_fma_f32 %1, %1, %13, %13\n\t"
                "v_fma_f32 %2, %2, %13, %13\n\tv_fma_f32 %3, %3, %13, %13\n\t"
                "v_rcp_f32 %0, %0\n\tv_rcp_f32 %1, %1\n\tv_rcp_f32 %2, %2\n\tv_rcp_f32 %3, %3\n\t"
                "v_mul_f32 %6, %6, %10\n\tv_mul_f32 %7, %7, %11\n\t"
                "v_mul_f32 %4, %4, %0\n\tv_mul_f32 %5, %5, %1\n\tv_mul_f32 %6, %6, %2\n\tv_mul_f32 %7, %7, %3"
                : "=&v"(t0), "=&v"(t1), "=&v"(t2), "=&v"(t3), "+v"(u0), "+v"(u1), "+v"(u2), "+v"(u3)
                : "v"(gp[0]), "v"(gp[1]), "v"(gp[2]), "v"(gp[3]), "v"(c), "v"(k));
            return u32x2{pk2(u0, u1), pk2(u2, u3)};
        };
        auto centerp = [&](f32x4 a, f32x4 c, float nm) INL {
            const f32x2 n2 = {nm, nm};
            const f32x2 lo = __builtin_elementwise_fma(n2, c.xy, a.xy), hi = __builtin_elementwise_fma(n2, c.zw, a.zw);
            return f32x4{lo.x, lo.y, hi.x, hi.y};
        };
        auto glu2 = [&](f32x2 gp, f32x2 up, float c, float k) INL {
            const f32x2 ea = gp * f32x2{c, c}, k2 = {k, k};
            const f32x2 d = __builtin_elementwise_fma(f32x2{__builtin_amdgcn_exp2f(ea.x), __builtin_amdgcn_exp2f(ea.y)}, k2, k2);
            const f32x2 hv = (gp * up) * f32x2{__builtin_amdgcn_rcpf(d.x), __builtin_amdgcn_rcpf(d.y)};
            return pk2(hv.x, hv.y);
        };
        auto glup = [&](f32x4 gp, f32x4 up, float c, float k) INL {
            return u32x2{glu2(gp.xy, up.xy, c, k), glu2(gp.zw, up.zw, c, k)};
        };
        u32x2 hp[2];
        auto blk = [&](int i, int j, auto pkc) INL {
            if constexpr (decltype(pkc)::value)
                hp[i & 1] = glup(centerp(acc[i][j], ccv[j], stv[i][0]), centerp(acc[i][j + 3], ccv[j + 3], stv[i][0]), stv[i][1], stv[i][2]);
            else
                hp[i & 1] = glu(center(acc[i][j], ccv[j], stv[i][0]), center(acc[i][j + 3], ccv[j + 3], stv[i][0]), stv[i][1], stv[i][2]);
        };
        auto blk2 = [&](int i, int j) INL {
            f32x2 gp[4], up[4], ea[4];
#pragma unroll
            for (int x = 0; x < 4; ++x) {
                const f32x4 a = acc[i + (x >> 1)][j], c = ccv[j];
                const float nm = stv[i + (x >> 1)][0];
                gp[x] = __builtin_elementwise_fma(f32x2{nm, nm}, x & 1 ? c.zw : c.xy, x & 1 ? a.zw : a.xy);
            }
            SCHED();
#pragma unroll
            for (int x = 0; x < 4; ++x) { const float c = stv[i + (x >> 1)][1]; ea[x] = gp[x] * f32x2{c, c}; }
            SCHED();
#pragma unroll
            for (int x = 0; x < 4; ++x) {
                const f32x4 a = acc[i + (x >> 1)][j + 3], c = ccv[j + 3];
                const float nm = stv[i + (x >> 1)][0];
                ea[x] = f32x2{__builtin_amdgcn_exp2f(ea[x].x), __builtin_amdgcn_exp2f(ea[x].y)};
                up[x] = __builtin_elementwise_fma(f32x2{nm, nm}, x & 1 ? c.zw : c.xy, x & 1 ? a.zw : a.xy);
                SCHED();
            }
#pragma unroll
            for (int x = 0; x < 4; ++x) { const float k = stv[i + (x >> 1)][2]; ea[x] = __builtin_elementwise_fma(ea[x], f32x2{k, k}, f32x2{k, k}); }
            SCHED();
#pragma unroll
            for (int x = 0; x < 4; ++x) {
                ea[x] = f32x2{__builtin_amdgcn_rcpf(ea[x].x), __builtin_amdgcn_rcpf(ea[x].y)};
                up[x] = gp[x] * up[x];
                SCHED();
            }
#pragma unroll
            for (int x = 0; x < 4; ++x) up[x] = up[x] * ea[x];
            SCHED();
#pragma unroll
            for (int x = 0; x < 4; ++x) hp[x & 1][x >> 1] = pk2(up[(x & 1) * 2 + (x >> 1)].x, up[(x & 1) * 2 + (x >> 1)].y);
        };
        auto flush = [&](int hf, int j) INL {
            const auto t0 = __builtin_amdgcn_permlane16_swap(hp[0][0], hp[1][0], false, false);
            const auto t1 = __builtin_amdgcn_permlane16_swap(hp[0][1], hp[1][1], false, false);
            store16_wt(C, gof + hf * 8 * (8 * F_) + j * 128, u32x4{t0[0], t1[0], t0[1], t1[1]});
            SCHED();
        };
        auto head = [&](auto pkc) INL { blk(0, 0, pkc); blk(1, 0, pkc); flush(0, 0); blk(0, 1, pkc); };
        auto sync = [&]() INL {
            WAIT_LGKM0();
            BARRIER();
            asm volatile("" ::: "memory");
            SCHED();
        };
        const bool late = wid >= 4;
        if (!late) head(CT<0>{});
        sync();
#pragma unroll
        for (int i = 0; i < MI; ++i)
#pragma unroll
            for (int j = 0; j < NI; ++j) asm volatile("" : "+v"(acc[i][j]));
#pragma unroll
        for (int i = 0; i < MI; i += 2) {
            const f32x4 x = centerp(acc[i][6], ccv[6], stv[i][0]), y = centerp(acc[i + 1][6], ccv[6], stv[i + 1][0]);
            f32x4 gm, um;
#pragma unroll
            for (int r = 0; r < 4; ++r) {
                const auto t = __builtin_amdgcn_permlane32_swap(__float_as_uint(x[r]), __float_as_uint(y[r]), false, false);
                gm[r] = __uint_as_float(t[0]); um[r] = __uint_as_float(t[1]);
            }
            const bool hi = (tide & 32) != 0;
            *(LDS_AS u32x2*)(size_t)(hsw + i * 512) =
                glup(gm, um, hi ? stv[i + 1][1] : stv[i][1], hi ? stv[i + 1][2] : stv[i][2]);
        }
        u32x4 mv;
        auto mixed = [&]() INL {
            sync();
            mv = *(const LDS_AS u32x4*)(size_t)(hsr);
            SCHED();
        };
        if constexpr (MXP == 0) { mixed(); store16_wt(C, gofm, mv); }
        if (late) {
            __builtin_amdgcn_s_setprio(LPR);
            blk2(0, 0);
            flush(0, 0);
            blk2(0, 1);
        } else {
            blk(1, 1, CT<1>{});
        }
        flush(0, 1);
        if constexpr (MXP == 1) mixed();
        blk2(0, 2);
        if constexpr (MXP == 1) store16_wt(C, gofm, mv);
        flush(0, 2);
#pragma unroll
        for (int j = 0; j < 3; ++j) {
            blk2(2, j);
            flush(1, j);
        }
        __builtin_amdgcn_s_setprio(0);
        publish(flags + (512 + tm * 32 + tn) * 32, tok, wid);
    }
}

constexpr long long W_OB = 12, W_H = 16, W_XA = 32, W_XB = 36, W_CS = 40, W_SPA = 42, W_SPB = 43, W_FL = 44, W_EP = 45, W_SL = 48, W_L1 = 48;
extern "C" __global__ __launch_bounds__(512, 2) void body_k(const u16* __restrict__ x0, const u16* __restrict__ wqkv,
                                                 const u16* __restrict__ wo, const u16* __restrict__ wg,
                                                 const u16* __restrict__ wu, const u16* __restrict__ wd, u16* out,
                                                 char* ws) {
    extern __shared__ __attribute__((aligned(16))) char smem[];
    {
        const int tid = threadIdx.x, lane = tid & 63, wid = __builtin_amdgcn_readfirstlane(tid >> 6);
        if (wid >= 2 && wid < 6) {
            const int id = (blockIdx.x & 7) * 32 + (blockIdx.x >> 3);
            const int tm = (id / 128) * 8 + id % 8, tn = (id % 128) / 8, h = wid - 2;
            const int row = h * 8 + (lane >> 3), hr = h * 4 + ((lane >> 3) & 3);
            const unsigned la = (unsigned)((tm * 128 + row) * D_ + ((lane & 7) ^ ((row >> 1) & 7)) * 8) * 2u;
            const unsigned lh = (unsigned)((tm ? tm * 128 - 16 + hr : 0) * D_ + ((lane & 7) ^ ((hr >> 1) & 7)) * 8) * 2u;
            const unsigned lb = b_off(h, lane, 3 * D_, tn * 64);
#pragma unroll 1
            for (int kt = 0; kt < 2; ++kt) {
#pragma unroll
                for (int i = 0; i < 4; ++i) glds16(uni((const char*)x0 + kt * 128 + i * 32 * D_ * 2), la, (2 - 2 * kt) * QAS + h * 1024 + i * 4096);
                glds16_mask(uni((const char*)x0 + kt * 128), lh, (2 - 2 * kt) * QAS + 16384 + h * 512, h == 0 ? 0xffffff00ull : 0xffffffffull);
                qkv_stage_b(0, wqkv, lb, h, kt, 2 - 2 * kt);
            }
        } else if (wid == 1) {
            code_touch(SINK, lane);
        }
    }
    u64 tok = 0;
#pragma unroll 1
    for (int L = 0; L < 2; ++L) {
        auto at = [&](long long mb, int l) INL { return sopq(ws) + ((mb + l * W_L1) << 20); };
        auto fl = [&](int ph) INL { return (unsigned*)at(W_FL, 0) + 16384 + ph * 8192; };
        qkv_phase(smem, L ? (u16*)at(W_XB, 0) : x0, sopq(wqkv), (u16*)at(W_OB, L), (float*)at(W_CS, 0), (float*)at(W_SPB, 0), fl(4),
                  fl(7 - L), sopq(wo), L == 0, sopq(wg), sopq(wu), (u64*)at(W_EP, 0), tok);
        wo_phase(smem, (u16*)at(W_OB, L), sopq(wo), (u16*)at(W_XA, L), (float*)at(W_SPA, L), fl(7 - L),
                 fl(L), L == 0, sopq(wg), sopq(wu), tok);
        gu_phase(smem, (u16*)at(W_XA, L), sopq(wg), sopq(wu), (u16*)at(W_H, L), (float*)at(W_CS, 0), (float*)at(W_SPA, L), sopq(wd),
                 L == 0, fl(L), tok);
        dn_phase(smem, (u16*)at(W_H, L), sopq(wd), L ? out : (u16*)at(W_XB, 0), L ? nullptr : (float*)at(W_SPB, 0),
                 (float*)at(W_SL, L), fl(2 + L), fl(4 + L), L ? nullptr : sopq(wqkv), tok);
    }
    asm volatile(".Lbody_end:");
}

extern "C" void kernel_launch(void* const* d_in, const int* in_sizes, int n_in, void* d_out, int out_size,
                              void* d_ws, size_t ws_size, hipStream_t stream) {
    const u16* x0 = (const u16*)d_in[0];
    const u16* wqkv = (const u16*)d_in[1];
    const u16* wo = (const u16*)d_in[2];
    const u16* wg = (const u16*)d_in[4];
    const u16* wu = (const u16*)d_in[5];
    const u16* wd = (const u16*)d_in[6];
    constexpr int smem = 163840;
    (void)hipFuncSetAttribute((const void*)body_k, hipFuncAttributeMaxDynamicSharedMemorySize, smem);
    body_k<<<256, 512, smem, stream>>>(x0, wqkv, wo, wg, wu, wd, (u16*)d_out, (char*)d_ws);
}
```

```cpp
#include <hip/hip_runtime.h>
#include <hip/hip_bf16.h>
#include <stdint.h>

#pragma clang diagnostic ignored "-Winline-asm"
typedef unsigned short u16;
using bf16x8 = __attribute__((ext_vector_type(8))) short;
using s16x4  = __attribute__((ext_vector_type(4))) short;
using f32x4  = __attribute__((ext_vector_type(4))) float;
using f32x2  = __attribute__((ext_vector_type(2))) float;
using u32x2  = __attribute__((ext_vector_type(2))) unsigned;
using u32x4  = __attribute__((ext_vector_type(4))) unsigned;
typedef __bf16 bf2_t __attribute__((ext_vector_type(2)));
#define LDS_AS __attribute__((address_space(3)))
#define INL __attribute__((always_inline))

constexpr int GLT = 20, WLK = 2, WL2 = 0, DLK = 2, DL2 = 0, GPV = 2, TCU = -1, TCT = 25, STT = 22, STK = 10, MXP = 1,
              LPR = 1, SPU = 10, PRT = 20, WOLD = 12, WOBAR = 4, WVN = 2, GL2 = 16, QL1 = 16, QG1 = 10, GGS = 6,
              WV0 = 5, PV0 = 6, HPRIO = 2, SLP = 3, KIN = 9, KSKIP = 2, NSOLO = 4, QPS = 0, DNE = 26, DPAR = 0, XO = 5,
              XA = 28, XB = 8, DBAR = 4, DBAR2 = 4, CSP = 1, WT0 = 11, WT1 = 12, QBT = 12, GBT = 6, GW2 = 0, QSO = 0,
              QSP = 2, WN4 = 2, MKP = 0, WN1 = 3, WN2 = 2, WN3 = 3;
constexpr unsigned long long DSCH = 0x87654321a9000000ull;
constexpr int S_ = 2048, D_ = 1024, F_ = 3584;

__device__ __forceinline__ float bf2f(u16 v) { return __uint_as_float(((unsigned)v) << 16); }
__device__ __forceinline__ u16 f2bf(float f) {
    __hip_bfloat16 h = __float2bfloat16(f);
    return *reinterpret_cast<u16*>(&h);
}
__device__ __forceinline__ float rbf(float f) { return bf2f(f2bf(f)); }
__device__ __forceinline__ unsigned pk2(float a, float b) {
    const f32x2 v = {a, b};
    return __builtin_bit_cast(unsigned, __builtin_convertvector(v, bf2_t));
}
__device__ __forceinline__ float addf(float a, float b) {
    float r;
    asm("v_add_f32 %0, %1, %2" : "=v"(r) : "v"(a), "v"(b));
    return r;
}
__device__ __forceinline__ float mulf(float a, float b) {
    float r;
    asm("v_mul_f32 %0, %1, %2" : "=v"(r) : "v"(a), "v"(b));
    return r;
}
__device__ __forceinline__ float subf(float a, float b) {
    float r;
    asm("v_sub_f32 %0, %1, %2" : "=v"(r) : "v"(a), "v"(b));
    return r;
}

template <int V> struct CT { static constexpr int value = V; };
template <int I, int N, class F> __device__ __forceinline__ void static_for(F&& f) {
    if constexpr (I < N) { f(CT<I>{}); static_for<I + 1, N>(f); }
}

#define WAIT_LGKM0() asm volatile("s_waitcnt lgkmcnt(0)" ::: "memory")
#define BARRIER() do { asm volatile("" ::: "memory"); __builtin_amdgcn_s_barrier(); asm volatile("" ::: "memory"); } while (0)
#define SCHED() __builtin_amdgcn_sched_barrier(0)
template <int N> __device__ __forceinline__ void wait_vm() {
    static_assert(N >= 0 && N < 64, "vmcnt range");
    asm volatile("s_waitcnt vmcnt(%0)" :: "n"(N) : "memory");
}

__device__ __forceinline__ void glds16(const void* sbase, unsigned voff, unsigned lds_dst) {
    asm volatile("s_mov_b32 m0, %2\n\ts_nop 3\n\tglobal_load_lds_dwordx4 %0, %1" :: "v"(voff), "s"(sbase), "s"(__builtin_amdgcn_readfirstlane(lds_dst)) : "memory", "m0");
}
template <int NLANE>
__device__ __forceinline__ void glds16_part(const void* sbase, unsigned voff, unsigned lds_dst) {
    unsigned long long ex;
    asm volatile("s_mov_b64 %0, exec\n\ts_mov_b32 m0, %3\n\ts_mov_b64 exec, %4\n\ts_nop 1\n\t"
                 "global_load_lds_dwordx4 %1, %2 sc1\n\ts_mov_b64 exec, %0"
                 : "=&s"(ex) : "v"(voff), "s"(sbase), "s"(__builtin_amdgcn_readfirstlane(lds_dst)), "n"((1ull << NLANE) - 1) : "memory", "m0");
}
__device__ __forceinline__ void glds16_mask(const void* sbase, unsigned voff, unsigned lds_dst, unsigned long long mask) {
    unsigned long long ex;
    asm volatile("s_mov_b64 %0, exec\n\ts_mov_b32 m0, %3\n\ts_mov_b64 exec, %4\n\ts_nop 1\n\t"
                 "global_load_lds_dwordx4 %1, %2\n\ts_mov_b64 exec, %0"
                 : "=&s"(ex) : "v"(voff), "s"(sbase), "s"(__builtin_amdgcn_readfirstlane(lds_dst)), "s"(mask) : "memory", "m0");
}
__device__ __forceinline__ void glds16_dev(const void* sbase, unsigned voff, unsigned lds_dst) {
    asm volatile("s_mov_b32 m0, %2\n\ts_nop 3\n\tglobal_load_lds_dwordx4 %0, %1 sc1" :: "v"(voff), "s"(sbase), "s"(__builtin_amdgcn_readfirstlane(lds_dst)) : "memory", "m0");
}
template <int V> __device__ __forceinline__ void s_set(unsigned& d) { asm volatile("s_mov_b32 %0, %1" : "=s"(d) : "n"(V)); }
template <int V> __device__ __forceinline__ void m0_set(unsigned base) { asm volatile("s_add_i32 m0, %0, %1" :: "s"(base), "n"(V) : "m0", "scc"); }
template <bool DEV> __device__ __forceinline__ void blds16(__amdgpu_buffer_rsrc_t r, unsigned voff, unsigned soff) {
    if constexpr (DEV) asm volatile("buffer_load_dwordx4 %0, %1, %2 offen sc1 lds" :: "v"(voff), "s"(r), "s"(soff) : "memory");
    else asm volatile("buffer_load_dwordx4 %0, %1, %2 offen lds" :: "v"(voff), "s"(r), "s"(soff) : "memory");
}
__device__ __forceinline__ void gload16_dev(f32x4& d, const void* sbase, unsigned voff) {
    asm volatile("s_nop 4\n\tglobal_load_dwordx4 %0, %1, %2 sc1" : "=v"(d) : "v"(voff), "s"(sbase) : "memory");
}
__device__ __forceinline__ void gload8_dev(unsigned long long& d, const void* p) {
    asm volatile("global_load_dwordx2 %0, %1, off sc1" : "=v"(d) : "v"(p) : "memory");
}
template <int OFF, class T> __device__ __forceinline__ void gload16_devo(T& d, const void* sbase, unsigned voff) {
    asm volatile("global_load_dwordx4 %0, %1, %2 offset:%3 sc1" : "=v"(d) : "v"(voff), "s"(sbase), "n"(OFF) : "memory");
}
template <int OFF> __device__ __forceinline__ void gload16(f32x4& d, const void* sbase, unsigned voff) {
    asm volatile("s_nop 4\n\tglobal_load_dwordx4 %0, %1, %2 offset:%3" : "=v"(d) : "v"(voff), "s"(sbase), "n"(OFF) : "memory");
}
constexpr unsigned SINK = 163584;
__device__ __forceinline__ void touch1(unsigned sink, const void* sbase, unsigned voff) {
    asm volatile("s_mov_b32 m0, %2\n\ts_nop 3\n\tglobal_load_lds_dword %0, %1" :: "v"(voff), "s"(sbase), "s"(sink) : "memory", "m0");
}
__device__ __forceinline__ const char* uni(const char* p) {
    const unsigned long long v = (unsigned long long)p;
    const unsigned lo = __builtin_amdgcn_readfirstlane((unsigned)v), hi = __builtin_amdgcn_readfirstlane((unsigned)(v >> 32));
    return (const char*)(((unsigned long long)hi << 32) | lo);
}
__device__ __forceinline__ void store16_wt(void* base, unsigned off, u32x4 v) {
    const __amdgpu_buffer_rsrc_t r = __builtin_amdgcn_make_buffer_rsrc(base, 0, 0x7fffffff, 0x00020000);
    __builtin_amdgcn_raw_buffer_store_b128(v, r, off, 0, 16);
}
__device__ __forceinline__ unsigned opaque(unsigned v) { asm volatile("" : "+v"(v)); return v; }
__device__ __forceinline__ unsigned bid_() { unsigned b = blockIdx.x; asm volatile("" : "+s"(b)); return b; }
__device__ __forceinline__ int wg_id() { const unsigned b = bid_(); return (b & 7) * 32 + (b >> 3); }
template <class T> __device__ __forceinline__ T* sopq(T* p) { asm volatile("" : "+s"(p)); return p; }
__device__ __forceinline__ void code_touch(unsigned sink, unsigned lane) {
    const char* pc;
    int off;
    unsigned len;
    asm volatile("s_getpc_b64 %0\n.Lct%=:\n\ts_mov_b32 %1, body_k-.Lct%=\n\ts_mov_b32 %2, .Lbody_end-body_k"
                 : "=s"(pc), "=s"(off), "=s"(len));
    const unsigned o = ((bid_() >> 3) + lane * 32u) * 128u;
    touch1(sink, pc + off, o < len ? o : 0u);
}
template <int OFF> __device__ __forceinline__ bf16x8 lds_a(unsigned base) {
    static_assert(OFF >= 0 && OFF < 65536, "ds offset");
    return *(const LDS_AS bf16x8*)(size_t)(base + OFF);
}
template <int OFF> __device__ __forceinline__ s16x4 lds_h(unsigned base) {
    static_assert(OFF >= 0 && OFF < 65536, "ds offset");
    return __builtin_amdgcn_ds_read_tr16_b64_v4i16((LDS_AS s16x4*)(size_t)(base + OFF));
}
__device__ __forceinline__ bf16x8 cat(s16x4 lo, s16x4 hi) { return __builtin_shufflevector(lo, hi, 0, 1, 2, 3, 4, 5, 6, 7); }
__device__ __forceinline__ f32x4 mma(bf16x8 a, bf16x8 b, f32x4 c) {
    return __builtin_amdgcn_mfma_f32_16x16x32_bf16(a, b, c, 0, 0, 0);
}

__device__ __forceinline__ void mma_ip(bf16x8 a, bf16x8 b, f32x4& c) {
    asm volatile("v_mfma_f32_16x16x32_bf16 %0, %1, %2, %0" : "+v"(c) : "v"(a), "v"(b));
}
__device__ __forceinline__ void mma_z(bf16x8 a, bf16x8 b, f32x4& c) {
    asm volatile("v_mfma_f32_16x16x32_bf16 %0, %1, %2, 0" : "=&v"(c) : "v"(a), "v"(b));
}

constexpr int CSTR = 64 * 2 + 16;
struct ResC { unsigned rd, co, so; };
__device__ __forceinline__ void res_setup(ResC& c, unsigned lds, int m0, int n0, int tid) {
    const int row = (tid >> 6) * 16 + ((tid >> 3) & 7), ch = tid & 7;
    c.rd = opaque(lds + row * CSTR + ch * 16);
    c.co = opaque((unsigned)((m0 + row) * D_ + n0 + ch * 8) * 2u);
    c.so = opaque((unsigned)(((m0 + row) >> 5) * 512 + (n0 >> 6) * 32 + (row & 31) + (ch >> 2) * 8) * 8u);
}
__device__ __forceinline__ void res_tail(const ResC& c, u16* C, float* sp) {
    const u32x4 v = *(const LDS_AS u32x4*)(size_t)c.rd, w = *(const LDS_AS u32x4*)(size_t)(c.rd + 8 * CSTR);
    const __amdgpu_buffer_rsrc_t rc = __builtin_amdgcn_make_buffer_rsrc(C, 0, 0x7fffffff, 0x00020000);
    __builtin_amdgcn_raw_buffer_store_b128(v, rc, c.co, 0, 16);
    __builtin_amdgcn_raw_buffer_store_b128(w, rc, c.co, 8 * D_ * 2, 16);
    SCHED();
    if (sp == nullptr) return;
    unsigned s0, q0, s1, q1;
    asm("v_dot2_f32_bf16 %0, %4, %12, 0\n\tv_dot2_f32_bf16 %1, %4, %4, 0\n\tv_dot2_f32_bf16 %0, %5, %12, %0\n\tv_dot2_f32_bf16 %1, %5, %5, %1\n\t"
        "v_dot2_f32_bf16 %0, %6, %12, %0\n\tv_dot2_f32_bf16 %1, %6, %6, %1\n\tv_dot2_f32_bf16 %0, %7, %12, %0\n\tv_dot2_f32_bf16 %1, %7, %7, %1\n\t"
        "v_dot2_f32_bf16 %2, %8, %12, 0\n\tv_dot2_f32_bf16 %3, %8, %8, 0\n\tv_dot2_f32_bf16 %2, %9, %12, %2\n\tv_dot2_f32_bf16 %3, %9, %9, %3\n\t"
        "v_dot2_f32_bf16 %2, %10, %12, %2\n\tv_dot2_f32_bf16 %3, %10, %10, %3\n\tv_dot2_f32_bf16 %2, %11, %12, %2\n\tv_dot2_f32_bf16 %3, %11, %11, %3\n\t"
        "v_add_f32_dpp %0, %0, %0 row_half_mirror row_mask:0xf bank_mask:0x5\n\tv_add_f32_dpp %1, %1, %1 row_half_mirror row_mask:0xf bank_mask:0x5\n\t"
        "v_add_f32_dpp %0, %2, %2 row_half_mirror row_mask:0xf bank_mask:0xa\n\tv_add_f32_dpp %1, %3, %3 row_half_mirror row_mask:0xf bank_mask:0xa\n\t"
        "s_nop 0\n\tv_add_f32_dpp %0, %0, %0 quad_perm:[1,0,3,2] row_mask:0xf bank_mask:0xf\n\t"
        "v_add_f32_dpp %1, %1, %1 quad_perm:[1,0,3,2] row_mask:0xf bank_mask:0xf\n\t"
        "s_nop 0\n\tv_add_f32_dpp %0, %0, %0 quad_perm:[2,3,0,1] row_mask:0xf bank_mask:0xf\n\t"
        "v_add_f32_dpp %1, %1, %1 quad_perm:[2,3,0,1] row_mask:0xf bank_mask:0xf"
        : "=&v"(s0), "=&v"(q0), "=&v"(s1), "=&v"(q1)
        : "v"(v[0]), "v"(v[1]), "v"(v[2]), "v"(v[3]), "v"(w[0]), "v"(w[1]), "v"(w[2]), "v"(w[3]), "s"(0x3f803f80u));
    const __amdgpu_buffer_rsrc_t r = __builtin_amdgcn_make_buffer_rsrc(sp, 0, 0x7fffffff, 0x00020000);
    __builtin_amdgcn_raw_buffer_store_b64(u32x2{s0, q0}, r, c.so, 0, 16);
}
__device__ __forceinline__ bf16x8 pick(bool on, int e) {
    const unsigned one = on ? 0x3f80u << (e & 1) * 16 : 0u;
    bf16x8 m = __builtin_bit_cast(bf16x8, u32x4{(e >> 1) == 0 ? one : 0u, (e >> 1) == 1 ? one : 0u, (e >> 1) == 2 ? one : 0u, (e >> 1) == 3 ? one : 0u});
    asm volatile("" : "+v"(m));
    return m;
}

constexpr int ATX = 155648;
__device__ __forceinline__ void att_put(char* smem, int lane, int m0) {
    const int l15 = lane & 15, g = lane >> 4;
    u32x4 cx, cy, c0, ps;
#pragma unroll
    for (int r = 0; r < 4; ++r) {
        const int e = l15 - g * 4 - r, eh = ((l15 - g * 4) & ~1) - (r & 1) * 2;
        cx[r] = (e & 1) == 0 && e < 0 ? 0u : 0xff800000u;
        cy[r] = (e & 1) == 0 && e >= 0 ? 0u : 0xff800000u;
        c0[r] = m0 ? cx[r] : 0xff800000u;
        const unsigned hs = r & 1 ? 0x0302u : 0x0100u;
        ps[r] = (eh < 0) == (r < 2) ? (l15 & 1 ? hs << 16 | 0x0c0cu : 0x0c0c0000u | hs) : 0x0c0c0c0cu;
    }
    u32x4* const p = (u32x4*)(smem + ATX) + lane;
    p[0] = cx; p[64] = cy; p[128] = c0; p[192] = ps;
    *(unsigned*)(smem + 57088) = 0u;
}
using u32x8 = __attribute__((ext_vector_type(8))) unsigned;
#define AMM "v_mfma_f32_16x16x32_bf16 "
#define ACV "v_cvt_pk_bf16_f32 "
#define AX1(s) "v_max3_f32 v[" s "], v[" s "], v[" s "+1], v[" s "+4]\n\t"
#define AX2(s) "v_max3_f32 v[" s "+2], v[" s "+2], v[" s "+3], v[" s "+6]\n\t"
#define AX3(s) "v_max_f32 v[" s "], v[" s "], v[" s "+5]\n\t"
#define AX4(s) "v_max_f32 v[" s "+2], v[" s "+2], v[" s "+7]\n\t"
#define AOP(op, d, s) op " v" d ", v[" s "], v[" s "+2]\n\t"
#define AO2(op, d, e, a, b) op " v" d ", v" a ", v" b "\n\t" op " v" e ", v" a ", v" b "\n\t"
#define ASB(s, n) "v_sub_f32 v[" s "], v[" s "], v" n "\n\tv_sub_f32 v[" s "+2], v[" s "+2], v" n "\n\t"
#define AEX(s) "v_exp_f32 v[" s "], v[" s "]\n\t"
#define AMU(s, n) "v_mul_f32 v[" s "], v[" s "], v" n "\n\t"
#define APK(s) ACV "v[" s "+1], v[" s "], v[" s "+2]\n\t"
#define APM(s, i) "v_perm_b32 v[" s "+4+" i "], v[" s "+1], v[" s "+1], v[240+" i "]\n\t"
#define APV(o, v, s) AMM "v[" o ":" o "+3], v[" v ":" v "+3], v[" s "+4:" s "+7], 0\n\t"
#define AOC(s, o) ACV "v[" s "], v[" o "], v[" o "+1]\n\t" ACV "v[" s "+1], v[" o "+2], v[" o "+3]\n\t"
#define AST(s, off) "buffer_store_dwordx4 v[" s ":" s "+3], %[oo], %[rc], 0 offen offset:" off " sc1\n\t"
#define S16(a, b) "v_permlane16_swap_b32 v" a ", v" b "\n\t"
#define S32(a, b) "v_permlane32_swap_b32 v" a ", v" b "\n\t"
__device__ __forceinline__ void att_run(const u32x4 (&q)[4], const u32x4 (&k)[4], const f32x4 (&v)[3][2], const u32x2 (&v3)[2],
                                        const u32x4 (&am)[4], const f32x2 (&rk)[4], const f32x2 (&sc)[4], unsigned xa, unsigned oo, u16* C) {
    u32x4 kx;
    u32x8 s3, s2, w0, w1;
    asm volatile("ds_read_b128 %2, %3\n\tds_read_b64 v[164:165], %3 offset:1024\n\t"
                 AMM "v[228:231], %4, %6, %8\n\t" ACV "v166, %9, %10\n\t" ACV "v167, %11, %12\n\t" ACV "v168, %13, %14\n\t"
                 AMM "v[224:227], %5, %6, %7\n\t" ACV "v169, %15, %16\n\t" ACV "v170, %17, %18\n\t" ACV "v171, %19, %20"
                 : "=&{v[224:231]}"(s3), "=&{v[164:171]}"(w0), "=&v"(kx)
                 : "v"(xa), "v"(k[3]), "v"(k[2]), "v"(q[3]), "v"(am[0]), "v"(am[1]),
                   "v"(v[0][0][0]), "v"(v[0][0][1]), "v"(v[0][0][2]), "v"(v[0][0][3]), "v"(v[1][0][0]), "v"(v[1][0][1]),
                   "v"(v[1][0][2]), "v"(v[1][0][3]), "v"(v[2][0][0]), "v"(v[2][0][1]), "v"(v[2][0][2]), "v"(v[2][0][3]) : "memory");
    asm volatile("ds_read_b64 v[174:175], %2 offset:1032\n\t"
                 AMM "v[220:223], %3, %5, %7\n\t" ACV "v176, %8, %9\n\t" ACV "v177, %10, %11\n\t" ACV "v178, %12, %13\n\t"
                 AMM "v[216:219], %4, %5, %6\n\t" ACV "v179, %14, %15\n\t" ACV "v180, %16, %17\n\t" ACV "v181, %18, %19"
                 : "=&{v[216:223]}"(s2), "=&{v[174:181]}"(w1)
                 : "v"(xa), "v"(k[2]), "v"(k[1]), "v"(q[2]), "v"(am[0]), "v"(am[1]),
                   "v"(v[0][1][0]), "v"(v[0][1][1]), "v"(v[0][1][2]), "v"(v[0][1][3]), "v"(v[1][1][0]), "v"(v[1][1][1]),
                   "v"(v[1][1][2]), "v"(v[1][1][3]), "v"(v[2][1][0]), "v"(v[2][1][1]), "v"(v[2][1][2]), "v"(v[2][1][3]) : "memory");
    const __amdgpu_buffer_rsrc_t rc = __builtin_amdgcn_make_buffer_rsrc(C, 0, 0x7fffffff, 0x00020000);
    asm volatile(AMM "v[212:215], %[k1], %[q1], %[cy]\n\t" AX1("224") AX2("224") AX3("224")
                 AMM "v[208:211], %[k0], %[q1], %[cx]\n\t" AX4("224") AMU("224", "195") AMU("226", "199")
                 AMM "v[204:207], %[k0], %[q0], %[cy]\n\t" AOP("v_max_f32", "232", "224") AX1("216") AX2("216") "s_waitcnt lgkmcnt(0)\n\t"
                 AMM "v[200:203], %[kx], %[q0], %[c0]\n\t" AX3("216") AX4("216") AMU("216", "194") AMU("218", "198")
                 AOP("v_max_f32", "233", "216") AX1("208") AX2("208")
                 S16("232", "233") AX3("208") AO2("v_max_f32", "236", "237", "232", "233") AX4("208") AMU("208", "193")
                 S32("236", "237") AMU("210", "197") AO2("v_max_f32", "232", "233", "236", "237") AOP("v_max_f32", "234", "208") AX1("200")
                 S16("232", "233") AX2("200") AX3("200") AX4("200") ASB("224", "232") AMU("200", "192") AMU("202", "196")
                 AOP("v_max_f32", "235", "200") ASB("216", "233")
                 S16("234", "235") AEX("224") AEX("226") AO2("v_max_f32", "238", "239", "234", "235") AEX("216") AEX("218")
                 S32("238", "239") AOP("v_add_f32", "232", "224") AO2("v_max_f32", "234", "235", "238", "239")
                 AOP("v_add_f32", "233", "216") AMU("224", "187")
                 S16("234", "235") AMU("226", "191")
                 S16("232", "233") ASB("208", "234") AO2("v_add_f32", "236", "237", "232", "233") ASB("200", "235")
                 S32("236", "237") AEX("208") AEX("210") "v_add_f32 v232, v236, v237\n\t" AEX("200") "v_rcp_f32 v232, v232\n\t" AEX("202")
                 "v_mov_b32 v233, v232\n\t" AOP("v_add_f32", "234", "208") AOP("v_add_f32", "235", "200")
                 S16("232", "233") AMU("224", "232")
                 S16("234", "235") AMU("226", "232") AMU("216", "186") AO2("v_add_f32", "238", "239", "234", "235") AMU("218", "190") APK("224")
                 S32("238", "239") AMU("216", "233") "v_add_f32 v234, v238, v239\n\t" AMU("218", "233") "v_rcp_f32 v234, v234\n\t"
                 APM("224", "0") "v_mov_b32 v235, v234\n\t" APM("224", "1") APM("224", "2")
                 S16("234", "235") APM("224", "3") APK("216") AMU("208", "185")
                 APV("132", "170", "224") APM("216", "0") APM("216", "1") APM("216", "2")
                 APV("136", "180", "224") APM("216", "3") AMU("210", "189") AMU("208", "234")
                 APV("140", "168", "216") AMU("210", "234") AMU("200", "184") APK("208")
                 APV("144", "178", "216") APM("208", "0") APM("208", "1") APM("208", "2") APM("208", "3") AMU("202", "188") AMU("200", "235")
                 APV("148", "166", "208") AMU("202", "235") AOC("224", "132")
                 APV("152", "176", "208") APK("200") AOC("226", "136") AST("224", "3072")
                 APM("200", "0") APM("200", "1") APM("200", "2") APM("200", "3") AOC("216", "140")
                 APV("156", "164", "200") AOC("218", "144") AST("216", "2048")
                 APV("160", "174", "200") AOC("208", "148") AOC("210", "152") AST("208", "1024")
                 AOC("200", "156") "s_nop 0\n\t" AOC("202", "160") AST("200", "0") "s_nop 1"
                 : "+{v[224:231]}"(s3), "+{v[216:223]}"(s2)
                 : "{v[164:171]}"(w0), "{v[174:181]}"(w1), "{v[172:173]}"(v3[0]), "{v[182:183]}"(v3[1]),
                   [k1] "v"(k[1]), [k0] "v"(k[0]), [kx] "v"(kx), [q1] "v"(q[1]), [q0] "v"(q[0]),
                   [cx] "v"(am[0]), [cy] "v"(am[1]), [c0] "v"(am[2]), "{v[240:243]}"(am[3]),
                   "{v[184:185]}"(rk[0]), "{v[186:187]}"(rk[1]), "{v[188:189]}"(rk[2]), "{v[190:191]}"(rk[3]),
                   "{v[192:193]}"(sc[0]), "{v[194:195]}"(sc[1]), "{v[196:197]}"(sc[2]), "{v[198:199]}"(sc[3]), [oo] "v"(oo), [rc] "s"(rc)
                 : "memory", "v132", "v133", "v134", "v135", "v136", "v137", "v138", "v139", "v140", "v141", "v142", "v143", "v144", "v145",
                   "v146", "v147", "v148", "v149", "v150", "v151", "v152", "v153", "v154", "v155", "v156", "v157", "v158", "v159", "v160",
                   "v161", "v162", "v163", "v200", "v201", "v202", "v203", "v204", "v205", "v206", "v207", "v208", "v209", "v210",
                   "v211", "v212", "v213", "v214", "v215", "v232", "v233", "v234", "v235", "v236", "v237", "v238", "v239");
}

typedef unsigned long long u64;
typedef __attribute__((address_space(1))) u64 gu64;
template <class F> __device__ __forceinline__ void publish(unsigned* f, u64 tok, int wid, unsigned sink, F&& then) {
    asm volatile("s_cmp_eq_u32 %0, 0\n\ts_cselect_b64 exec, 1, 0\n\ts_waitcnt vmcnt(0)\n\ts_barrier\n\t"
                 "global_store_dwordx2 %1, %2, %3 sc1\n\ts_mov_b64 exec, -1" :: "s"(wid), "v"(0u), "v"(tok), "s"(f) : "memory", "scc");
    then();
    if (wid == 2) {
        code_touch(sink, opaque(threadIdx.x) & 63u);
        wait_vm<0>();
    }
}
template <int N> __device__ __forceinline__ void pubc(unsigned* f, u64 tok, unsigned cnt) {
    unsigned c = 1u;
    asm volatile("s_mov_b64 exec, 1\n\ts_waitcnt vmcnt(%5)\n\tds_add_rtn_u32 %0, %4, %0\n\ts_waitcnt lgkmcnt(0)\n\t"
                 "v_cmp_eq_u32 vcc, 3, %0\n\ts_cbranch_vccz 1f\n\tglobal_store_dwordx2 %1, %2, %3 sc1\n\ts_waitcnt vmcnt(1)\n\t"
                 "s_branch 2f\n1:\n\ts_waitcnt vmcnt(0)\n2:\n\ts_mov_b64 exec, -1"
                 : "+v"(c) : "v"(0u), "v"(tok), "s"(f), "v"(cnt), "n"(N) : "memory", "vcc");
}
__device__ __forceinline__ void poll(u64& have, u64 need, u64 at, const unsigned* f, u64 tok) {
    if (__builtin_expect((have & need) != need, 0)) {
        u64 v, t;
        unsigned n;
        asm volatile("s_andn2_b64 exec, %5, %0\n\ts_mov_b32 %3, 0x400000\n"
                     "1:\n\tglobal_load_dwordx2 %1, %6, off sc1\n\ts_sub_u32 %3, %3, 1\n\ts_cbranch_scc1 2f\n\ts_waitcnt vmcnt(0)\n\t"
                     "v_cmp_eq_u64 vcc, %7, %1\n\ts_or_b64 %0, %0, vcc\n\ts_andn2_b64 %2, %4, %0\n\ts_cbranch_scc1 1b\n"
                     "2:\n\ts_mov_b64 exec, -1"
                     : "+s"(have), "=&v"(v), "=&s"(t), "=&s"(n) : "s"(need), "s"(at), "v"(f), "s"(tok) : "memory", "vcc", "scc");
    }
}
__device__ __forceinline__ void look(u64& v, u64 at, const unsigned* f) {
    asm volatile("s_mov_b64 exec, %1\n\tglobal_load_dwordx2 %0, %2, off sc1\n\ts_mov_b64 exec, -1" : "=v"(v) : "s"(at), "v"(f) : "memory");
}
__device__ __forceinline__ u64 seen(u64& v, u64 at, u64 tok) {
    asm volatile("" : "+v"(v));
    return __ballot(v == tok) & at;
}
__device__ __forceinline__ void spin(u64& v, const void* base, unsigned off, u64 tok) {
    asm volatile("" : "+v"(v));
    if (__builtin_expect(__ballot(v != tok) != 0, 0)) {
        unsigned n;
        asm volatile("s_mov_b32 %1, 0x400000\n"
                     "1:\n\tglobal_load_dwordx2 %0, %2, %3 sc1\n\ts_sub_u32 %1, %1, 1\n\ts_waitcnt vmcnt(0)\n\ts_cbranch_scc1 2f\n\t"
                     "v_cmp_ne_u64 vcc, %4, %0\n\ts_cbranch_vccnz 1b\n2:"
                     : "=&v"(v), "=&s"(n) : "v"(off), "s"(base), "s"(tok) : "memory", "vcc", "scc");
    }
}
template <class M> __device__ __forceinline__ void await(M& have, M need, const unsigned* f, u64 tok) {
    if (__builtin_expect((have & need) != need, 0)) {
#pragma unroll 1
        for (unsigned spins = 0; spins < (1u << 22); ++spins) {
            have = (M)__ballot(__hip_atomic_load((gu64*)f, __ATOMIC_RELAXED, __HIP_MEMORY_SCOPE_AGENT) == tok);
            if ((have & need) == need) break;
            __builtin_amdgcn_s_sleep(1);
        }
        asm volatile("" ::: "memory");
    }
}
__device__ __forceinline__ unsigned gu_bsrc(int p, unsigned lane, int tn) {
    const unsigned rem = lane + (p < 14 ? p : p - 14) * 64, rowp = __umul24(rem, 4682u) >> 16;
    return __umul24(rowp ^ (rowp >> 1 & 4u), F_ * 2) + (rem * 16u + tn * 224u) - __umul24(rowp, 224u);
}
template <bool PERM = false>
__device__ __forceinline__ unsigned b_off(int h, int lane, int ld, int col) {
    const int row = h * 8 + (lane >> 3), cp = lane & 7;
    const int jj = (cp >> 1) ^ (((row >> 1) & 1) | ((row >> 2) & 2));
    return (unsigned)((PERM ? (lane >> 5) * 16 + h * 4 + (row & 3) : row) * ld + col + jj * 16 + (cp & 1) * 8) * 2u;
}

constexpr int DNO = F_ / 64 - NSOLO - DNE;
constexpr int dn_first(int n) { return (2 - n) & 3; }
static_assert(NSOLO == 4 && DNE < 28 && DNO < 28 && ((dn_first(DNE) ^ dn_first(DNO)) & 1) == 0, "k-tile program");
constexpr int DPX = DPAR == 0 ? 4 : (DPAR == 1 ? 1 : (DPAR == 2 ? 5 : (DPAR == 3 ? 7 : 3)));
__device__ __forceinline__ int dn_grp(int x) {
    return DPAR == 0 ? x & 3 : (DPAR == 1 ? x >> 1 : (DPAR == 4 ? (x >> 2) * 2 + ((x ^ x >> 1) & 1) : (x & 3) ^ (x >> 2) * (DPX & 3)));
}
constexpr int DNA = 65536, DNR = 98304;

constexpr int GBR = 3 * 32768, GBS = 224 * 128, GCS = GBR + 2 * GBS, GST = GCS + 224 * 4, GMX = DNR + 16384, GCN = 161024;
constexpr unsigned CSO = 4u << 20;
struct GuC { unsigned aoff[4], boff[4], tof, ab[2][2], bb[2][2], sto, cca, cc6, sta, gof, gofm, hsw, hsr, fo, cw, f1, f2; };
constexpr int gu_k(int i) { return i == 3 ? 4 : (i == 4 ? 3 : i); }
__device__ __forceinline__ void gu_setup(GuC& c, unsigned lds0, int wid, int id, bool first, unsigned fl) {
    const int tid = (int)opaque(threadIdx.x), lane = tid & 63;
    const int wm = wid >> 1, wn = wid & 1, tm = id & 7, tn = id >> 3, m0 = tm * 256;
    const int l15 = lane & 15, g = lane >> 4, q = l15 >> 2, p4 = l15 & 3;
#pragma unroll
    for (int i = 0; i < 4; ++i)
        c.aoff[i] = opaque((unsigned)((m0 + i * 64 + (tid >> 3)) * D_ + ((tid & 7) ^ ((tid >> 4) & 7)) * 8) * 2u);
    const int dk = DPAR ? (id >> 5) & 1 : id >> 7;
    if (wid < 4) {
        const unsigned j = (id & 31) * 256 + tid;
        unsigned rw = j >> 2;
        rw += rw < 1792 ? 0 : (dk ? DNE * 64 : DNO * 64);
        rw = (rw < F_ ? rw : 0) + (id >> 6) * 896;
        rw = rw < F_ ? rw : rw - F_;
        c.tof = opaque(rw * 2048 + 256 + (j & 1) * 128 + (j & 2) * 512);
    } else {
        c.tof = opaque((id * 224 + (tid < 480 ? tid - 256 : 0u)) * (first ? 128u : 0u));
    }
#pragma unroll
    for (int kk = 0; kk < 2; ++kk) {
        const unsigned sw = ((kk * 4 + g) ^ (l15 >> 1)) * 16;
        c.ab[kk][0] = opaque(lds0 + (wm * 64 + l15) * 128 + sw);
        c.ab[kk][1] = opaque(c.ab[kk][0] + 65536);
        const unsigned r = lds0 + GBR + ((g * 8 + kk * 4 + q) ^ (4 * (g & 1))) * 224;
        c.bb[kk][0] = opaque(r + wn * 128 + p4 * 8);
        c.bb[kk][1] = opaque(r + 96 + wn * 16 + (p4 & 1) * 8 + (p4 >> 1) * (GBS / 2));
    }
    c.sto = opaque(wid * 4096 + g * 1024 + l15 * 16);
    c.cca = opaque(lds0 + GCS + (wn * 64 + g * 4) * 4);
    c.cc6 = opaque(lds0 + GCS + (48 + wn * 8 + (g & 1) * 4 + (g >> 1) * 112) * 4);
    c.sta = opaque(lds0 + GST + wm * 1024 + (l15 & 1) * 256 + (l15 >> 1) * 16);
    c.gof = opaque(((m0 + wm * 64) / 4 + (g & 1) * 4 + q) * (8 * F_) + (tn * 7 + wn * 4) * 128 + p4 * 32 + (g >> 1) * 16);
    c.gofm = opaque(((m0 + wm * 64 + wn * 32) / 4 + (lane >> 3)) * (8 * F_) + (tn * 7 + 3) * 128 + (lane & 7) * 16);
    c.hsw = opaque(lds0 + GMX + (wm * 64 + (g >> 1) * 16 + l15) * 32 + wn * 16 + (g & 1) * 8);
    c.hsr = opaque(lds0 + GMX + wid * 1024 + lane * 16);
    const int pid = id ^ (DPX * 32), own = ((id / 64) * 4 + id % 4) * 16 + (id % 64) / 4, set = (first ? 7 : 5) * 256 + (own & ~15);
    const int w1 = (tm * 2 + wn) * 16 + wm + (wm == 3), j = wid * 4 + (lane & 3);
    c.f1 = opaque(fl + (w1 == own ? set : w1) * 128u);
    const int w2 = j < 24 ? (tm * 2 + (j & 1)) * 16 + 4 + j / 2 - (j < 2)
                          : (j & 1 ? ((pid / 64) * 4 + pid % 4) * 16 + (pid % 64) / 4 : (dn_grp(id >> 5) * 4 + (id & 3)) * 16 + ((id >> 2) & 7) * 2 + dk);
    c.f2 = opaque(fl + w2 * 128u);
    const int col = (wid < 7 ? wid : 6) * 32 + (lane & 7) * 4;
    c.fo = opaque((unsigned)((lane >> 3) * 2 * F_ + (col < 112 ? col : F_ + col - 112) + tn * 112) * 4u);
    c.cw = opaque(lds0 + GCS + (col + g) * 4);
}

constexpr int WOB = 65536, WONB = 12, QCN = 57088;
constexpr unsigned BOXO = 36u << 20;
__device__ __forceinline__ int wo_k(int i) { return (int)(0xb3fedca987654210ull >> (4 * i)) & 15; }
__device__ __forceinline__ unsigned wo_boff(int h, int lane) {
    return b_off<true>(h, lane, D_, (((wg_id()) % 64) / 4) * 64);
}
__device__ __forceinline__ void wo_stage_b(unsigned lds0, const u16* B, unsigned boff, int h, int step) {
#pragma unroll
    for (int i = 0; i < 2; ++i)
        glds16(uni((const char*)B + (wo_k(step) * 64 + i * 32) * D_ * 2), boff,
               lds0 + WOB + (step < WONB ? step : step - WONB) * 8192 + h * 1024 + i * 4096);
}
__device__ __forceinline__ void wo_phase(char* smem, const u16* A, const u16* __restrict__ B, u16* C, float* sp,
                                         unsigned* pflags, bool first, const u16* __restrict__ wg,
                                         const u16* __restrict__ wu, const float* cs, unsigned fl, GuC& gc, f32x4& cp,
                                         u64& lk, u64 tok) {
    constexpr int NK = 16, ASZ = 16384;
    const int tid = (int)opaque(threadIdx.x), lane = tid & 63;
    const int wid = __builtin_amdgcn_readfirstlane(tid >> 6);
    const int id = wg_id();
    const int tm = (id / 64) * 4 + id % 4, tn = (id % 64) / 4;
    const int m0 = tm * 128, n0 = tn * 64;
    const unsigned lds0 = __builtin_amdgcn_readfirstlane((unsigned)(size_t)(LDS_AS char*)smem);
    ResC rc;
    unsigned goff[7] = {0u, 0u, 0u, 0u, 0u, 0u, 0u};
    auto stage_g = [&](int i, int kt) INL {
        const int p = wid - 4 + 4 * i;
        glds16(uni((const char*)(p >= 14 ? wu : wg) + kt * 64 * F_ * 2), goff[i], lds0 + 98304 + kt * 28672 + p * 1024);
    };
    if (wid >= 4) {
        const int h = wid - 4;
        const unsigned aoff = (unsigned)(m0 * D_ * 2 + (h & 1) * 4096 + (h >> 1) * 1024 + lane * 16);
        const unsigned boff = wo_boff(h, lane);
#pragma unroll
        for (int i = 0; i < 7; ++i) goff[i] = opaque(gu_bsrc(h + 4 * i, opaque(lane), id >> 3));
        res_setup(rc, lds0 + ASZ, m0, n0, tid);
        gu_setup(gc, lds0, wid, id, first, fl);
        const int l = lane & 15, cb = (id >> 3) * 7 / 4 + l % 3;
        const int job = ((cb < 56 ? cb : 55) + (l >= 6 ? 56 : 0)) * 2 + (l / 3 & 1), oid = job + 8 * ((job + 32) / 56);
        const unsigned* const f = pflags + (lane < 16 ? tm * 16 + l : ((oid / 128) * 8 + oid % 8) * 16 + (oid % 128) / 8) * 32;
        const u64 all = first && h == 0 ? 0x0fffffffu : 0xffffu;
        u64 have = 0, at = WLK == 2 ? 3 : 0x17, lk2;
        auto stage = [&](int st, auto bc) INL {
            poll(have, 1u << wo_k(st), at, f, tok);
#pragma unroll
            for (int i = 0; i < 4; ++i)
                glds16_dev(uni((const char*)A + wo_k(st) * 8192 + (i >> 1) * 64 * D_ * 2 + (i & 1) * 2048), aoff,
                           lds0 + ((st + 1) & 3) * ASZ + h * 1024 + i * 4096);
            if constexpr (decltype(bc)::value) wo_stage_b(lds0, B, boff, h, st);
        };
        stage(0, CT<0>{});
        if constexpr (WL2 == 0) look(lk2, all, f);
        stage(1, CT<0>{});
        wait_vm<4>();
        BARRIER();
        if constexpr (WL2 == 0) { at = all; have |= seen(lk2, at, tok); }
        if constexpr (WL2 == 1) look(lk2, all, f);
        stage(2, CT<0>{});
        stage(3, CT<0>{});
        if constexpr (WL2 == 1) { wait_vm<8>(); at = all; have |= seen(lk2, at, tok); }
#pragma unroll 1
        for (int st = 4; st < WONB; ++st) {
            wait_vm<8>();
            BARRIER();
            stage(st, CT<0>{});
        }
#pragma unroll 1
        for (int st = WONB; st < NK; ++st) {
            wait_vm<8>();
            BARRIER();
            stage(st, CT<1>{});
        }
        poll(have, all, at, f, tok);
        wait_vm<12>();
        BARRIER();
        stage_g(0, 0); stage_g(1, 0); stage_g(2, 0);
        wait_vm<9>();
        BARRIER();
        stage_g(3, 0); stage_g(4, 0);
        wait_vm<5>();
        BARRIER();
        stage_g(5, 0); stage_g(6, 0);
        if (h == 3) code_touch(SINK, lane);
#pragma unroll
        for (int i = 0; i < 4; ++i) gc.boff[i] = goff[i < 3 ? 2 * i + 1 : 0];

    } else {
        const int wm = wid >> 1, wn = wid & 1;
        const int l15 = lane & 15, g = lane >> 4, q = l15 >> 2, p4 = l15 & 3;
        const unsigned ab = opaque(lds0 + wm * 8192 + lane * 16);
        unsigned bb[4];
#pragma unroll
        for (int x = 0; x < 4; ++x)
            bb[x] = opaque(lds0 + WOB + (g * 8 + q + ((x & 1) ^ wm) * 4) * 128 + p4 * 8 + (((wn * 2 + x / 2) ^ ((q >> 1) | ((g & 1) << 1))) << 5));
        f32x4 acc[4][2];
        {
            const unsigned ra = lds0 + (wm * 64 + l15) * 128 + (((wn * 4 + g) ^ (l15 >> 1)) * 16);
            const bf16x8 sel[2] = {pick(g == (l15 >> 3), l15 & 7), pick(g == 2 + (l15 >> 3), l15 & 7)};
            static_for<0, 4>([&](auto ic) INL {
                constexpr int i = decltype(ic)::value;
                const bf16x8 rr = lds_a<i * 2048>(ra);
#pragma unroll
                for (int j = 0; j < 2; ++j) {
                    acc[i][j] = mma(sel[j], rr, f32x4{0.f, 0.f, 0.f, 0.f});
                    asm volatile("" : "+v"(acc[i][j]));
                }
            });
        }
        res_setup(rc, lds0 + ASZ, m0, n0, tid);
        const unsigned cw = opaque(lds0 + ASZ + (wm * 64 + l15) * CSTR + (wn * 32 + g * 4) * 2);
        SCHED();
        pubc<0>(pflags + (((id / 128) * 8 + id % 8) * 16 + (id % 128) / 8) * 32, tok, lds0 + QCN);
        gu_setup(gc, lds0, wid, id, first, fl);
#pragma unroll
        for (int i = 0; i < 4; ++i) gc.boff[i] = opaque(gu_bsrc(wid + 8 * i, lane, id >> 3));
        auto ldop = [&](bf16x8 (&af)[8], s16x4 (&bf)[8], auto ktc, auto oc) INL {
            constexpr int KT = decltype(ktc)::value, O = decltype(oc)::value;
            if constexpr (O == 2 || (O >= 5 && O < 12)) {
                constexpr int X = O == 2 ? 0 : O - 4;
                af[(X & 1) * 4 + X / 2] = lds_a<((KT + 1) % 4) * ASZ + X * 1024>(ab);
            } else {
                constexpr int X = O < 2 ? O : (O < 5 ? O - 1 : O - 8), J = X / 4;
                bf[X] = lds_h<(KT % 4) * 8192 + ((X >> 1) & 1) * 4096>(bb[J * 2 + (X & 1)]);
            }
        };
        auto ktile = [&](bf16x8 (&af)[8], s16x4 (&bf)[8], bf16x8 (&an)[8], s16x4 (&bn)[8], auto nktc) INL {
            static_for<0, 16>([&](auto tc) INL {
                constexpr int T = decltype(tc)::value, j = T / 8, i = j ? 3 - (T / 2) % 4 : (T / 2) % 4, kk = T & 1;
                if constexpr (T == WOBAR && decltype(nktc)::value < NK) {
                    WAIT_LGKM0();
                    BARRIER();
                }
                mma_ip(cat(bf[j * 4 + kk * 2], bf[j * 4 + kk * 2 + 1]), af[kk * 4 + i], acc[i][j]);
                SCHED();
                if constexpr (decltype(nktc)::value < NK && T >= WOBAR) {
                    constexpr int U = T - WOBAR, lo_ = (U * 16 + WOLD - 1) / WOLD, hi_ = ((U + 1) * 16 + WOLD - 1) / WOLD;
                    static_for<lo_, (hi_ < 16 ? hi_ : 16)>([&](auto oc) INL { ldop(an, bn, nktc, oc); });
                }
                SCHED();
            });
        };
        bf16x8 a0[8], a1[8];
        s16x4 b0[8], b1[8];
        BARRIER();
        static_for<0, 16>([&](auto oc) INL { ldop(a0, b0, CT<0>{}, oc); });
#pragma unroll 1
        for (int it = 0; it < 3; ++it) {
            ktile(a0, b0, a1, b1, CT<1>{});
            ktile(a1, b1, a0, b0, CT<2>{});
            ktile(a0, b0, a1, b1, CT<3>{});
            const unsigned d = it == 2 ? -65536 : 32768;
            bb[0] += d; bb[1] += d; bb[2] += d; bb[3] += d;
            ktile(a1, b1, a0, b0, CT<4>{});
        }
        ktile(a0, b0, a1, b1, CT<13>{});
        ktile(a1, b1, a0, b0, CT<14>{});
        ktile(a0, b0, a1, b1, CT<15>{});
        ktile(a1, b1, a0, b0, CT<16>{});
#pragma unroll
        for (int t = 0; t < 8; ++t) {
            const int j = t / 4, i = j ? 7 - t : t;
            *(LDS_AS u32x2*)(size_t)(cw + i * 16 * CSTR + j * 32) = u32x2{pk2(acc[i][j][0], acc[i][j][1]), pk2(acc[i][j][2], acc[i][j][3])};
            SCHED();
        }
    }
    WAIT_LGKM0();
    BARRIER();
    res_tail(rc, C, sp);
    asm volatile("global_load_dwordx2 %0, %2, %4 sc1\n\tglobal_load_dwordx4 %1, %3, %4 sc1"
                 : "=&{v[194:195]}"(lk), "=&{v[196:199]}"(cp) : "v"(gc.f1), "v"(gc.fo), "s"(cs) : "memory");
}

constexpr int QAS = 144 * 128, QBS = 192 * 128, QBR = 57344;
constexpr int QSK = 3 * QAS, QCS = 159744, QGR = QAS, QXC = 57344;
constexpr int qsl(int kt) { return (kt + 2) % 3; }
__device__ __forceinline__ int qkv_k(int i) { return (int)(0xb3a2fedc98765410ull >> (4 * i)) & 15; }
__device__ __forceinline__ unsigned qkv_boff(int h, int lane) {
    return b_off(h, lane, 3 * D_, (((wg_id()) % 128) / 8) * 64);
}
__device__ __forceinline__ void qkv_stage_b(unsigned lds0, const u16* B, unsigned boff, int h, int kt, int sl) {
#pragma unroll
    for (int i = 0; i < 6; ++i)
        glds16(uni((const char*)B + ((kt * 64 + (i & 1) * 32) * 3 * D_ + (i >> 1) * D_) * 2), boff,
               lds0 + QBR + sl * QBS + h * 1024 + i * 4096);
}
__device__ __forceinline__ void qkv_phase(char* smem, const u16* A, const u16* __restrict__ B, u16* C, float* cs,
                                          const float* sp, const unsigned* pflags, unsigned* oflags,
                                          const u16* __restrict__ wo, bool first, const u16* __restrict__ wg,
                                          const u16* __restrict__ wu, u64* epoch, u64& tok) {
    constexpr int NK = 16;
    const int tid = (int)opaque(threadIdx.x), lane = tid & 63;
    const int wid = __builtin_amdgcn_readfirstlane(tid >> 6);
    const int id = wg_id();
    const int tm = (id / 128) * 8 + id % 8, tn = (id % 128) / 8;
    const int m0 = tm * 128;
    const unsigned lds0 = __builtin_amdgcn_readfirstlane((unsigned)(size_t)(LDS_AS char*)smem);
    float* const csl = (float*)(smem + QCS);
    float* const cso = cs + 16 * F_ + id * 192;
    auto token = [&]() INL {
        asm volatile("s_load_dwordx2 %0, %1, 0x0\n\ts_waitcnt lgkmcnt(0)" : "=s"(tok) : "s"(epoch) : "memory");
        tok += 0x9e3779b97f4a7c15ull;
    };
    if (wid >= 4) {
        const int h = wid - 4, hl = tid - 256;
        const int row = h * 8 + (lane >> 3), hr = h * 4 + ((lane >> 3) & 3);
        const unsigned la = (unsigned)((m0 + row) * D_ + ((lane & 7) ^ ((row >> 1) & 7)) * 8) * 2u;
        const unsigned lh = (unsigned)((tm ? m0 - 16 + hr : 0) * D_ + ((lane & 7) ^ ((hr >> 1) & 7)) * 8) * 2u;
        const unsigned lb = qkv_boff(h, lane);
        const unsigned wb = wo_boff(h, lane);
        constexpr auto wn = [](int i) { return i == 0 ? 2 : (i == 1 ? WN1 : (i == 2 ? WN2 : (i == 3 ? WN3 : (i == 4 ? WN4 : 10 - WN1 - WN2 - WN3 - WN4)))); };
        static_assert(WN1 + WN2 <= 6 && WN1 + WN2 + WN3 <= 9, "free slots");
        auto wo_pre = [&](int i) INL {
            int e0 = 0;
#pragma unroll
            for (int x = 0; x < i; ++x) e0 += wn(x);
#pragma unroll
            for (int e = e0; e < e0 + wn(i); ++e) wo_stage_b(lds0, wo, wb, h, (int)(0xb765a9843210ull >> (4 * e)) & 15);
        };
        const int wtile = ((id / 64) * 4 + id % 4) * 16 + (id % 64) / 4;
        auto wo_res = [&]() INL {
#pragma unroll
            for (int i = 0; i < 4; ++i)
                glds16_dev(uni((const char*)A + (((wtile >> 4) - tm) * 128 + i * 32) * D_ * 2 + (wtile & 15) * 128), la, lds0 + h * 1024 + i * 4096);
        };
        auto wo_slow = [&]() INL {
            const unsigned bx = bid_();
            if (h == 1) touch1(SINK, wo, ((bx >> 3) * 32 + (lane & 31)) * 2048 + ((bx & 1) * 8 + 3) * 128);
        };
        auto marks = [&]() INL {
            const unsigned em = opaque(~0u), bo = BOXO + bid_() * 16384u + (unsigned)hl * 16u;
            const __amdgpu_buffer_rsrc_t r = __builtin_amdgcn_make_buffer_rsrc(C, 0, 0x7fffffff, 0x00020000);
#pragma unroll
            for (int i = 0; i < 4; ++i) __builtin_amdgcn_raw_buffer_store_b128(u32x4{em, em, em, em}, r, bo, i * 4096, 16);
        };
        auto put_stat = [&](float sum, float sq, int row, bool on) INL {
            const float mean = mulf(sum, 1.f / D_);
            const float var = fmaxf(subf(mulf(sq, 1.f / D_), mulf(mean, mean)), 0.f);
            const float rs = __builtin_amdgcn_rcpf(rbf(__builtin_amdgcn_sqrtf(rbf(addf(rbf(var), 1e-5f))))), nm = -rbf(mean);
            if (on) {
                *(float*)(smem + QSK + row * 4) = nm;
                *(float*)(smem + QSK + 576 + row * 4) = rs * (0.17677669529663687f * 1.44269504088896341f);
                *(float*)(smem + QSK + 1152 + row * 4) = rs;
            }
        };
        if (first) {
            const unsigned long long hmask = h == 0 ? 0xffffff00ull : 0xffffffffull;
            auto piece = [&](int kt, int p) INL {
                const unsigned da = lds0 + qsl(kt) * QAS;
                if (p < 4) glds16(uni((const char*)A + kt * 128 + p * 32 * D_ * 2), la, da + h * 1024 + p * 4096);
                else if (p == 4) glds16_mask(uni((const char*)A + kt * 128), lh, da + 16384 + h * 512, hmask);
                else glds16(uni((const char*)B + ((kt * 64 + ((p - 5) & 1) * 32) * 3 * D_ + ((p - 5) >> 1) * D_) * 2), lb,
                            lds0 + QBR + qsl(kt) * QBS + h * 1024 + (p - 5) * 4096);
            };
            __builtin_amdgcn_s_setprio(HPRIO);
            constexpr unsigned tw = SINK;
            const unsigned one2 = opaque(0x3f803f80u);
            if (hl < 24) *(u32x4*)(smem + (hl >> 3) * QAS + 128 * 128 + (hl & 7) * 16) = u32x4{one2, one2, one2, one2};
            const bool idle = (tn & 7) == 3;
            const int job = idle ? 0 : id - 8 * ((id + 32) >> 6);
            const int cb = job >> 1, kh = job & 1;
            const char* const wp = uni((const char*)((cb < 56 ? wg : wu) + (long long)kh * 512 * F_ + (cb % 56) * 64));
            const unsigned wpo = (unsigned)((idle ? 0 : hl >> 3) * F_ + (hl & 7) * 8) * 2u;
            const int wst = idle ? 0 : 64 * F_;
            f32x4 wv[NK];
            float sm[8];
#pragma unroll
            for (int e = 0; e < 8; ++e) sm[e] = 0.f;
            auto wadd1 = [&](int r, int e) INL {
                float t0, t1;
                asm volatile("v_lshlrev_b32 %2, 16, %4\n\tv_and_b32 %3, 0xffff0000, %4\n\tv_add_f32 %0, %0, %2\n\tv_add_f32 %1, %1, %3"
                             : "+v"(sm[2 * e]), "+v"(sm[2 * e + 1]), "=&v"(t0), "=&v"(t1) : "v"(wv[r][e]));
            };
            auto wadd = [&](int r) INL {
#pragma unroll
                for (int e = 0; e < 4; ++e) wadd1(r, e);
            };
            const unsigned bx = bid_();
            const int kq = bx >> 4;
            auto sweep = [&]() INL {
                touch1(tw, B, (unsigned)(bx * 192 + (hl < 192 ? hl : 0)) * 128u);
                touch1(tw, A, (unsigned)(((bx & 15) * 128 + (hl & 127)) * 2048 + kq * 128));
            };
            if (kq < KIN && kq >= KSKIP) {
                if (h >= 2) {
                    for (int i = (kq - KSKIP + 1) * SLP; i > 0; --i) __builtin_amdgcn_s_sleep(1);
                    touch1(tw, B, (unsigned)(bx * 192 + (hl & 127)) * 128u);
                    touch1(tw, A, (unsigned)(((bx & 15) * 128 + (hl & 127)) * 2048 + kq * 128));
                    if (h == 2) touch1(tw, B, (unsigned)(bx * 192 + hl) * 128u);
                }
            }
            token();
            static_for<0, NK>([&](auto kc) INL {
                constexpr int KT = decltype(kc)::value;
                constexpr auto ex = [](int k) { return (k >= WV0 && k < WV0 + 16 / WVN ? WVN : 0) + (k == PV0 ? 1 : 0) + (k == NK - 2 ? 8 : 0); };
                wait_vm<(KT + 1 < NK ? 11 : 0) + ex(KT - 2) + ex(KT - 1)>();
                BARRIER();
                static_for<0, 11>([&](auto pc) INL {
                    constexpr int P = decltype(pc)::value;
                    if constexpr (KT + 2 < NK) piece(KT + 2, P);
                    if constexpr (KT - 3 >= WV0 && KT - 3 < WV0 + 16 / WVN && P < 8) {
#pragma unroll
                        for (int u = 0; u < WVN / 2; ++u) wadd1((KT - 3 - WV0) * WVN + u * 2 + P / 4, P % 4);
                    }
                    SCHED();
                });
                if constexpr (KT + KIN < NK) { if (kq == KT + KIN) sweep(); }
                if constexpr (KT >= WV0 && KT < WV0 + 16 / WVN) {
#pragma unroll
                    for (int u = 0; u < WVN; ++u) gload16<0>(wv[(KT - WV0) * WVN + u], uni(wp + ((KT - WV0) * WVN + u) * wst), wpo);
                }
                if constexpr (KT == PV0) touch1(tw, wo, (unsigned)(bx * 64 + (hl & 63)) * 128u);
                if constexpr (KT == WT0) wo_slow();
                if constexpr (KT == NK - 2) { wo_res(); wo_pre(0); }
                if constexpr (KT == 7) { if (h == 0) att_put(smem, lane, m0); }
                if constexpr (KT == 4) {
                    if (h == 0) touch1(tw, oflags - 7 * 8192, ((lane < 12 ? lane : 0) * 256 + bx) * 128u);
                }
                WAIT_LGKM0();
            });
            auto colsum = [&]() INL {
#pragma unroll
            for (int r = NK - 3 > WV0 ? (NK - 3 - WV0) * WVN : 0; r < NK; ++r) wadd(r);
            asm volatile("s_nop 1\n\tv_add_f32_dpp %0, %0, %0 row_ror:8 row_mask:0xf bank_mask:0xf\n\tv_add_f32_dpp %1, %1, %1 row_ror:8 row_mask:0xf bank_mask:0xf\n\t"
                         "v_add_f32_dpp %2, %2, %2 row_ror:8 row_mask:0xf bank_mask:0xf\n\tv_add_f32_dpp %3, %3, %3 row_ror:8 row_mask:0xf bank_mask:0xf\n\t"
                         "v_add_f32_dpp %4, %4, %4 row_ror:8 row_mask:0xf bank_mask:0xf\n\tv_add_f32_dpp %5, %5, %5 row_ror:8 row_mask:0xf bank_mask:0xf\n\t"
                         "v_add_f32_dpp %6, %6, %6 row_ror:8 row_mask:0xf bank_mask:0xf\n\tv_add_f32_dpp %7, %7, %7 row_ror:8 row_mask:0xf bank_mask:0xf\n\t"
                         "s_nop 1\n\tv_permlane16_swap_b32 %0, %4\n\tv_permlane16_swap_b32 %1, %5\n\tv_permlane16_swap_b32 %2, %6\n\t"
                         "v_permlane16_swap_b32 %3, %7\n\tv_add_f32 %0, %0, %4\n\tv_add_f32 %1, %1, %5\n\tv_add_f32 %2, %2, %6\n\t"
                         "v_add_f32 %3, %3, %7\n\ts_nop 1\n\tv_permlane32_swap_b32 %0, %2\n\tv_permlane32_swap_b32 %1, %3\n\t"
                         "v_add_f32 %0, %0, %2\n\tv_add_f32 %1, %1, %3"
                         : "+v"(sm[0]), "+v"(sm[1]), "+v"(sm[2]), "+v"(sm[3]), "+v"(sm[4]), "+v"(sm[5]), "+v"(sm[6]), "+v"(sm[7]));
            if ((lane & 8) == 0 && !idle) {
                const __amdgpu_buffer_rsrc_t r = __builtin_amdgcn_make_buffer_rsrc(cs, 0, 0x7fffffff, 0x00020000);
                __builtin_amdgcn_raw_buffer_store_b64(u32x2{__float_as_uint(sm[0]), __float_as_uint(sm[1])}, r,
                    (unsigned)((kh * 4 + h) * 2 * F_ + cb * 64 + (lane & 7) * 8 + ((lane >> 4) & 1) * 4 + (lane >> 5) * 2) * 4u, 0, 16);
            }
            };
            if constexpr (CSP == 1) { if constexpr (WV0 > 5) wait_vm<(WV0 > 7 ? 0 : 8)>(); colsum(); }
            wo_pre(1);
            BARRIER();
            {
                const int l = lane & 15, gi = h * 3 + (lane < 48 ? lane >> 4 : 0), mr = gi * 15 + l - 1;
                const float* const raw = (const float*)(smem + QGR + gi * 1024);
                put_stat(raw[l * 4], raw[((l >> 2) * 16 + l) * 4 + (l & 3)], l == 0 ? 0 : (gi < 9 ? 16 + (mr < 128 ? mr : 127) : l), gi < 10);
            }
            __builtin_amdgcn_s_setprio(0);
            if constexpr (GW2 == 0) wo_pre(2);
            WAIT_LGKM0();
            BARRIER();
            if constexpr (GW2 == 1) wo_pre(2);
            if (hl < 48) store16_wt(cso, (unsigned)hl * 16u, *(const u32x4*)(csl + hl * 4));
            wo_pre(3);
            if constexpr (CSP == 2) { wait_vm<2 * (WN1 + WN2 + WN3)>(); colsum(); }
            marks();
            if (h == 3) code_touch(51200, lane);
            wait_vm<20>();
            BARRIER();
            if constexpr (CSP == 3) { wait_vm<2 * (WN1 + WN2 + WN3) + (MKP == 0 ? 4 : 0)>(); colsum(); }
        } else {
            const unsigned* const f = pflags + (lane < 32 ? (tm && (lane & 16) ? tm - 1 : tm) * 16 + (lane & 15) : wtile) * 32;
            u64 have = 0;
            auto stage = [&](int st, int sl, auto bc) INL {
                const int kt = qkv_k(st);
                await(have, 0x10001ull << kt, f, tok);
                const unsigned da = lds0 + sl * QAS;
#pragma unroll
                for (int i = 0; i < 4; ++i) glds16_dev(uni((const char*)A + kt * 128 + i * 32 * D_ * 2), la, da + h * 1024 + i * 4096);
                glds16_part<32>(uni((const char*)A + kt * 128), lh, da + 16384 + h * 512);
                if constexpr (decltype(bc)::value) qkv_stage_b(lds0, B, lb, h, kt, sl);
            };
            {
                u64 lk;
                gload8_dev(lk, f);
                qkv_stage_b(lds0, B, lb, h, qkv_k(2), qsl(2));
                wait_vm<6>();
                asm volatile("" : "+v"(lk));
                have = __ballot(lk == tok);
            }
            stage(0, qsl(0), CT<0>{});
            stage(1, qsl(1), CT<0>{});
            wait_vm<5>();
            BARRIER();
            stage(2, qsl(2), CT<0>{});
            if (h == 0) att_put(smem, lane, m0);
            wait_vm<5>();
            BARRIER();
            stage(3, qsl(3), CT<1>{});
            if (h == 0) glds16_dev(uni((const char*)cso), lane * 16u, lds0 + QCS);
            int sl = qsl(4);
#pragma unroll 1
            for (int st = 4; st < NK; ++st) {
                wait_vm<11>();
                BARRIER();
                stage(st, sl, CT<1>{});
                if (__builtin_expect(st == WT1, 0)) wo_slow();
                sl = sl == 2 ? 0 : sl + 1;
            }
            f32x4 spv[8];
            await(have, 1ull << 32, f, tok);
            {
                const int srow = m0 - 16 + (hl < 144 ? hl & ~1 : 0);
                const int sr = srow < 0 ? 0 : srow;
                const unsigned so = (unsigned)((sr >> 5) * 512 + (hl & 1) * 256 + (sr & 31)) * 8u;
                gload16_dev(spv[0], sp, so);
                static_for<1, 8>([&](auto ic) INL { gload16_devo<decltype(ic)::value * 256>(spv[decltype(ic)::value], sp, so); });
            }
            wait_vm<19>();
            BARRIER();
            wo_res();
            wo_pre(0);
            wait_vm<16>();
            BARRIER();
            if constexpr (QSP) __builtin_amdgcn_s_setprio(QSP);
            wait_vm<8>();
            float sm[4];
#pragma unroll
            for (int i = 0; i < 8; ++i) {
                asm volatile("" : "+v"(spv[i]));
#pragma unroll
                for (int e = 0; e < 4; ++e) sm[e] = i ? addf(sm[e], spv[i][e]) : spv[i][e];
            }
#pragma unroll
            for (int e = 0; e < 4; ++e)
                sm[e] = addf(sm[e], __uint_as_float(__builtin_amdgcn_update_dpp(0u, __float_as_uint(sm[e]), 0xB1, 0xf, 0xf, false)));
            put_stat(hl & 1 ? sm[2] : sm[0], hl & 1 ? sm[3] : sm[1], hl, hl < 144);
            if constexpr (QSP) __builtin_amdgcn_s_setprio(0);
            if constexpr (QSO == 0) wo_pre(1);
            WAIT_LGKM0();
            BARRIER();
            if constexpr (QSO == 1) wo_pre(1); else wo_pre(2);
            BARRIER();
            if constexpr (QSO == 1) wo_pre(2);
            wo_pre(3);
            marks();
            if (h == 3) code_touch(51200, lane);
            wait_vm<20>();
            BARRIER();
        }
        static_assert(WN1 + WN2 + WN3 == 8 && WN4 == 2 && CSP == 1, "counts");
        wo_pre(4);
    } else {
        const int wm = wid >> 1, wn = wid & 1;
        const int l15 = lane & 15, g = lane >> 4, q = l15 >> 2, p4 = l15 & 3;
        unsigned ab[2], hb[2], bj[6];
#pragma unroll
        for (int kk = 0; kk < 2; ++kk) {
            const unsigned sw = ((kk * 4 + g) ^ (l15 >> 1)) * 16;
            ab[kk] = opaque(lds0 + (wm * 64 + l15) * 128 + sw);
            hb[kk] = opaque(lds0 + (128 + l15) * 128 + sw);
        }
#pragma unroll
        for (int j = 0; j < 6; ++j)
            bj[j] = opaque(lds0 + QBR + (j >> 1) * 8192 + (g * 8 + q) * 128 + p4 * 8 + (((wn * 2 + ((j & 1) ^ wm)) ^ ((q >> 1) | ((g & 1) << 1))) << 5));
        f32x4 acc[5][6];
        constexpr auto qcol = [](int c) { return c < 3 ? 2 * c + 1 : 2 * c - 6; };
        auto ldop = [&](bf16x8 (&af)[10], s16x4 (&bf)[24], auto ktc, auto oc) INL {
            constexpr int KT = decltype(ktc)::value, O = decltype(oc)::value;
            if constexpr (O == 2 || (O >= 5 && O < 12)) {
                constexpr int X = O == 2 ? 0 : O - 4;
                af[X] = lds_a<qsl(KT) * QAS + (X / 2) * 2048>(ab[X & 1]);
            } else if constexpr (O == 24 || O == 25) {
                af[O - 16] = lds_a<qsl(KT) * QAS>(hb[O & 1]);
            } else {
                constexpr int X = O < 2 ? O : (O < 5 ? O - 1 : (O < 24 ? O - 8 : O - 10));
                bf[X] = lds_h<qsl(KT) * QBS + ((X >> 1) & 1) * 4096 + (X & 1) * 512>(bj[qcol(X / 4)]);
            }
        };
        unsigned ha[3][2];
        f32x4 rg[3];
        bf16x8 gf[2];
        auto hrow = [&](int i, int l) INL {
            const int gi = wid * 3 + i, mr = gi * 15 + l - 1;
            return l == 0 ? 128 : (gi < 9 ? (mr < 128 ? mr : 127) : 128 + l);
        };
        unsigned ea[10], ma[2], xw[2], xa, oo, l16, p4_, g16, cwa, cwb;
        f32x4 vnm[5], ccv[4];
        f32x2 rnm[3], rsc[2], rk[4];
        float ccw[2];
        u32x4 am[4];
        constexpr int NEA = 15;
        auto eadr = [&](auto tc) INL {
            constexpr int T = decltype(tc)::value;
            if constexpr (T == 0) asm volatile("v_and_b32 %0, 63, %1\n\tv_lshlrev_b32 %0, 4, %0" : "=v"(l16) : "v"(threadIdx.x));
            if constexpr (T == 1) { p4_ = opaque(l16 >> 2 & 60u); ma[0] = opaque(l16 + lds0 + ATX); }
            if constexpr (T == 2) ma[1] = opaque(ma[0] + (1 - wm) * 2048);
            if constexpr (T == 3) { asm volatile("v_and_b32 %0, 48, %1" : "=v"(g16) : "v"(threadIdx.x)); ea[0] = opaque(p4_ + lds0 + QSK); ea[1] = opaque(g16 + lds0 + QSK); }
            if constexpr (T == 4) { ea[2] = opaque(ea[0] + wm * 256); ea[3] = opaque(ea[1] + wm * 256); }
            if constexpr (T == 5) ea[4] = opaque(g16 + lds0 + QCS + (wn * 2 + wm) * 64);
            if constexpr (T == 6) { ea[6] = opaque(ea[4] ^ 64u); ea[5] = opaque(ea[4] - g16 + p4_); }
            if constexpr (T == 7) { ea[7] = opaque(ea[5] ^ 64u); ea[9] = opaque((p4_ & 56u) - g16); }
            if constexpr (T == 8) ea[8] = opaque(ea[2] - ((ea[9] & 63u) | -1216u));
            if constexpr (T == 9) ea[9] = opaque(ea[2] - (((ea[9] - 8u) & 63u) | -1216u));
            if constexpr (T == 10) xa = opaque(l16 + lds0 + QXC + wn * 4096);
            if constexpr (T == 11) xw[1] = opaque(xa + (1 - wm * 5) * 2048);
            if constexpr (T == 12) { xw[0] = opaque(xa + wm * 8); xa = opaque(opaque(xa) + wm * 2048); }
            if constexpr (T == 13) oo = opaque(l16 + (tm * 32 + tn) * 8192);
            if constexpr (T == 14) oo = opaque(oo + (wm * 32 + wn) * 4096);
            if constexpr (T == 15) cwa = opaque(p4_ == 0 ? ea[4] : xa);
            if constexpr (T == 16) cwb = opaque(g16 == 0 ? ea[5] : xa);
        };
        auto ld2 = [&](unsigned a) INL { return f32x2{*(const LDS_AS float*)(size_t)a, *(const LDS_AS float*)(size_t)(a + 64)}; };
        auto eld = [&](auto oc) INL {
            constexpr int O = decltype(oc)::value;
            if constexpr (O < 4) am[O] = *(const LDS_AS u32x4*)(size_t)(O == 2 ? ma[1] : ma[0] + O * 1024);
            else if constexpr (O == 5 || O == 7) ccw[O / 2 - 2] = *(const LDS_AS float*)(size_t)(ea[O] + 512);
            else if constexpr (O < 8) ccv[O / 2] = *(const LDS_AS f32x4*)(size_t)(ea[O] + 256);
            else if constexpr (O < 10) ccv[O - 8] = *(const LDS_AS f32x4*)(size_t)(ea[2 * O - 12]);
            else if constexpr (O == 10) rnm[2].x = *(const LDS_AS float*)(size_t)ea[0];
            else if constexpr (O == 11) vnm[4] = *(const LDS_AS f32x4*)(size_t)ea[1];
            else if constexpr (O == 12 || O == 15) rnm[O == 12] = ld2(ea[2] + 64 + (O == 12) * 128);
            else if constexpr (O < 18) { constexpr int I = O < 15 ? 16 - O : 17 - O; vnm[I] = *(const LDS_AS f32x4*)(size_t)(ea[3] + (I + 1) * 64); }
            else if constexpr (O < 20) rsc[O - 18] = ld2(ea[2] + 576 + 64 + (O - 18) * 128);
            else rk[O - 20] = ld2(ea[8 + (O - 20) / 2] + (O & 1) * 128);
        };
        auto unit = [&](bf16x8 (&af)[10], s16x4 (&bf)[24], bf16x8 (&an)[10], s16x4 (&bn)[24], auto ktc, auto hc, auto fic) INL {
            constexpr bool FI = decltype(fic)::value;
            constexpr int KT = decltype(ktc)::value, HF = decltype(hc)::value;
            static_for<0, 26>([&](auto tc) INL {
                constexpr int T = decltype(tc)::value, X = HF * 26 + T, kk = X & 1;
                constexpr int c = X < 32 ? X / 8 : (X < 42 ? 4 : 5), p = (X < 32 ? X % 8 : (X < 42 ? X - 32 : X - 42)) / 2;
                constexpr int i = c == 5 ? 4 - p : (c & 1 ? 3 - p : p);
                if constexpr (qcol(c) >= 4) mma_ip(af[i * 2 + kk], cat(bf[c * 4 + kk * 2], bf[c * 4 + kk * 2 + 1]), acc[i][qcol(c)]);
                else mma_ip(cat(bf[c * 4 + kk * 2], bf[c * 4 + kk * 2 + 1]), af[i * 2 + kk], acc[i][qcol(c)]);
                SCHED();
                if constexpr (HF == 1) {
                    if constexpr (KT + 1 == NK && FI) {
                        if constexpr (T == GBT - 4) *(LDS_AS f32x4*)(size_t)(cwa + 2048) = rg[2];
                        else if constexpr (T == GBT) { WAIT_LGKM0(); BARRIER(); }
                        else if constexpr (T < 17) eadr(CT<T + 2 - (T > GBT - 4) - (T > GBT)>{});
                        else if constexpr (T < 21) eld(CT<T - 17>{});
                        else if constexpr (T == 21) *(LDS_AS f32x4*)(size_t)cwa = acc[4][0];
                        else if constexpr (T == 22) *(LDS_AS f32x4*)(size_t)(cwa + 256) = acc[4][2];
                        else if constexpr (T == 24) *(LDS_AS float*)(size_t)(cwb + 512) = acc[4][4][0];
                    }
                    if constexpr (KT + 1 == NK && !FI) {
                        static_assert(QBT >= 10, "order");
                        constexpr int EN = 26 - QBT < 14 ? 26 - QBT : 14;
                        if constexpr (T < 10) eld(tc);
                        if constexpr (T == QBT - 1) BARRIER();
                        if constexpr (T >= QBT)
                            static_for<(T - QBT) * 14 / EN, ((T + 1 - QBT) * 14 / EN < 14 ? (T + 1 - QBT) * 14 / EN : 14)>([&](auto oc) INL { eld(CT<10 + decltype(oc)::value>{}); });
                    }
                    if constexpr (KT + 1 < NK && T < QG1) ldop(an, bn, CT<KT + 1>{}, tc);
                    if constexpr (KT + 1 < NK && T >= QL1 && T < QL1 + 16 - QG1) ldop(an, bn, CT<KT + 1>{}, CT<T - QL1 + QG1>{});
                } else if constexpr (FI) {
                    if constexpr (T % 8 < 2 && T < 24) gf[T & 1] = lds_a<qsl(KT) * QAS>(ha[T / 8][T & 1]);
                    else if constexpr (T < 20) ldop(af, bf, ktc, CT<14 + T>{});
                    else if constexpr (KT + 1 == NK) {
                        if constexpr (T == 20) eadr(CT<0>{});
                        if constexpr (T == 25) eadr(CT<1>{});
                        if constexpr (T == 21) cwa = opaque(l16 + lds0 + QGR + wid * 3072);
                        if constexpr (T == 22 || T == 24) *(LDS_AS f32x4*)(size_t)(cwa + (T - 22) * 512) = rg[T / 2 - 11];
                    }
                } else {
                    if constexpr (T < 18) ldop(af, bf, ktc, CT<16 + T>{});
                }
                SCHED();
                if constexpr (FI && HF == 0 && T % 8 == 7) {
                    mma_ip(gf[0], gf[0], rg[T / 8]);
                    SCHED();
                    if constexpr (T < 16) ldop(af, bf, ktc, CT<16 + T - 1>{});
                    SCHED();
                    mma_ip(gf[1], gf[1], rg[T / 8]);
                    SCHED();
                    if constexpr (T < 16) ldop(af, bf, ktc, CT<16 + T>{});
                    SCHED();
                }
                if constexpr (FI && X == 31) {
                    mma_ip(cat(bf[12], bf[13]), af[8], acc[4][0]);
                    SCHED();
                    mma_ip(cat(bf[14], bf[15]), af[9], acc[4][0]);
                    SCHED();
                }
            });
        };
        if (first) token();
        if (!first && id == 0 && wid == 3) {
#pragma unroll 1
            for (unsigned spins = 0; spins < (1u << 22); ++spins) {
                bool ok = true;
#pragma unroll
                for (int i = 0; i < 4; ++i)
                    ok &= __hip_atomic_load((gu64*)(oflags + 8192 + (i * 64 + lane) * 32), __ATOMIC_RELAXED, __HIP_MEMORY_SCOPE_AGENT) == tok;
                if (__all(ok)) break;
            }
            if (lane == 0) __hip_atomic_store((gu64*)epoch, tok, __ATOMIC_RELAXED, __HIP_MEMORY_SCOPE_AGENT);
        }
        bf16x8 a0[10], a1[10], a2[10];
        s16x4 b0[24], b1[24], b2[24];
        auto loop = [&](auto fic) INL {
#pragma unroll
        for (int i = 0; i < 5; ++i)
#pragma unroll
            for (int j = 0; j < 6; ++j)
                if (i < 4 || j == 2 || j == 4) {
                    acc[i][j] = f32x4{0.f, 0.f, 0.f, 0.f};
                    asm volatile("" : "+v"(acc[i][j]));
                }
            if constexpr (decltype(fic)::value) wait_vm<11>();
            BARRIER();
            static_for<0, 16>([&](auto oc) INL { ldop(a0, b0, CT<0>{}, oc); });
            auto ktile = [&](bf16x8 (&af)[10], s16x4 (&bf)[24], bf16x8 (&an)[10], s16x4 (&bn)[24], auto kc) INL {
                unit(af, bf, an, bn, kc, CT<0>{}, fic);
                WAIT_LGKM0();
                if constexpr (decltype(kc)::value == 0 && decltype(fic)::value) wait_vm<0>();
                BARRIER();
                unit(af, bf, an, bn, kc, CT<1>{}, fic);
            };
#pragma unroll 1
            for (int it = 0; it < 5; ++it) {
                ktile(a0, b0, a1, b1, CT<0>{});
                ktile(a1, b1, a2, b2, CT<1>{});
                ktile(a2, b2, a0, b0, CT<2>{});
            }
            unit(a0, b0, a1, b1, CT<NK - 1>{}, CT<0>{}, fic);
            unit(a0, b0, a1, b1, CT<NK - 1>{}, CT<1>{}, fic);
        };
        if (first) {
#pragma unroll
            for (int i = 0; i < 3; ++i) {
                const int hr = hrow(i, l15);
#pragma unroll
                for (int kk = 0; kk < 2; ++kk) ha[i][kk] = opaque(lds0 + hr * 128 + (((kk * 4 + g) ^ ((hr >> 1) & 7)) * 16));
                rg[i] = f32x4{0.f, 0.f, 0.f, 0.f};
                asm volatile("" : "+v"(rg[i]));
            }
            acc[4][0] = f32x4{0.f, 0.f, 0.f, 0.f};
            asm volatile("" : "+v"(acc[4][0]));
            loop(CT<1>{});
            WAIT_LGKM0();
            BARRIER();
            static_for<0, 20>([&](auto oc) INL {
                constexpr int O = decltype(oc)::value;
                eld(CT<(O < 10 ? (int)(0x98d7c6b5a4ull >> (4 * O)) & 15 : O + 4)>{});
                SCHED();
            });
        } else {
            static_for<0, NEA>([&](auto tc) INL { eadr(tc); });
            loop(CT<0>{});
            BARRIER();
        }
        SCHED();
        auto ln2 = [&](int i, int j0, int j1) INL {
            f32x2 y[4];
#pragma unroll
            for (int x = 0; x < 4; ++x) {
                const int j = x < 2 ? j0 : j1;
                const f32x2 a = x & 1 ? acc[i][j].zw : acc[i][j].xy;
                if (j < 4) y[x] = __builtin_elementwise_fma(i & 1 ? rnm[i / 2].yy : rnm[i / 2].xx, x & 1 ? ccv[j].zw : ccv[j].xy, a);
                else y[x] = __builtin_elementwise_fma(x & 1 ? vnm[i].zw : vnm[i].xy, f32x2{ccw[j - 4], ccw[j - 4]}, a);
            }
            SCHED();
            acc[i][j0] = f32x4{y[0].x, y[0].y, y[1].x, y[1].y};
            acc[i][j1] = f32x4{y[2].x, y[2].y, y[3].x, y[3].y};
        };
        auto rnd = [&](f32x4 a, f32x4 b) INL {
            u32x4 r = {pk2(a[0], a[1]), pk2(a[2], a[3]), pk2(b[0], b[1]), pk2(b[2], b[3])};
            asm volatile("" : "+v"(r));
            SCHED();
            return r;
        };
        u32x4 qf[4], kf[4];
        u32x2 v3[2];
        ln2(4, 2, 4);
        {
            const u32x4 hv = rnd(acc[4][2], acc[4][4]);
            *(LDS_AS u32x2*)(size_t)(xw[0]) = hv.xy;
            *(LDS_AS u32x2*)(size_t)(xw[0] + 1024) = hv.zw;
        }
        ln2(3, 2, 3);
        kf[3] = rnd(acc[3][2], acc[3][3]);
        *(LDS_AS u32x2*)(size_t)(xw[1] + 8) = kf[3].xy;
        *(LDS_AS u32x2*)(size_t)(xw[1]) = kf[3].zw;
        ln2(3, 4, 5);
        v3[0] = u32x2{pk2(acc[3][4][0], acc[3][4][1]), pk2(acc[3][4][2], acc[3][4][3])};
        v3[1] = u32x2{pk2(acc[3][5][0], acc[3][5][1]), pk2(acc[3][5][2], acc[3][5][3])};
        *(LDS_AS u32x2*)(size_t)(xw[1] + 1032) = v3[0];
        *(LDS_AS u32x2*)(size_t)(xw[1] + 1024) = v3[1];
        SCHED();
        f32x4 vf[3][2];
        f32x2 sc[4];
#pragma unroll
        for (int i = 3; i >= 0; --i) {
            ln2(i, 0, 1);
            qf[i] = rnd(acc[i][0], acc[i][1]);
            if (i < 3) {
                ln2(i, 2, 3);
                kf[i] = rnd(acc[i][2], acc[i][3]);
                ln2(i, 4, 5);
                vf[i][0] = acc[i][4];
                vf[i][1] = acc[i][5];
                asm volatile("" : "+v"(vf[i][0]), "+v"(vf[i][1]));
            }
        }
#pragma unroll
        for (int i = 0; i < 4; ++i) {
            sc[i] = rk[i] * rsc[i & 1];
            asm volatile("" : "+v"(sc[i]));
        }
        WAIT_LGKM0();
        BARRIER();
        SCHED();
        att_run(qf, kf, vf, v3, am, rk, sc, xa, oo, C);
        SCHED();
    }
}

__device__ __forceinline__ unsigned dn_boff(int h, int lane) { return b_off(h, lane, D_, (bid_() >> 5) * 128); }
__device__ __forceinline__ void dn_stage_b(unsigned lds0, const u16* B, unsigned boff, int kh, int h, int i, int kg, int sl) {
    glds16(uni((const char*)B + (long long)(kg * 64 + (i & 1) * 32) * D_ * 2 + ((i >> 1) ^ kh) * 128), boff,
           lds0 + h * 1024 + sl * 16384 + i * 4096);
}
#define BOX_LOOK(r) "global_load_dwordx4 v[" r ":" r "+3], %14, %15 sc1\n\tglobal_load_dwordx4 v[" r "+4:" r "+7], %14, %15 offset:1024 sc1\n\t" \
    "global_load_dwordx4 v[" r "+8:" r "+11], %14, %15 offset:2048 sc1\n\tglobal_load_dwordx4 v[" r "+12:" r "+15], %14, %15 offset:3072 sc1\n\t"
#define BOX_MAX(r, i) "v_max3_u32 %12, %12, v[" r "+" i "], v[" r "+" i "+1]\n\t"
#define BOX_TEST(r) "s_waitcnt vmcnt(4)\n\tv_max_u32 %12, v[" r "], v[" r "+1]\n\t" BOX_MAX(r, "2") BOX_MAX(r, "4") BOX_MAX(r, "6") \
    BOX_MAX(r, "8") BOX_MAX(r, "10") BOX_MAX(r, "12") BOX_MAX(r, "14") "v_cmp_eq_u32 vcc, -1, %12\n\t"
#define BOX_MOV(r, i) "v_mov_b64 v[200+" i ":201+" i "], v[" r "+" i ":" r "+1+" i "]\n\t"
#define BOX_TAKE(r) BOX_MOV(r, "0") BOX_MOV(r, "2") BOX_MOV(r, "4") BOX_MOV(r, "6") BOX_MOV(r, "8") BOX_MOV(r, "10") BOX_MOV(r, "12") BOX_MOV(r, "14")
__device__ __forceinline__ void box_wait(u32x4 (&a)[4], u32x4 (&b)[4], u32x4 (&c)[4], const void* box, unsigned off) {
    unsigned t, n;
    asm volatile(BOX_TEST("200") "s_cbranch_vccz 3f\n\ts_mov_b32 %13, 0x100000\n"
                 "1:\n\t" BOX_LOOK("232") BOX_TEST("216") "s_cbranch_vccz 2f\n\t" BOX_LOOK("216") BOX_TEST("232")
                 "s_cbranch_vccz 4f\n\ts_sub_u32 %13, %13, 1\n\ts_cbranch_scc0 1b\n"
                 "4:\n\t" BOX_TAKE("232") "s_branch 5f\n"
                 "2:\n\t" BOX_TAKE("216") "\n5:\n\ts_nop 1\n3:"
                 : "+{v[200:203]}"(a[0]), "+{v[204:207]}"(a[1]), "+{v[208:211]}"(a[2]), "+{v[212:215]}"(a[3]),
                   "+{v[216:219]}"(b[0]), "+{v[220:223]}"(b[1]), "+{v[224:227]}"(b[2]), "+{v[228:231]}"(b[3]),
                   "=&{v[232:235]}"(c[0]), "=&{v[236:239]}"(c[1]), "=&{v[240:243]}"(c[2]), "=&{v[244:247]}"(c[3]), "=&v"(t), "=&s"(n)
                 : "v"(off), "s"(box) : "memory", "vcc", "scc");
}
__device__ __forceinline__ void dn_phase(char* smem, const u16* A, const u16* __restrict__ B, u16* C, float* sp,
                                         float* slab, unsigned* gflags, unsigned* oflags,
                                         const u16* pf, u64 tok) {
    constexpr int BM = 128, NS = 4, NSO = NSOLO;
    constexpr int ASZ = BM * 128, SLOT = 2 * ASZ;
    constexpr int lda = F_;
    constexpr int BOX = 16384;

    const int tid = (int)opaque(threadIdx.x), lane = tid & 63;
    const int wid = __builtin_amdgcn_readfirstlane(tid >> 6);
    const unsigned bx = bid_();
    const int xcd = bx & 7, r_ = bx >> 3;
    const int kh = DPAR ? xcd & 1 : xcd >> 2, tm = dn_grp(xcd) * 4 + (r_ & 3), tn = r_ >> 2;
    const int m0 = tm * BM, n0 = tn * 128 + kh * 64;
    const unsigned lds0 = __builtin_amdgcn_readfirstlane((unsigned)(size_t)(LDS_AS char*)smem);
    ResC rc;
    u32x4 pb[4], pc[4];
    if (wid >= 4) {
        const int h = wid - 4;
        unsigned aoff = (unsigned)((m0 / 4 + h * 2 + (lane >> 5)) * (8 * F_) + (((lane >> 3) & 3) ^ (h & 1)) * 128 + (lane & 7) * 16);
        unsigned boff = dn_boff(h, lane);
        const unsigned* const f = gflags + (tm * 32 + (lane & 31)) * 32;
        u64 have, at = (u64)(((1u << DLK) - 1u) << (xcd & 6) * 4), lk;
        look(lk, at, f);
        asm volatile("" : "+v"(aoff), "+v"(boff), "+s"(A));
        pubc<1>(gflags + ((r_ & 7) * 64 + 32 + xcd * 4 + (r_ >> 3)) * 32, tok, lds0 + GCN + 4);
        have = seen(lk, at, tok);
        auto stage = [&](int kg, int sl, auto nbc) INL {
            poll(have, (1u << (kg * 4 / 7)) | (1u << ((kg * 64 + 63) / 112)), at, f, tok);
#pragma unroll
            for (int i = 0; i < 4; ++i)
                glds16_dev(uni((const char*)A + kg * 512 + i * 32 * lda * 2), aoff, lds0 + DNA + h * 1024 + sl * ASZ + i * 4096);
#pragma unroll
            for (int i = 0; i < decltype(nbc)::value; ++i) dn_stage_b(lds0, B, boff, kh, h, i, kg, sl);
        };
        const int kg0 = (xcd & 6) * 7;
        int kg = kg0, sl = kh ? dn_first(DNO) : dn_first(DNE);
        auto next = [&](auto nbc) INL {
            stage(kg, sl, nbc);
            kg = kg == 55 ? 0 : kg + 1;
            sl = (sl + 1) & 3;
        };
        const u64 DW2 = 0xffffffffull;
        next(CT<0>{});
        if constexpr (DNE != DNO) BARRIER();
        if constexpr (DL2 == 0) look(lk, DW2, f);
        next(CT<0>{});
        wait_vm<4>();
        BARRIER();
        if constexpr (DL2 == 0) { at = DW2; have |= seen(lk, at, tok); }
        if constexpr (DL2 == 1) look(lk, DW2, f);
        next(CT<0>{});
        next(CT<0>{});
        wait_vm<8>();
        BARRIER();
        if constexpr (DL2 == 1) { at = DW2; have |= seen(lk, at, tok); }
        next(CT<4>{});
        wait_vm<12>();
        BARRIER();
        next(CT<4>{});
#pragma unroll 1
        for (int n = (kh ? DNO : DNE) - 6; n > 0; --n) {
            wait_vm<16>();
            BARRIER();
            next(CT<4>{});
        }
        static_for<-NS, NSO - 1>([&](auto kc) INL {
            constexpr int KT = decltype(kc)::value;
            constexpr int P2 = KT + 2 < 0 ? 8 : (KT + 2 < NSO ? 6 : 0), P3 = KT + 3 < 0 ? 8 : (KT + 3 < NSO ? 6 : 0);
            wait_vm<P2 + P3>();
            BARRIER();
            if constexpr (KT < 0) {
                constexpr int J = KT + NS, RE = DNE + J < 28 ? DNE + J : 52 + J, RO = DNO + J < 28 ? DNO + J : 52 + J;
                const int ks = kg0 + (kh ? RO : RE);
                stage(ks < 56 ? ks : ks - 56, (KT + 2) & 3, CT<2>{});
            }
        });
        if (pf != nullptr) {
            for (int i = QPS; i > 0; --i) __builtin_amdgcn_s_sleep(1);
            qkv_stage_b(lds0, pf, qkv_boff(h, lane), h, qkv_k(0), qsl(0));
            qkv_stage_b(lds0, pf, qkv_boff(h, lane), h, qkv_k(1), qsl(1));
        }
        res_setup(rc, lds0 + NS * SLOT, m0, n0, tid);
        if (pf != nullptr && (bx & 1) == 0 && wid >= 6) {
            const unsigned ix = (bx >> 3) * 96 + (tid < 480 ? tid - 384 : 0);
            touch1(SINK, pf, ix * 2048 + ((bx & 3) * 4 + 3) * 128);
        }
#pragma unroll
        for (int i = 0; i < 4; ++i) asm volatile("" : "=v"(pb[i]), "=v"(pc[i]));
    } else {
        const int wm = wid >> 1, wn = wid & 1;
        const int l15 = lane & 15, g = lane >> 4, q = l15 >> 2, p4 = l15 & 3;
        unsigned ab[2], bb[2];
#pragma unroll
        for (int x = 0; x < 2; ++x) {
            ab[x] = opaque(lds0 + DNA + (wm * 16 + q) * 512 + (((x * 2 + (g >> 1)) ^ (l15 >> 3)) * 128) + p4 * 32 + (g & 1) * 16);
            bb[x] = opaque(lds0 + (g * 8 + q) * 128 + p4 * 8 + (((wn * 2 + x) ^ ((q >> 1) | ((g & 1) << 1))) << 5));
        }
        f32x4 acc[4][4];
        bf16x8 rr[4];
        const bf16x8 selr[2] = {pick(g == (l15 >> 3), l15 & 7), pick(g == 2 + (l15 >> 3), l15 & 7)};
        {
            const unsigned ra = lds0 + DNR + (wm * 64 + l15) * 128 + (((wn * 4 + g) ^ (l15 >> 1)) * 16);
            static_for<0, 4>([&](auto ic) INL {
                constexpr int i = decltype(ic)::value;
                rr[i] = lds_a<i * 2048>(ra);
#pragma unroll
                for (int j = 0; j < 2; ++j) {
                    acc[i][j] = f32x4{0.f, 0.f, 0.f, 0.f};
                    asm volatile("" : "+v"(acc[i][j]));
                }
            });
#pragma unroll
            for (int i = 0; i < 4; ++i) asm volatile("" : "+v"(rr[i]));
            if constexpr (DNE != DNO) BARRIER();
        }
        auto ldop = [&](bf16x8 (&af)[8], s16x4 (&bf)[16], auto slc, auto j0c, auto oc) INL {
            constexpr int SL = decltype(slc)::value, O = decltype(oc)::value;
            if constexpr (O == 2 || (O >= 5 && O < 12)) {
                constexpr int X = O == 2 ? 0 : O - 4;
                af[(X & 1) * 4 + X / 2] = lds_a<SL * ASZ + (X / 2) * 2048>(ab[X & 1]);
            } else {
                constexpr int X = decltype(j0c)::value * 4 + (O < 2 ? O : (O < 5 ? O - 1 : O - 8)), J = X / 4;
                bf[X] = lds_h<SL * ASZ + (J < 2 ? 8192 : 0) + ((X >> 1) & 1) * 4096 + (X & 1) * 512>(bb[J & 1]);
            }
        };
        auto ktile = [&](bf16x8 (&af)[8], s16x4 (&bf)[16], bf16x8 (&an)[8], s16x4 (&bn)[16], auto nslc, auto j0c, auto jnc, auto&& gap) INL {
            constexpr int J0 = decltype(j0c)::value, JN = decltype(jnc)::value;
            constexpr int NM = 8 * (4 - J0), NL = JN < 4 ? 8 + 4 * (4 - JN) : 0, TB = J0 ? DBAR2 : DBAR;
            constexpr int NG = NM - TB < NL ? NM - TB : NL;
            static_for<0, NM>([&](auto tc) INL {
                constexpr int T = decltype(tc)::value, j = J0 + T / 8, i = j & 1 ? 3 - (T / 2) % 4 : (T / 2) % 4, kk = T & 1;
                if constexpr (T == TB && NL > 0) {
                    WAIT_LGKM0();
                    BARRIER();
                }
                mma_ip(cat(bf[j * 4 + kk * 2], bf[j * 4 + kk * 2 + 1]), af[kk * 4 + i], acc[i][j]);
                SCHED();
                if constexpr (NL > 0 && T >= TB) {
                    constexpr int U = T - TB, lo_ = (U * NL + NG - 1) / NG, hi_ = ((U + 1) * NL + NG - 1) / NG;
                    static_for<lo_, (hi_ < NL ? hi_ : NL)>([&](auto oc) INL { ldop(an, bn, nslc, jnc, oc); });
                }
                gap(tc);
                SCHED();
            });
        };
        bf16x8 a0[8], a1[8];
        s16x4 b0[16], b1[16];
        const unsigned bo = opaque((unsigned)(wid * 4096 + lane * 16));
        const bf16x8 sel[2] = {pick(g == q, p4), pick(g == q, 4 + p4)};
        res_setup(rc, lds0 + NS * SLOT, m0, n0, tid);
        const unsigned cw = opaque(lds0 + NS * SLOT + (wm * 64 + l15) * CSTR + (wn * 32 + g * 4) * 2);
        const char* const rin = sopq((const char*)slab + (bx ^ DPX) * BOX);
        const __amdgpu_buffer_rsrc_t rout = __builtin_amdgcn_make_buffer_rsrc((char*)slab + bx * BOX, 0, BOX, 0x00020000);
        BARRIER();
        auto none = [&](auto) INL {};
        auto head = [&](auto nc) INL {
            constexpr int H = (decltype(nc)::value - 1) % NS;
            static_for<0, 24>([&](auto oc) INL {
                if constexpr (H & 1) ldop(a1, b1, CT<(1 - H) & 3>{}, CT<0>{}, oc);
                else ldop(a0, b0, CT<(1 - H) & 3>{}, CT<0>{}, oc);
            });
            SCHED();
#pragma unroll
            for (int t = 0; t < 8; ++t) mma_z(selr[t & 1], rr[t >> 1], acc[t >> 1][2 + (t & 1)]);
            SCHED();
            static_for<0, H>([&](auto jc) INL {
                constexpr int E = H - decltype(jc)::value;
                if constexpr (E & 1) ktile(a1, b1, a0, b0, CT<(2 - E) & 3>{}, CT<0>{}, CT<0>{}, none);
                else ktile(a0, b0, a1, b1, CT<(2 - E) & 3>{}, CT<0>{}, CT<0>{}, none);
            });
        };
        if ((DNE - DNO) % NS == 0 || kh) head(CT<DNO>{}); else head(CT<DNE>{});
#pragma unroll 1
        for (int n = (kh ? DNO - 1 : DNE - 1) / NS; n > 0; --n) {
            ktile(a0, b0, a1, b1, CT<2>{}, CT<0>{}, CT<0>{}, none);
            ktile(a1, b1, a0, b0, CT<3>{}, CT<0>{}, CT<0>{}, none);
            ktile(a0, b0, a1, b1, CT<0>{}, CT<0>{}, CT<0>{}, none);
            ktile(a1, b1, a0, b0, CT<1>{}, CT<0>{}, CT<0>{}, none);
        }
        u32x4 ow, pa[4];
        auto look = [&](u32x4& d, auto ic) INL { gload16_devo<decltype(ic)::value * 1024>(d, rin, bo); };
        static_for<-1, NSO>([&](auto sc) INL {
            constexpr int KT = decltype(sc)::value;
            constexpr int J0 = KT < 0 ? 0 : 2, JN = KT + 1 < NSO ? 2 : 4;
            auto gap = [&](auto tc) INL {
                constexpr int T = decltype(tc)::value;
                if constexpr (KT < 0 && T >= XO && T < XO + 16) {
                    constexpr int U = T - XO, j = U / 8, i = j ? 3 - (U / 2) % 4 : (U / 2) % 4;
                    asm volatile("v_cvt_pk_bf16_f32 %0, %1, %2\n\tv_min_u32 %0, -2, %0"
                                 : "=v"(ow[(i & 1) * 2 + (U & 1)]) : "v"(acc[i][j][(U & 1) * 2]), "v"(acc[i][j][(U & 1) * 2 + 1]));
                    if constexpr (U % 4 == 3) __builtin_amdgcn_raw_buffer_store_b128(ow, rout, bo + (j * 2 + i / 2) * 1024, 0, 16);
                }
                constexpr int X = KT < 0 ? -999 : (KT - NSO) * 16 + T;
                if constexpr (X >= -XA && X < -XA + 4) look(pa[X + XA], CT<X + XA>{});
                if constexpr (X >= -XB && X < -XB + 4) look(pb[X + XB], CT<X + XB>{});
            };
            if constexpr (KT & 1) ktile(a0, b0, a1, b1, CT<(KT + 3) % NS>{}, CT<J0>{}, CT<JN>{}, gap);
            else ktile(a1, b1, a0, b0, CT<(KT + 3) % NS>{}, CT<J0>{}, CT<JN>{}, gap);
        });
        box_wait(pa, pb, pc, rin, bo);
#pragma unroll
        for (int t = 0; t < 8; ++t) mma_ip(sel[(t >> 1) & 1], __builtin_bit_cast(bf16x8, pa[(t & 1) * 2 + t / 4]), acc[t >> 1][2 + (t & 1)]);
        asm volatile("s_nop 1" ::: "memory");
        SCHED();
#pragma unroll
        for (int i = 0; i < 4; ++i) {
#pragma unroll
            for (int x = 0; x < 2; ++x) {
                const f32x4 a = acc[i][2 + x];
                *(LDS_AS u32x2*)(size_t)(cw + i * 16 * CSTR + x * 32) = u32x2{pk2(a[0], a[1]), pk2(a[2], a[3])};
            }
            SCHED();
        }
    }
    WAIT_LGKM0();
    BARRIER();
    res_tail(rc, C, sp);
    if (pf != nullptr) publish(oflags + (tm * 16 + (n0 >> 6)) * 32, tok, wid, SINK, []() {}); else wait_vm<0>();
#pragma unroll
    for (int i = 0; i < 4; ++i) asm volatile("" :: "v"(pb[i]), "v"(pc[i]));
}

__device__ __forceinline__ void gu_phase(char* smem, const u16* A, const u16* B0, const u16* B1, u16* C, const float* cs,
                                         const float* sp, const u16* wd, unsigned* wflags, unsigned* gflags, const GuC& gc, f32x4 cp,
                                         u64 lk, u64 tok) {
    constexpr int BM = 256, BN = 224, MI = 4, NI = 7, NK = D_ / 64;
    constexpr int ASZ = BM * 128, BSZ = GBS, NSA = 3, NSB = 2, BRING = GBR, STL = GST;
    constexpr auto asl = [](int kt) { return (kt + 2) % NSA; };

    const int wid = __builtin_amdgcn_readfirstlane(opaque(threadIdx.x) >> 6);
    const int id = wg_id();
    const int tm = id & 7, tn = id >> 3, m0 = tm * BM;
    const unsigned lds0 = __builtin_amdgcn_readfirstlane((unsigned)(size_t)(LDS_AS char*)smem);
    const unsigned ldsw = lds0 + wid * 1024;

    const auto& aoff = gc.aoff;
    const auto& boff = gc.boff;
    const __amdgpu_buffer_rsrc_t ra = __builtin_amdgcn_make_buffer_rsrc((void*)A, 0, 0x7fffffff, 0x00020000);
    const __amdgpu_buffer_rsrc_t rb[4] = {__builtin_amdgcn_make_buffer_rsrc((void*)B0, 0, 0x7fffffff, 0x00020000),
                                          __builtin_amdgcn_make_buffer_rsrc((void*)(wid >= 6 ? B1 : B0), 0, 0x7fffffff, 0x00020000),
                                          __builtin_amdgcn_make_buffer_rsrc((void*)B1, 0, 0x7fffffff, 0x00020000),
                                          __builtin_amdgcn_make_buffer_rsrc((void*)B1, 0, wid < 4 ? 0x7fffffff : 0, 0x00020000)};
    const unsigned b3d[2] = {wid < 4 ? ldsw + BRING + 24576 : lds0 + 161792, wid < 4 ? ldsw + BRING + BSZ + 24576 : lds0 + 161792};
    unsigned soff = 0u;
    auto prep = [&](auto bc, auto pc, auto ktc) INL {
        constexpr int P = decltype(pc)::value, KT = decltype(ktc)::value;
        constexpr bool ISB = decltype(bc)::value;
        if constexpr (P == 0) s_set<(ISB ? gu_k(KT) * 64 * F_ * 2 : gu_k(KT) * 128)>(soff);
        if constexpr (ISB && P == 3) m0_set<0>(b3d[KT % NSB]);
        else m0_set<(ISB ? BRING + (KT % NSB) * BSZ : asl(KT) * ASZ) + P * 8192>(ldsw);
    };
    const int dk = DPAR ? (id >> 5) & 1 : id >> 7;
    const int dtm = dn_grp(id >> 5) * 4 + (id & 3), dtn = ((id >> 2) & 7) * 2 + dk;
    const unsigned dkh = dk * (128 + (dn_first(DNE) ^ dn_first(DNO)) * 64 * D_ * 2);
    const __amdgpu_buffer_rsrc_t rd = __builtin_amdgcn_make_buffer_rsrc((char*)wd + (id >> 6) * (14 * 64 * D_ * 2) + (dtn >> 1) * 256, 0,
        0x7fffffff, 0x00020000);
    const __amdgpu_buffer_rsrc_t rr = __builtin_amdgcn_make_buffer_rsrc((char*)A + (dtm * 128 - m0) * D_ * 2 + dtn * 128, 0, 0x7fffffff, 0x00020000);
    unsigned doff;
    constexpr auto dreq = [](int sl) { return sl < 0 ? -1 : (int)((DSCH >> (4 * sl)) & 15) - 1; };
    constexpr int DV = (dreq(0) >= 0) + (dreq(1) >= 0) + (dreq(2) >= 0) + (dreq(3) >= 0) + (TCU <= 0);
    auto dprep = [&](auto xc) INL {
        constexpr int X = decltype(xc)::value;
        if constexpr (X == 0) {
            const int t = (int)opaque(threadIdx.x);
            doff = opaque(b_off(t >> 6, t & 63, D_, 0));
        }
        if constexpr (X < 8) asm volatile("s_xor_b32 %0, %1, %2" : "=s"(soff) : "s"(dkh), "n"((((X >> 1) - dn_first(DNE)) & 3) * 64 * D_ * 2 + (X & 1) * 128) : "scc");
        m0_set<(X < 8 ? 0 : DNR - 65536) + X * 8192>(ldsw);
    };
    auto dgo = [&](auto xc) INL {
        constexpr int X = decltype(xc)::value;
        if constexpr (X < 8) blds16<false>(rd, doff, soff);
        else blds16<true>(rr, aoff[X - 8], 0u);
    };
    auto go = [&](auto bc, auto pc) INL {
        if constexpr (decltype(bc)::value) blds16<false>(rb[decltype(pc)::value], boff[decltype(pc)::value], soff);
        else blds16<true>(ra, aoff[decltype(pc)::value], soff);
    };
    auto stage_a = [&](auto ktc) INL {
        static_for<0, 4>([&](auto pc) INL { prep(CT<0>{}, pc, ktc); asm volatile("s_nop 0"); go(CT<0>{}, pc); });
    };
    __builtin_amdgcn_s_setprio(2);
    const auto& ab = gc.ab;
    const auto& bb = gc.bb;
    f32x4 acc[MI][NI];
    bf16x8 af[2][2 * MI];
    s16x4 bf[2][4 * NI];
    auto ldop = [&](auto ktc, auto oc) INL {
        constexpr int KT = decltype(ktc)::value, O = decltype(oc)::value;
        if constexpr (O == 2 || (O >= 5 && O < 12)) {
            constexpr int X = O == 2 ? 0 : O - 4, off = asl(KT) * ASZ + (X / 2) * 2048;
            af[KT & 1][X] = lds_a<(off & 65535)>(ab[X & 1][off >> 16]);
        } else {
            constexpr int X = O < 2 ? O : (O < 5 ? O - 1 : O - 8), J = X / 4;
            bf[KT & 1][X] = lds_h<(KT % NSB) * BSZ + ((X >> 1) & 1) * 32 * BN + (J == 6 ? 0 : (J / 3) * (BSZ / 2) + (J % 3) * 32)>(bb[X & 1][J == 6]);
        }
    };
    f32x4 stn[4];
    float sx[8];
    const char* const spw = uni((const char*)sp + m0 * 128);
    auto stats = [&](auto zc) INL {
        constexpr int Z = decltype(zc)::value;
        if constexpr (Z == 0) {
#pragma unroll
            for (int i = 0; i < 4; ++i) asm volatile("" : "+v"(stn[i]));
        }
        if constexpr (Z < 8) asm volatile("v_add_f32 %0, %1, %2" : "=v"(sx[Z]) : "v"(stn[Z / 4 * 2][Z % 4]), "v"(stn[Z / 4 * 2 + 1][Z % 4]));
        else if constexpr (Z < 12) asm volatile("v_add_f32 %0, %0, %1" : "+v"(sx[(Z & 1) * 2 + (Z / 2 & 1)]) : "v"(sx[4 + (Z & 1) * 2 + (Z / 2 & 1)]));
        else if constexpr (Z < 14) asm volatile("v_permlane16_swap_b32 %0, %1" : "+v"(sx[Z - 12]), "+v"(sx[Z - 10]));
        else if constexpr (Z < 16) asm volatile("v_add_f32 %0, %0, %1" : "+v"(sx[Z - 14]) : "v"(sx[Z - 12]));
        else if constexpr (Z == 17 || Z == 22) asm volatile("v_permlane32_swap_b32 %0, %1" : "+v"(sx[0]), "+v"(sx[1]));
        else if constexpr (Z == 18) asm volatile("v_add_f32 %0, %0, %1" : "+v"(sx[0]) : "v"(sx[1]));
        else if constexpr (Z == 19) asm volatile("v_mul_f32 %0, 0x3a800000, %1" : "=v"(sx[1]) : "v"(sx[0]));
        else if constexpr (Z == 20) asm volatile("v_mul_f32 %0, 0x3a800000, %0" : "+v"(sx[0]));
        else if constexpr (Z == 23) asm volatile("v_mul_f32 %0, %1, %1" : "=v"(sx[2]) : "v"(sx[0]));
        else if constexpr (Z == 24) asm volatile("v_sub_f32 %0, %0, %1" : "+v"(sx[1]) : "v"(sx[2]));
        else if constexpr (Z == 25) asm volatile("v_max_f32 %0, 0, %0" : "+v"(sx[1]));
        else if constexpr (Z == 26) asm volatile("v_cvt_pk_bf16_f32 %0, %0, -%1" : "+v"(sx[1]) : "v"(sx[0]));
        else if constexpr (Z == 27) asm volatile("v_and_b32 %0, 0xffff0000, %1" : "=v"(sx[0]) : "v"(sx[1]));
        else if constexpr (Z == 28 || Z == 31) asm volatile("v_lshlrev_b32 %0, 16, %0" : "+v"(sx[1]));
        else if constexpr (Z == 29) asm volatile("v_add_f32 %0, 0x3727c5ac, %0" : "+v"(sx[1]));
        else if constexpr (Z == 30 || Z == 33) asm volatile("v_cvt_pk_bf16_f32 %0, %0, %0" : "+v"(sx[1]));
        else if constexpr (Z == 32) asm volatile("v_sqrt_f32 %0, %0" : "+v"(sx[1]));
        else if constexpr (Z == 34) asm volatile("v_lshlrev_b32 %0, 16, %1" : "=v"(sx[2]) : "v"(sx[1]));
        else if constexpr (Z == 35) asm volatile("v_rcp_f32 %0, %1" : "=v"(sx[1]) : "v"(sx[2]));
        else if constexpr (Z == 36) asm volatile("v_mul_f32 %0, %0, %0" : "+v"(sx[2]));
        else if constexpr (Z == 37) asm volatile("v_mul_f32 %0, 0xbfb8aa3b, %0" : "+v"(sx[1]));
        else if constexpr (Z == 16) asm volatile("v_and_b32 %0, 31, %1" : "=v"(sx[4]) : "v"(threadIdx.x));
        else if constexpr (Z == 21) asm volatile("v_lshl_add_u32 %0, %0, 4, %1" : "+v"(sx[4]) : "s"(lds0 + STL + wid * 512));
        else if constexpr (Z == 38) *(LDS_AS f32x4*)(size_t)(__float_as_uint(sx[4])) = f32x4{sx[0], sx[1], sx[2], sx[3]};
    };
    static_assert((2 * (NK - 2) + 1 - (2 * STK + 5)) * SPU >= 39, "statistics in time");
    f32x4 ccv[NI], stv[MI];
    const unsigned cca = gc.cca, cc6 = gc.cc6, sta = gc.sta, gof = gc.gof, gofm = gc.gofm, hsw = gc.hsw, hsr = gc.hsr;
    auto eld = [&](auto oc) INL {
        constexpr int O = decltype(oc)::value;
        constexpr int SI = O == 0 ? 0 : (O == 3 ? 1 : (O == 7 ? 2 : (O == 8 ? 3 : -1)));
        constexpr int J = O < 3 ? O * 3 - 3 : (O < 6 ? O * 3 - 11 : (O == 6 ? 6 : O * 3 - 25));
        if constexpr (SI >= 0) stv[SI] = *(const LDS_AS f32x4*)(size_t)(sta + (SI >> 1) * 512 + (SI & 1) * 128);
        else if constexpr (J == 6) ccv[6] = *(const LDS_AS f32x4*)(size_t)(cc6);
        else ccv[J] = *(const LDS_AS f32x4*)(size_t)(cca + ((J / 3) * 112 + (J % 3) * 16) * 4);
    };
    auto unit = [&](auto ktc, auto kkc) INL {
        constexpr int KT = decltype(ktc)::value, KK = decltype(kkc)::value;
        constexpr int NKT = KK ? KT + 1 : KT;
        static_for<0, MI * NI>([&](auto tc) INL {
            constexpr int T = decltype(tc)::value, X = KK * MI * NI + T, j = X / 8, i = j & 1 ? MI - 1 - (X / 2) % MI : (X / 2) % MI;
            if constexpr (KT == 0 && (X & 1) == 0) mma_z(cat(bf[0][4 * j], bf[0][4 * j + 1]), af[0][2 * i], acc[i][j]);
            else mma_ip(cat(bf[KT & 1][4 * j + 2 * (X & 1)], bf[KT & 1][4 * j + 2 * (X & 1) + 1]), af[KT & 1][2 * i + (X & 1)], acc[i][j]);
            SCHED();
            constexpr bool RQ = KT + 2 < NK && T < 4 * GGS;
            if constexpr (RQ && T % GGS == 0) prep(kkc, CT<T / GGS>{}, CT<KT + 2>{});
            if constexpr (RQ && T % GGS == 1) go(kkc, CT<T / GGS>{});
            constexpr int DX = T < 4 * GGS ? dreq((2 * (KT - NK + 2) + KK) * 4 + T / GGS) : -1;
            if constexpr (DX >= 0 && T % GGS == 0) dprep(CT<DX>{});
            if constexpr (DX >= 0 && T % GGS == 1) dgo(CT<DX>{});
            if constexpr (2 * (KT - NK + 2) + KK == TCU && T == TCT) touch1(SINK, wd, gc.tof);
            constexpr int O = T - (T + GGS - 2) / GGS;
            if constexpr (NKT < NK && T % GGS != 1 && O < (KK ? 12 : 36 - GL2)) ldop(CT<NKT>{}, CT<(KK ? O : O + GL2)>{});
            if constexpr (NKT < NK && KK == 1 && T >= 20 && T < 8 + GL2) ldop(CT<NKT>{}, CT<T - 8>{});
            if constexpr (KT == 0 && KK == 1 && T == GLT) asm volatile("global_load_dwordx2 %0, %1, %2 sc1" : "=v"(lk) : "v"(gc.f2), "s"(cs) : "memory");
            if constexpr (KT == STK && KK == 1 && T >= STT && T < STT + 4) gload16_devo<(T - STT) * 256>(stn[T - STT], spw, gc.sto);
            constexpr int Z = (2 * KT + KK - 2 * STK - 5) * SPU + T - (MI * NI - SPU);
            if constexpr (Z >= 0 && Z < 39 && T >= MI * NI - SPU) stats(CT<Z>{});
            if constexpr (NKT == NK && T < 11) eld(tc);
            if constexpr (KK == 0 && T == PRT && KT < NK - 1) __builtin_amdgcn_s_setprio(0);
            SCHED();
        });
    };

        asm volatile("" :: "s"(ra), "s"(rb[0]), "s"(rb[1]), "s"(rb[2]), "s"(rb[3]), "s"(b3d[0]), "s"(b3d[1]), "s"(rd), "s"(rr), "s"(dkh), "s"(spw), "s"(ldsw));
    static_for<0, 4>([&](auto pc) INL { prep(CT<1>{}, pc, CT<1>{}); asm volatile("s_nop 0"); go(CT<1>{}, pc); });
    asm volatile("s_cmp_eq_u32 %2, 0\n\ts_cselect_b64 exec, 1, 0\n\ts_waitcnt vmcnt(6)\n\ts_barrier\n\t"
                 "global_store_dwordx2 %3, %4, %5 sc1\n\ts_mov_b64 exec, -1\n\ts_cbranch_scc1 1f\n\ts_waitcnt vmcnt(4)\n"
                 "1:\n\ts_waitcnt vmcnt(5)"
                 : "+{v[194:195]}"(lk), "+{v[196:199]}"(cp) : "s"(wid), "v"(0u), "v"(tok),
                   "s"(wflags + (((id / 64) * 4 + id % 4) * 16 + (id % 64) / 4) * 32) : "memory", "scc");
    spin(lk, cs, gc.f1, tok);
    BARRIER();
    stage_a(CT<0>{});
    asm volatile("v_add_f32_dpp v196, v196, v196 row_ror:8 row_mask:0xf bank_mask:0xf\n\t"
                 "v_add_f32_dpp v197, v197, v197 row_ror:8 row_mask:0xf bank_mask:0xf\n\t"
                 "v_add_f32_dpp v198, v198, v198 row_ror:8 row_mask:0xf bank_mask:0xf\n\t"
                 "v_add_f32_dpp v199, v199, v199 row_ror:8 row_mask:0xf bank_mask:0xf\n\t"
                 "v_permlane16_swap_b32 v196, v197\n\ts_nop 0\n\tv_permlane16_swap_b32 v198, v199\n\t"
                 "v_add_f32 v196, v196, v197\n\tv_add_f32 v198, v198, v199\n\ts_nop 1\n\tv_permlane32_swap_b32 v196, v198\n\t"
                 "v_add_f32 v196, v196, v198\n\tds_write_b32 %1, v196" : "+{v[196:199]}"(cp) : "v"(gc.cw) : "memory");
    *(LDS_AS u32x2*)(size_t)(lds0 + GCN) = u32x2{0u, 0u};
    constexpr auto isa = [](int o) { return o == 2 || (o >= 5 && o < 12); };
    if constexpr (GPV >= 1) static_for<0, GL2>([&](auto oc) INL { if constexpr (!isa(decltype(oc)::value)) ldop(CT<0>{}, oc); });
    SCHED();
    if constexpr (GPV == 2) stage_a(CT<1>{});
    wait_vm<(GPV == 2 ? 4 : 0)>();
    BARRIER();
    if constexpr (GPV == 0) stage_a(CT<1>{});
    static_for<0, GL2>([&](auto oc) INL { if constexpr (GPV == 0 || isa(decltype(oc)::value)) { ldop(CT<0>{}, oc); SCHED(); } });
    SCHED();
    if constexpr (GPV == 1) stage_a(CT<1>{});
    static_for<0, NK>([&](auto kc) INL {
        constexpr int KT = decltype(kc)::value;
        unit(CT<KT>{}, CT<0>{});
        if constexpr (KT < NK - 1) {
            WAIT_LGKM0();
            wait_vm<(KT == STK + 1 ? 8 : (KT == NK - 2 ? DV : 4))>();
            if constexpr (KT == 1) spin(lk, cs, gc.f2, tok);
            BARRIER();
            if constexpr (PRT < 28) __builtin_amdgcn_s_setprio(2);
        }
        unit(CT<KT>{}, CT<1>{});
    });
    asm volatile("s_nop 15\n\ts_nop 7" ::: "memory");
    SCHED();
    const int tide = (int)opaque(threadIdx.x);
    {
        __builtin_amdgcn_s_setprio(0);
        auto center = [&](f32x4 a, f32x4 c, float nm) INL {
            float a0, a1, a2, a3;
            asm("v_fma_f32 %0, %4, %5, %9\n\tv_fma_f32 %1, %4, %6, %10\n\tv_fma_f32 %2, %4, %7, %11\n\tv_fma_f32 %3, %4, %8, %12"
                : "=&v"(a0), "=&v"(a1), "=&v"(a2), "=&v"(a3)
                : "v"(nm), "v"(c[0]), "v"(c[1]), "v"(c[2]), "v"(c[3]), "v"(a[0]), "v"(a[1]), "v"(a[2]), "v"(a[3]));
            return f32x4{a0, a1, a2, a3};
        };
        auto glu = [&](f32x4 gp, f32x4 up, float c, float k) INL {
            float t0, t1, t2, t3, u0 = up[0], u1 = up[1], u2 = up[2], u3 = up[3];
            asm("v_mul_f32 %0, %12, %8\n\tv_mul_f32 %1, %12, %9\n\tv_mul_f32 %2, %12, %10\n\tv_mul_f32 %3, %12, %11\n\t"
                "v_exp_f32 %0, %0\n\tv_exp_f32 %1, %1\n\tv_exp_f32 %2, %2\n\tv_exp_f32 %3, %3\n\t"
                "v_mul_f32 %4, %4, %8\n\tv_mul_f32 %5, %5, %9\n\t"
                "v_fma_f32 %0, %0, %13, %13\n\tv_fma_f32 %1, %1, %13, %13\n\t"
                "v_fma_f32 %2, %2, %13, %13\n\tv_fma_f32 %3, %3, %13, %13\n\t"
                "v_rcp_f32 %0, %0\n\tv_rcp_f32 %1, %1\n\tv_rcp_f32 %2, %2\n\tv_rcp_f32 %3, %3\n\t"
                "v_mul_f32 %6, %6, %10\n\tv_mul_f32 %7, %7, %11\n\t"
                "v_mul_f32 %4, %4, %0\n\tv_mul_f32 %5, %5, %1\n\tv_mul_f32 %6, %6, %2\n\tv_mul_f32 %7, %7, %3"
                : "=&v"(t0), "=&v"(t1), "=&v"(t2), "=&v"(t3), "+v"(u0), "+v"(u1), "+v"(u2), "+v"(u3)
                : "v"(gp[0]), "v"(gp[1]), "v"(gp[2]), "v"(gp[3]), "v"(c), "v"(k));
            return u32x2{pk2(u0, u1), pk2(u2, u3)};
        };
        auto centerp = [&](f32x4 a, f32x4 c, float nm) INL {
            const f32x2 n2 = {nm, nm};
            const f32x2 lo = __builtin_elementwise_fma(n2, c.xy, a.xy), hi = __builtin_elementwise_fma(n2, c.zw, a.zw);
            return f32x4{lo.x, lo.y, hi.x, hi.y};
        };
        auto glu2 = [&](f32x2 gp, f32x2 up, float c, float k) INL {
            const f32x2 ea = gp * f32x2{c, c}, k2 = {k, k};
            const f32x2 d = __builtin_elementwise_fma(f32x2{__builtin_amdgcn_exp2f(ea.x), __builtin_amdgcn_exp2f(ea.y)}, k2, k2);
            const f32x2 hv = (gp * up) * f32x2{__builtin_amdgcn_rcpf(d.x), __builtin_amdgcn_rcpf(d.y)};
            return pk2(hv.x, hv.y);
        };
        auto glup = [&](f32x4 gp, f32x4 up, float c, float k) INL {
            return u32x2{glu2(gp.xy, up.xy, c, k), glu2(gp.zw, up.zw, c, k)};
        };
        u32x2 hp[2];
        auto blk = [&](int i, int j, auto pkc) INL {
            if constexpr (decltype(pkc)::value)
                hp[i & 1] = glup(centerp(acc[i][j], ccv[j], stv[i][0]), centerp(acc[i][j + 3], ccv[j + 3], stv[i][0]), stv[i][1], stv[i][2]);
            else
                hp[i & 1] = glu(center(acc[i][j], ccv[j], stv[i][0]), center(acc[i][j + 3], ccv[j + 3], stv[i][0]), stv[i][1], stv[i][2]);
        };
        auto blk2 = [&](int i, int j) INL {
            f32x2 gp[4], up[4], ea[4];
#pragma unroll
            for (int x = 0; x < 4; ++x) {
                const f32x4 a = acc[i + (x >> 1)][j], c = ccv[j];
                const float nm = stv[i + (x >> 1)][0];
                gp[x] = __builtin_elementwise_fma(f32x2{nm, nm}, x & 1 ? c.zw : c.xy, x & 1 ? a.zw : a.xy);
            }
            SCHED();
#pragma unroll
            for (int x = 0; x < 4; ++x) { const float c = stv[i + (x >> 1)][1]; ea[x] = gp[x] * f32x2{c, c}; }
            SCHED();
#pragma unroll
            for (int x = 0; x < 4; ++x) {
                const f32x4 a = acc[i + (x >> 1)][j + 3], c = ccv[j + 3];
                const float nm = stv[i + (x >> 1)][0];
                ea[x] = f32x2{__builtin_amdgcn_exp2f(ea[x].x), __builtin_amdgcn_exp2f(ea[x].y)};
                up[x] = __builtin_elementwise_fma(f32x2{nm, nm}, x & 1 ? c.zw : c.xy, x & 1 ? a.zw : a.xy);
                SCHED();
            }
#pragma unroll
            for (int x = 0; x < 4; ++x) { const float k = stv[i + (x >> 1)][2]; ea[x] = __builtin_elementwise_fma(ea[x], f32x2{k, k}, f32x2{k, k}); }
            SCHED();
#pragma unroll
            for (int x = 0; x < 4; ++x) {
                ea[x] = f32x2{__builtin_amdgcn_rcpf(ea[x].x), __builtin_amdgcn_rcpf(ea[x].y)};
                up[x] = gp[x] * up[x];
                SCHED();
            }
#pragma unroll
            for (int x = 0; x < 4; ++x) up[x] = up[x] * ea[x];
            SCHED();
#pragma unroll
            for (int x = 0; x < 4; ++x) hp[x & 1][x >> 1] = pk2(up[(x & 1) * 2 + (x >> 1)].x, up[(x & 1) * 2 + (x >> 1)].y);
        };
        auto flush = [&](int hf, int j) INL {
            const auto t0 = __builtin_amdgcn_permlane16_swap(hp[0][0], hp[1][0], false, false);
            const auto t1 = __builtin_amdgcn_permlane16_swap(hp[0][1], hp[1][1], false, false);
            store16_wt(C, gof + hf * 8 * (8 * F_) + j * 128, u32x4{t0[0], t1[0], t0[1], t1[1]});
            SCHED();
        };
        auto head = [&](auto pkc) INL { blk(0, 0, pkc); blk(1, 0, pkc); flush(0, 0); blk(0, 1, pkc); };
        auto sync = [&]() INL {
            WAIT_LGKM0();
            BARRIER();
            asm volatile("" ::: "memory");
            SCHED();
        };
        const bool late = wid >= 4;
        if (!late) head(CT<0>{});
        sync();
#pragma unroll
        for (int i = 0; i < MI; ++i)
#pragma unroll
            for (int j = 0; j < NI; ++j) asm volatile("" : "+v"(acc[i][j]));
#pragma unroll
        for (int i = 0; i < MI; i += 2) {
            const f32x4 x = centerp(acc[i][6], ccv[6], stv[i][0]), y = centerp(acc[i + 1][6], ccv[6], stv[i + 1][0]);
            f32x4 gm, um;
#pragma unroll
            for (int r = 0; r < 4; ++r) {
                const auto t = __builtin_amdgcn_permlane32_swap(__float_as_uint(x[r]), __float_as_uint(y[r]), false, false);
                gm[r] = __uint_as_float(t[0]); um[r] = __uint_as_float(t[1]);
            }
            const bool hi = (tide & 32) != 0;
            *(LDS_AS u32x2*)(size_t)(hsw + i * 512) =
                glup(gm, um, hi ? stv[i + 1][1] : stv[i][1], hi ? stv[i + 1][2] : stv[i][2]);
        }
        u32x4 mv;
        auto mixed = [&]() INL {
            wait_vm<2>();
            sync();
            mv = *(const LDS_AS u32x4*)(size_t)(hsr);
            SCHED();
        };
        if constexpr (MXP == 0) { mixed(); store16_wt(C, gofm, mv); }
        if (late) {
            __builtin_amdgcn_s_setprio(LPR);
            blk2(0, 0);
            flush(0, 0);
            blk2(0, 1);
        } else {
            blk(1, 1, CT<1>{});
        }
        flush(0, 1);
        if constexpr (MXP == 1) mixed();
        blk2(0, 2);
        if constexpr (MXP == 1) store16_wt(C, gofm, mv);
        flush(0, 2);
#pragma unroll
        for (int j = 0; j < 3; ++j) {
            blk2(2, j);
            flush(1, j);
        }
        __builtin_amdgcn_s_setprio(0);
        if (!late) {
            pubc<0>(gflags + (tm * 64 + tn) * 32, tok, lds0 + GCN);
            if (wid == 2) code_touch(SINK, tide & 63);
        }
    }
}

constexpr long long W_OB = 12, W_H = 16, W_XA = 32, W_XB = 36, W_CS = 40, W_SPA = 42, W_SPB = 43, W_FL = 44, W_EP = 45, W_SL = 48, W_L1 = 48;
extern "C" __global__ __launch_bounds__(512, 2) void body_k(const u16* __restrict__ x0, const u16* __restrict__ wqkv,
                                                 const u16* __restrict__ wo, const u16* __restrict__ wg,
                                                 const u16* __restrict__ wu, const u16* __restrict__ wd, u16* out,
                                                 char* ws) {
    extern __shared__ __attribute__((aligned(16))) char smem[];
    {
        const int tid = threadIdx.x, lane = tid & 63, wid = __builtin_amdgcn_readfirstlane(tid >> 6);
        if (wid >= 2 && wid < 6) {
            const int id = (blockIdx.x & 7) * 32 + (blockIdx.x >> 3);
            const int tm = (id / 128) * 8 + id % 8, tn = (id % 128) / 8, h = wid - 2;
            const int row = h * 8 + (lane >> 3), hr = h * 4 + ((lane >> 3) & 3);
            const unsigned la = (unsigned)((tm * 128 + row) * D_ + ((lane & 7) ^ ((row >> 1) & 7)) * 8) * 2u;
            const unsigned lh = (unsigned)((tm ? tm * 128 - 16 + hr : 0) * D_ + ((lane & 7) ^ ((hr >> 1) & 7)) * 8) * 2u;
            const unsigned lb = b_off(h, lane, 3 * D_, tn * 64);
#pragma unroll 1
            for (int kt = 0; kt < 2; ++kt) {
#pragma unroll
                for (int i = 0; i < 4; ++i) glds16(uni((const char*)x0 + kt * 128 + i * 32 * D_ * 2), la, (2 - 2 * kt) * QAS + h * 1024 + i * 4096);
                glds16_mask(uni((const char*)x0 + kt * 128), lh, (2 - 2 * kt) * QAS + 16384 + h * 512, h == 0 ? 0xffffff00ull : 0xffffffffull);
                qkv_stage_b(0, wqkv, lb, h, kt, 2 - 2 * kt);
            }
        } else if (wid == 1) {
            code_touch(SINK, lane);
        }
    }
    u64 tok = 0;
#pragma unroll 1
    for (int L = 0; L < 2; ++L) {
        auto at = [&](long long mb, int l) INL { return sopq(ws) + ((mb + l * W_L1) << 20); };
        auto fl = [&](int ph) INL { return (unsigned*)at(W_FL, 0) + 16384 + ph * 8192; };
        qkv_phase(smem, L ? (u16*)at(W_XB, 0) : x0, sopq(wqkv), (u16*)at(W_OB, L), (float*)at(W_CS, 0), (float*)at(W_SPB, 0), fl(4),
                  fl(7 - L), sopq(wo), L == 0, sopq(wg), sopq(wu), (u64*)at(W_EP, 0), tok);
        const float* const cs = (float*)at(W_CS, 0);
        GuC gc;
        f32x4 cp;
        u64 lk;
        wo_phase(smem, (u16*)at(W_OB, L), sopq(wo), (u16*)at(W_XA, L), (float*)at(W_SPA, L), fl(7 - L),
                 L == 0, sopq(wg), sopq(wu), cs, CSO + (2 + L) * 32768, gc, cp, lk, tok);
        gu_phase(smem, (u16*)at(W_XA, L), sopq(wg), sopq(wu), (u16*)at(W_H, L), cs, (float*)at(W_SPA, L), sopq(wd),
                 fl(L), fl(8 + 2 * L), gc, cp, lk, tok);
        dn_phase(smem, (u16*)at(W_H, L), sopq(wd), L ? out : (u16*)at(W_XB, 0), L ? nullptr : (float*)at(W_SPB, 0),
                 (float*)at(W_SL, L), fl(8 + 2 * L), fl(4 + L), L ? nullptr : sopq(wqkv), tok);
    }
    asm volatile(".Lbody_end:");
}

extern "C" void kernel_launch(void* const* d_in, const int* in_sizes, int n_in, void* d_out, int out_size,
                              void* d_ws, size_t ws_size, hipStream_t stream) {
    const u16* x0 = (const u16*)d_in[0];
    const u16* wqkv = (const u16*)d_in[1];
    const u16* wo = (const u16*)d_in[2];
    const u16* wg = (const u16*)d_in[4];
    const u16* wu = (const u16*)d_in[5];
    const u16* wd = (const u16*)d_in[6];
    constexpr int smem = 163840;
    (void)hipFuncSetAttribute((const void*)body_k, hipFuncAttributeMaxDynamicSharedMemorySize, smem);
    body_k<<<256, 512, smem, stream>>>(x0, wqkv, wo, wg, wu, wd, (u16*)d_out, (char*)d_ws);
}
```

```cpp
#include <hip/hip_runtime.h>
#include <hip/hip_bf16.h>
#include <stdint.h>

#pragma clang diagnostic ignored "-Winline-asm"
typedef unsigned short u16;
using bf16x8 = __attribute__((ext_vector_type(8))) short;
using s16x4  = __attribute__((ext_vector_type(4))) short;
using f32x4  = __attribute__((ext_vector_type(4))) float;
using f32x2  = __attribute__((ext_vector_type(2))) float;
using u32x2  = __attribute__((ext_vector_type(2))) unsigned;
using u32x4  = __attribute__((ext_vector_type(4))) unsigned;
typedef __bf16 bf2_t __attribute__((ext_vector_type(2)));
#define LDS_AS __attribute__((address_space(3)))
#define INL __attribute__((always_inline))

constexpr int WNB = 4, WS0 = 2, WSD = 3, WNG = 3, WPS = 2, GLT = 20, WLK = 2, WL2 = 0, DLK = 2, DL2 = 0, GPV = 2,
              TCU = -1, TCT = 25, STT = 22, STK = 10, MXP = 1, LPR = 1, SPU = 10, PRT = 20, WOLD = 12, WOBAR = 4,
              GL2 = 16, QL1 = 16, QG1 = 10, GGS = 6, PV0 = 6, HPRIO = 2, SLP = 1, KIN = 9, KSKIP = 2, NSOLO = 4,
              QPS = 0, DNE = 26, DPAR = 0, XO = 5, XA = 28, XB = 8, DBAR = 4, DBAR2 = 4, CSP = 1, WT0 = 11, WT1 = 12,
              QBT = 12, GBT = 6, GW2 = 0, QSO = 0, QSP = 2, WN4 = 2, MKP = 0, WN1 = 3, WN2 = 2, WN3 = 3;
constexpr unsigned long long DSCH = 0x87654321a9000000ull;
constexpr int S_ = 2048, D_ = 1024, F_ = 3584;

__device__ __forceinline__ float bf2f(u16 v) { return __uint_as_float(((unsigned)v) << 16); }
__device__ __forceinline__ u16 f2bf(float f) {
    __hip_bfloat16 h = __float2bfloat16(f);
    return *reinterpret_cast<u16*>(&h);
}
__device__ __forceinline__ float rbf(float f) { return bf2f(f2bf(f)); }
__device__ __forceinline__ unsigned pk2(float a, float b) {
    const f32x2 v = {a, b};
    return __builtin_bit_cast(unsigned, __builtin_convertvector(v, bf2_t));
}
__device__ __forceinline__ float addf(float a, float b) {
    float r;
    asm("v_add_f32 %0, %1, %2" : "=v"(r) : "v"(a), "v"(b));
    return r;
}
__device__ __forceinline__ float mulf(float a, float b) {
    float r;
    asm("v_mul_f32 %0, %1, %2" : "=v"(r) : "v"(a), "v"(b));
    return r;
}
__device__ __forceinline__ float subf(float a, float b) {
    float r;
    asm("v_sub_f32 %0, %1, %2" : "=v"(r) : "v"(a), "v"(b));
    return r;
}

template <int V> struct CT { static constexpr int value = V; };
template <int I, int N, class F> __device__ __forceinline__ void static_for(F&& f) {
    if constexpr (I < N) { f(CT<I>{}); static_for<I + 1, N>(f); }
}

#define WAIT_LGKM0() asm volatile("s_waitcnt lgkmcnt(0)" ::: "memory")
#define BARRIER() do { asm volatile("" ::: "memory"); __builtin_amdgcn_s_barrier(); asm volatile("" ::: "memory"); } while (0)
#define SCHED() __builtin_amdgcn_sched_barrier(0)
template <int N> __device__ __forceinline__ void wait_vm() {
    static_assert(N >= 0 && N < 64, "vmcnt range");
    asm volatile("s_waitcnt vmcnt(%0)" :: "n"(N) : "memory");
}

__device__ __forceinline__ void glds16(const void* sbase, unsigned voff, unsigned lds_dst) {
    asm volatile("s_mov_b32 m0, %2\n\ts_nop 3\n\tglobal_load_lds_dwordx4 %0, %1" :: "v"(voff), "s"(sbase), "s"(__builtin_amdgcn_readfirstlane(lds_dst)) : "memory", "m0");
}
template <int NLANE>
__device__ __forceinline__ void glds16_part(const void* sbase, unsigned voff, unsigned lds_dst) {
    unsigned long long ex;
    asm volatile("s_mov_b64 %0, exec\n\ts_mov_b32 m0, %3\n\ts_mov_b64 exec, %4\n\ts_nop 1\n\t"
                 "global_load_lds_dwordx4 %1, %2 sc1\n\ts_mov_b64 exec, %0"
                 : "=&s"(ex) : "v"(voff), "s"(sbase), "s"(__builtin_amdgcn_readfirstlane(lds_dst)), "n"((1ull << NLANE) - 1) : "memory", "m0");
}
__device__ __forceinline__ void glds16_mask(const void* sbase, unsigned voff, unsigned lds_dst, unsigned long long mask) {
    unsigned long long ex;
    asm volatile("s_mov_b64 %0, exec\n\ts_mov_b32 m0, %3\n\ts_mov_b64 exec, %4\n\ts_nop 1\n\t"
                 "global_load_lds_dwordx4 %1, %2\n\ts_mov_b64 exec, %0"
                 : "=&s"(ex) : "v"(voff), "s"(sbase), "s"(__builtin_amdgcn_readfirstlane(lds_dst)), "s"(mask) : "memory", "m0");
}
__device__ __forceinline__ void glds16_dev(const void* sbase, unsigned voff, unsigned lds_dst) {
    asm volatile("s_mov_b32 m0, %2\n\ts_nop 3\n\tglobal_load_lds_dwordx4 %0, %1 sc1" :: "v"(voff), "s"(sbase), "s"(__builtin_amdgcn_readfirstlane(lds_dst)) : "memory", "m0");
}
template <int V> __device__ __forceinline__ void s_set(unsigned& d) { asm volatile("s_mov_b32 %0, %1" : "=s"(d) : "n"(V)); }
template <int V> __device__ __forceinline__ void m0_set(unsigned base) { asm volatile("s_add_i32 m0, %0, %1" :: "s"(base), "n"(V) : "m0", "scc"); }
template <bool DEV> __device__ __forceinline__ void blds16(__amdgpu_buffer_rsrc_t r, unsigned voff, unsigned soff) {
    if constexpr (DEV) asm volatile("buffer_load_dwordx4 %0, %1, %2 offen sc1 lds" :: "v"(voff), "s"(r), "s"(soff) : "memory");
    else asm volatile("buffer_load_dwordx4 %0, %1, %2 offen lds" :: "v"(voff), "s"(r), "s"(soff) : "memory");
}
__device__ __forceinline__ void gload16_dev(f32x4& d, const void* sbase, unsigned voff) {
    asm volatile("s_nop 4\n\tglobal_load_dwordx4 %0, %1, %2 sc1" : "=v"(d) : "v"(voff), "s"(sbase) : "memory");
}
__device__ __forceinline__ void gload8_dev(unsigned long long& d, const void* p) {
    asm volatile("global_load_dwordx2 %0, %1, off sc1" : "=v"(d) : "v"(p) : "memory");
}
template <int OFF, class T> __device__ __forceinline__ void gload16_devo(T& d, const void* sbase, unsigned voff) {
    asm volatile("global_load_dwordx4 %0, %1, %2 offset:%3 sc1" : "=v"(d) : "v"(voff), "s"(sbase), "n"(OFF) : "memory");
}
template <int OFF> __device__ __forceinline__ void gload16(f32x4& d, const void* sbase, unsigned voff) {
    asm volatile("s_nop 4\n\tglobal_load_dwordx4 %0, %1, %2 offset:%3" : "=v"(d) : "v"(voff), "s"(sbase), "n"(OFF) : "memory");
}
template <int G> __device__ __forceinline__ void gload4_if(f32x4& a, f32x4& b, f32x4& c, f32x4& d, unsigned& voff, const void* sbase, int st, int g) {
    asm volatile("s_cmp_lg_u32 %7, %8\n\ts_cbranch_scc1 .Lwb%=\n\ts_nop 2\n\t"
                 "global_load_dwordx4 %0, %4, %5\n\tv_add_u32 %4, %6, %4\n\tglobal_load_dwordx4 %1, %4, %5\n\tv_add_u32 %4, %6, %4\n\t"
                 "global_load_dwordx4 %2, %4, %5\n\tv_add_u32 %4, %6, %4\n\tglobal_load_dwordx4 %3, %4, %5\n\tv_add_u32 %4, %6, %4\n.Lwb%=:"
                 : "+v"(a), "+v"(b), "+v"(c), "+v"(d), "+v"(voff) : "s"(sbase), "s"(st), "s"(g), "n"(G) : "memory", "scc");
}
template <int N1, int N0, int BIT> __device__ __forceinline__ void wait_vm_if(unsigned m) {
    asm volatile("s_waitcnt vmcnt(%1)\n\ts_bitcmp1_b32 %0, %3\n\ts_cbranch_scc1 .Lwq%=\n\ts_waitcnt vmcnt(%2)\n.Lwq%=:"
                 :: "s"(m), "n"(N1), "n"(N0), "n"(BIT) : "memory", "scc");
}
constexpr unsigned SINK = 163584;
__device__ __forceinline__ void touch1(unsigned sink, const void* sbase, unsigned voff) {
    asm volatile("s_mov_b32 m0, %2\n\ts_nop 3\n\tglobal_load_lds_dword %0, %1" :: "v"(voff), "s"(sbase), "s"(sink) : "memory", "m0");
}
__device__ __forceinline__ const char* uni(const char* p) {
    const unsigned long long v = (unsigned long long)p;
    const unsigned lo = __builtin_amdgcn_readfirstlane((unsigned)v), hi = __builtin_amdgcn_readfirstlane((unsigned)(v >> 32));
    return (const char*)(((unsigned long long)hi << 32) | lo);
}
__device__ __forceinline__ void store16_wt(void* base, unsigned off, u32x4 v) {
    const __amdgpu_buffer_rsrc_t r = __builtin_amdgcn_make_buffer_rsrc(base, 0, 0x7fffffff, 0x00020000);
    __builtin_amdgcn_raw_buffer_store_b128(v, r, off, 0, 16);
}
__device__ __forceinline__ unsigned opaque(unsigned v) { asm volatile("" : "+v"(v)); return v; }
__device__ __forceinline__ unsigned bid_() { unsigned b = blockIdx.x; asm volatile("" : "+s"(b)); return b; }
__device__ __forceinline__ int wg_id() { const unsigned b = bid_(); return (b & 7) * 32 + (b >> 3); }
template <class T> __device__ __forceinline__ T* sopq(T* p) { asm volatile("" : "+s"(p)); return p; }
__device__ __forceinline__ void code_touch(unsigned sink, unsigned lane) {
    const char* pc;
    int off;
    unsigned len;
    asm volatile("s_getpc_b64 %0\n.Lct%=:\n\ts_mov_b32 %1, body_k-.Lct%=\n\ts_mov_b32 %2, .Lbody_end-body_k"
                 : "=s"(pc), "=s"(off), "=s"(len));
    const unsigned o = ((bid_() >> 3) + lane * 32u) * 128u;
    touch1(sink, pc + off, o < len ? o : 0u);
}
template <int OFF> __device__ __forceinline__ bf16x8 lds_a(unsigned base) {
    static_assert(OFF >= 0 && OFF < 65536, "ds offset");
    return *(const LDS_AS bf16x8*)(size_t)(base + OFF);
}
template <int OFF> __device__ __forceinline__ s16x4 lds_h(unsigned base) {
    static_assert(OFF >= 0 && OFF < 65536, "ds offset");
    return __builtin_amdgcn_ds_read_tr16_b64_v4i16((LDS_AS s16x4*)(size_t)(base + OFF));
}
__device__ __forceinline__ bf16x8 cat(s16x4 lo, s16x4 hi) { return __builtin_shufflevector(lo, hi, 0, 1, 2, 3, 4, 5, 6, 7); }
__device__ __forceinline__ f32x4 mma(bf16x8 a, bf16x8 b, f32x4 c) {
    return __builtin_amdgcn_mfma_f32_16x16x32_bf16(a, b, c, 0, 0, 0);
}

__device__ __forceinline__ void mma_ip(bf16x8 a, bf16x8 b, f32x4& c) {
    asm volatile("v_mfma_f32_16x16x32_bf16 %0, %1, %2, %0" : "+v"(c) : "v"(a), "v"(b));
}
__device__ __forceinline__ void mma_z(bf16x8 a, bf16x8 b, f32x4& c) {
    asm volatile("v_mfma_f32_16x16x32_bf16 %0, %1, %2, 0" : "=&v"(c) : "v"(a), "v"(b));
}

constexpr int CSTR = 64 * 2 + 16;
struct ResC { unsigned rd, co, so; };
__device__ __forceinline__ void res_setup(ResC& c, unsigned lds, int m0, int n0, int tid) {
    const int row = (tid >> 6) * 16 + ((tid >> 3) & 7), ch = tid & 7;
    c.rd = opaque(lds + row * CSTR + ch * 16);
    c.co = opaque((unsigned)((m0 + row) * D_ + n0 + ch * 8) * 2u);
    c.so = opaque((unsigned)(((m0 + row) >> 5) * 512 + (n0 >> 6) * 32 + (row & 31) + (ch >> 2) * 8) * 8u);
}
__device__ __forceinline__ void res_tail(const ResC& c, u16* C, float* sp) {
    const u32x4 v = *(const LDS_AS u32x4*)(size_t)c.rd, w = *(const LDS_AS u32x4*)(size_t)(c.rd + 8 * CSTR);
    const __amdgpu_buffer_rsrc_t rc = __builtin_amdgcn_make_buffer_rsrc(C, 0, 0x7fffffff, 0x00020000);
    __builtin_amdgcn_raw_buffer_store_b128(v, rc, c.co, 0, 16);
    __builtin_amdgcn_raw_buffer_store_b128(w, rc, c.co, 8 * D_ * 2, 16);
    SCHED();
    if (sp == nullptr) return;
    unsigned s0, q0, s1, q1;
    asm("v_dot2_f32_bf16 %0, %4, %12, 0\n\tv_dot2_f32_bf16 %1, %4, %4, 0\n\tv_dot2_f32_bf16 %0, %5, %12, %0\n\tv_dot2_f32_bf16 %1, %5, %5, %1\n\t"
        "v_dot2_f32_bf16 %0, %6, %12, %0\n\tv_dot2_f32_bf16 %1, %6, %6, %1\n\tv_dot2_f32_bf16 %0, %7, %12, %0\n\tv_dot2_f32_bf16 %1, %7, %7, %1\n\t"
        "v_dot2_f32_bf16 %2, %8, %12, 0\n\tv_dot2_f32_bf16 %3, %8, %8, 0\n\tv_dot2_f32_bf16 %2, %9, %12, %2\n\tv_dot2_f32_bf16 %3, %9, %9, %3\n\t"
        "v_dot2_f32_bf16 %2, %10, %12, %2\n\tv_dot2_f32_bf16 %3, %10, %10, %3\n\tv_dot2_f32_bf16 %2, %11, %12, %2\n\tv_dot2_f32_bf16 %3, %11, %11, %3\n\t"
        "v_add_f32_dpp %0, %0, %0 row_half_mirror row_mask:0xf bank_mask:0x5\n\tv_add_f32_dpp %1, %1, %1 row_half_mirror row_mask:0xf bank_mask:0x5\n\t"
        "v_add_f32_dpp %0, %2, %2 row_half_mirror row_mask:0xf bank_mask:0xa\n\tv_add_f32_dpp %1, %3, %3 row_half_mirror row_mask:0xf bank_mask:0xa\n\t"
        "s_nop 0\n\tv_add_f32_dpp %0, %0, %0 quad_perm:[1,0,3,2] row_mask:0xf bank_mask:0xf\n\t"
        "v_add_f32_dpp %1, %1, %1 quad_perm:[1,0,3,2] row_mask:0xf bank_mask:0xf\n\t"
        "s_nop 0\n\tv_add_f32_dpp %0, %0, %0 quad_perm:[2,3,0,1] row_mask:0xf bank_mask:0xf\n\t"
        "v_add_f32_dpp %1, %1, %1 quad_perm:[2,3,0,1] row_mask:0xf bank_mask:0xf"
        : "=&v"(s0), "=&v"(q0), "=&v"(s1), "=&v"(q1)
        : "v"(v[0]), "v"(v[1]), "v"(v[2]), "v"(v[3]), "v"(w[0]), "v"(w[1]), "v"(w[2]), "v"(w[3]), "s"(0x3f803f80u));
    const __amdgpu_buffer_rsrc_t r = __builtin_amdgcn_make_buffer_rsrc(sp, 0, 0x7fffffff, 0x00020000);
    __builtin_amdgcn_raw_buffer_store_b64(u32x2{s0, q0}, r, c.so, 0, 16);
}
__device__ __forceinline__ bf16x8 pick(bool on, int e) {
    const unsigned one = on ? 0x3f80u << (e & 1) * 16 : 0u;
    bf16x8 m = __builtin_bit_cast(bf16x8, u32x4{(e >> 1) == 0 ? one : 0u, (e >> 1) == 1 ? one : 0u, (e >> 1) == 2 ? one : 0u, (e >> 1) == 3 ? one : 0u});
    asm volatile("" : "+v"(m));
    return m;
}

constexpr int ATX = 155648;
__device__ __forceinline__ void att_put(char* smem, int lane, int m0) {
    const int l15 = lane & 15, g = lane >> 4;
    u32x4 cx, cy, c0, ps;
#pragma unroll
    for (int r = 0; r < 4; ++r) {
        const int e = l15 - g * 4 - r, eh = ((l15 - g * 4) & ~1) - (r & 1) * 2;
        cx[r] = (e & 1) == 0 && e < 0 ? 0u : 0xff800000u;
        cy[r] = (e & 1) == 0 && e >= 0 ? 0u : 0xff800000u;
        c0[r] = m0 ? cx[r] : 0xff800000u;
        const unsigned hs = r & 1 ? 0x0302u : 0x0100u;
        ps[r] = (eh < 0) == (r < 2) ? (l15 & 1 ? hs << 16 | 0x0c0cu : 0x0c0c0000u | hs) : 0x0c0c0c0cu;
    }
    u32x4* const p = (u32x4*)(smem + ATX) + lane;
    p[0] = cx; p[64] = cy; p[128] = c0; p[192] = ps;
    *(unsigned*)(smem + 57088 + (lane & 1) * 4) = 0u;
}
using u32x8 = __attribute__((ext_vector_type(8))) unsigned;
#define AMM "v_mfma_f32_16x16x32_bf16 "
#define ACV "v_cvt_pk_bf16_f32 "
#define AX1(s) "v_max3_f32 v[" s "], v[" s "], v[" s "+1], v[" s "+4]\n\t"
#define AX2(s) "v_max3_f32 v[" s "+2], v[" s "+2], v[" s "+3], v[" s "+6]\n\t"
#define AX3(s) "v_max_f32 v[" s "], v[" s "], v[" s "+5]\n\t"
#define AX4(s) "v_max_f32 v[" s "+2], v[" s "+2], v[" s "+7]\n\t"
#define AOP(op, d, s) op " v" d ", v[" s "], v[" s "+2]\n\t"
#define AO2(op, d, e, a, b) op " v" d ", v" a ", v" b "\n\t" op " v" e ", v" a ", v" b "\n\t"
#define ASB(s, n) "v_sub_f32 v[" s "], v[" s "], v" n "\n\tv_sub_f32 v[" s "+2], v[" s "+2], v" n "\n\t"
#define AEX(s) "v_exp_f32 v[" s "], v[" s "]\n\t"
#define AMU(s, n) "v_mul_f32 v[" s "], v[" s "], v" n "\n\t"
#define APK(s) ACV "v[" s "+1], v[" s "], v[" s "+2]\n\t"
#define APM(s, i) "v_perm_b32 v[" s "+4+" i "], v[" s "+1], v[" s "+1], v[240+" i "]\n\t"
#define APV(o, v, s) AMM "v[" o ":" o "+3], v[" v ":" v "+3], v[" s "+4:" s "+7], 0\n\t"
#define AOC(s, o) ACV "v[" s "], v[" o "], v[" o "+1]\n\t" ACV "v[" s "+1], v[" o "+2], v[" o "+3]\n\t"
#define AST(s, off) "buffer_store_dwordx4 v[" s ":" s "+3], %[oo], %[rc], 0 offen offset:" off " sc1\n\t"
#define S16(a, b) "v_permlane16_swap_b32 v" a ", v" b "\n\t"
#define S32(a, b) "v_permlane32_swap_b32 v" a ", v" b "\n\t"
__device__ __forceinline__ void att_run(const u32x4 (&q)[4], const u32x4 (&k)[4], const f32x4 (&v)[3][2], const u32x2 (&v3)[2],
                                        const u32x4 (&am)[4], const f32x2 (&rk)[4], const f32x2 (&sc)[4], unsigned xa, unsigned oo, u16* C) {
    u32x4 kx;
    u32x8 s3, s2, w0, w1;
    asm volatile("ds_read_b128 %2, %3\n\tds_read_b64 v[164:165], %3 offset:1024\n\t"
                 AMM "v[228:231], %4, %6, %8\n\t" ACV "v166, %9, %10\n\t" ACV "v167, %11, %12\n\t" ACV "v168, %13, %14\n\t"
                 AMM "v[224:227], %5, %6, %7\n\t" ACV "v169, %15, %16\n\t" ACV "v170, %17, %18\n\t" ACV "v171, %19, %20"
                 : "=&{v[224:231]}"(s3), "=&{v[164:171]}"(w0), "=&v"(kx)
                 : "v"(xa), "v"(k[3]), "v"(k[2]), "v"(q[3]), "v"(am[0]), "v"(am[1]),
                   "v"(v[0][0][0]), "v"(v[0][0][1]), "v"(v[0][0][2]), "v"(v[0][0][3]), "v"(v[1][0][0]), "v"(v[1][0][1]),
                   "v"(v[1][0][2]), "v"(v[1][0][3]), "v"(v[2][0][0]), "v"(v[2][0][1]), "v"(v[2][0][2]), "v"(v[2][0][3]) : "memory");
    asm volatile("ds_read_b64 v[174:175], %2 offset:1032\n\t"
                 AMM "v[220:223], %3, %5, %7\n\t" ACV "v176, %8, %9\n\t" ACV "v177, %10, %11\n\t" ACV "v178, %12, %13\n\t"
                 AMM "v[216:219], %4, %5, %6\n\t" ACV "v179, %14, %15\n\t" ACV "v180, %16, %17\n\t" ACV "v181, %18, %19"
                 : "=&{v[216:223]}"(s2), "=&{v[174:181]}"(w1)
                 : "v"(xa), "v"(k[2]), "v"(k[1]), "v"(q[2]), "v"(am[0]), "v"(am[1]),
                   "v"(v[0][1][0]), "v"(v[0][1][1]), "v"(v[0][1][2]), "v"(v[0][1][3]), "v"(v[1][1][0]), "v"(v[1][1][1]),
                   "v"(v[1][1][2]), "v"(v[1][1][3]), "v"(v[2][1][0]), "v"(v[2][1][1]), "v"(v[2][1][2]), "v"(v[2][1][3]) : "memory");
    const __amdgpu_buffer_rsrc_t rc = __builtin_amdgcn_make_buffer_rsrc(C, 0, 0x7fffffff, 0x00020000);
    asm volatile(AMM "v[212:215], %[k1], %[q1], %[cy]\n\t" AX1("224") AX2("224") AX3("224")
                 AMM "v[208:211], %[k0], %[q1], %[cx]\n\t" AX4("224") AMU("224", "195") AMU("226", "199")
                 AMM "v[204:207], %[k0], %[q0], %[cy]\n\t" AOP("v_max_f32", "232", "224") AX1("216") AX2("216") "s_waitcnt lgkmcnt(0)\n\t"
                 AMM "v[200:203], %[kx], %[q0], %[c0]\n\t" AX3("216") AX4("216") AMU("216", "194") AMU("218", "198")
                 AOP("v_max_f32", "233", "216") AX1("208") AX2("208")
                 S16("232", "233") AX3("208") AO2("v_max_f32", "236", "237", "232", "233") AX4("208") AMU("208", "193")
                 S32("236", "237") AMU("210", "197") AO2("v_max_f32", "232", "233", "236", "237") AOP("v_max_f32", "234", "208") AX1("200")
                 S16("232", "233") AX2("200") AX3("200") AX4("200") ASB("224", "232") AMU("200", "192") AMU("202", "196")
                 AOP("v_max_f32", "235", "200") ASB("216", "233")
                 S16("234", "235") AEX("224") AEX("226") AO2("v_max_f32", "238", "239", "234", "235") AEX("216") AEX("218")
                 S32("238", "239") AOP("v_add_f32", "232", "224") AO2("v_max_f32", "234", "235", "238", "239")
                 AOP("v_add_f32", "233", "216") AMU("224", "187")
                 S16("234", "235") AMU("226", "191")
                 S16("232", "233") ASB("208", "234") AO2("v_add_f32", "236", "237", "232", "233") ASB("200", "235")
                 S32("236", "237") AEX("208") AEX("210") "v_add_f32 v232, v236, v237\n\t" AEX("200") "v_rcp_f32 v232, v232\n\t" AEX("202")
                 "v_mov_b32 v233, v232\n\t" AOP("v_add_f32", "234", "208") AOP("v_add_f32", "235", "200")
                 S16("232", "233") AMU("224", "232")
                 S16("234", "235") AMU("226", "232") AMU("216", "186") AO2("v_add_f32", "238", "239", "234", "235") AMU("218", "190") APK("224")
                 S32("238", "239") AMU("216", "233") "v_add_f32 v234, v238, v239\n\t" AMU("218", "233") "v_rcp_f32 v234, v234\n\t"
                 APM("224", "0") "v_mov_b32 v235, v234\n\t" APM("224", "1") APM("224", "2")
                 S16("234", "235") APM("224", "3") APK("216") AMU("208", "185")
                 APV("132", "170", "224") APM("216", "0") APM("216", "1") APM("216", "2")
                 APV("136", "180", "224") APM("216", "3") AMU("210", "189") AMU("208", "234")
                 APV("140", "168", "216") AMU("210", "234") AMU("200", "184") APK("208")
                 APV("144", "178", "216") APM("208", "0") APM("208", "1") APM("208", "2") APM("208", "3") AMU("202", "188") AMU("200", "235")
                 APV("148", "166", "208") AMU("202", "235") AOC("224", "132")
                 APV("152", "176", "208") APK("200") AOC("226", "136") AST("224", "3072")
                 APM("200", "0") APM("200", "1") APM("200", "2") APM("200", "3") AOC("216", "140")
                 APV("156", "164", "200") AOC("218", "144") AST("216", "2048")
                 APV("160", "174", "200") AOC("208", "148") AOC("210", "152") AST("208", "1024")
                 AOC("200", "156") "s_nop 0\n\t" AOC("202", "160") AST("200", "0") "s_nop 1"
                 : "+{v[224:231]}"(s3), "+{v[216:223]}"(s2)
                 : "{v[164:171]}"(w0), "{v[174:181]}"(w1), "{v[172:173]}"(v3[0]), "{v[182:183]}"(v3[1]),
                   [k1] "v"(k[1]), [k0] "v"(k[0]), [kx] "v"(kx), [q1] "v"(q[1]), [q0] "v"(q[0]),
                   [cx] "v"(am[0]), [cy] "v"(am[1]), [c0] "v"(am[2]), "{v[240:243]}"(am[3]),
                   "{v[184:185]}"(rk[0]), "{v[186:187]}"(rk[1]), "{v[188:189]}"(rk[2]), "{v[190:191]}"(rk[3]),
                   "{v[192:193]}"(sc[0]), "{v[194:195]}"(sc[1]), "{v[196:197]}"(sc[2]), "{v[198:199]}"(sc[3]), [oo] "v"(oo), [rc] "s"(rc)
                 : "memory", "v132", "v133", "v134", "v135", "v136", "v137", "v138", "v139", "v140", "v141", "v142", "v143", "v144", "v145",
                   "v146", "v147", "v148", "v149", "v150", "v151", "v152", "v153", "v154", "v155", "v156", "v157", "v158", "v159", "v160",
                   "v161", "v162", "v163", "v200", "v201", "v202", "v203", "v204", "v205", "v206", "v207", "v208", "v209", "v210",
                   "v211", "v212", "v213", "v214", "v215", "v232", "v233", "v234", "v235", "v236", "v237", "v238", "v239");
}

typedef unsigned long long u64;
typedef __attribute__((address_space(1))) u64 gu64;
template <class F> __device__ __forceinline__ void publish(unsigned* f, u64 tok, int wid, unsigned sink, F&& then) {
    asm volatile("s_cmp_eq_u32 %0, 0\n\ts_cselect_b64 exec, 1, 0\n\ts_waitcnt vmcnt(0)\n\ts_barrier\n\t"
                 "global_store_dwordx2 %1, %2, %3 sc1\n\ts_mov_b64 exec, -1" :: "s"(wid), "v"(0u), "v"(tok), "s"(f) : "memory", "scc");
    then();
    if (wid == 2) {
        code_touch(sink, opaque(threadIdx.x) & 63u);
        wait_vm<0>();
    }
}
template <int N, int W = 4> __device__ __forceinline__ void pubc(unsigned* f, u64 tok, unsigned cnt) {
    unsigned c = 1u;
    asm volatile("s_mov_b64 exec, 1\n\ts_waitcnt vmcnt(%5)\n\tds_add_rtn_u32 %0, %4, %0\n\ts_waitcnt lgkmcnt(0)\n\t"
                 "v_cmp_eq_u32 vcc, %6, %0\n\ts_cbranch_vccz 1f\n\tglobal_store_dwordx2 %1, %2, %3 sc1\n\ts_waitcnt vmcnt(1)\n\t"
                 "s_branch 2f\n1:\n\ts_waitcnt vmcnt(0)\n2:\n\ts_mov_b64 exec, -1"
                 : "+v"(c) : "v"(0u), "v"(tok), "s"(f), "v"(cnt), "n"(N), "n"(W - 1) : "memory", "vcc");
}
__device__ __forceinline__ void poll(u64& have, u64 need, u64 at, const unsigned* f, u64 tok) {
    if (__builtin_expect((have & need) != need, 0)) {
        u64 v, t;
        unsigned n;
        asm volatile("s_andn2_b64 exec, %5, %0\n\ts_mov_b32 %3, 0x400000\n"
                     "1:\n\tglobal_load_dwordx2 %1, %6, off sc1\n\ts_sub_u32 %3, %3, 1\n\ts_cbranch_scc1 2f\n\ts_waitcnt vmcnt(0)\n\t"
                     "v_cmp_eq_u64 vcc, %7, %1\n\ts_or_b64 %0, %0, vcc\n\ts_andn2_b64 %2, %4, %0\n\ts_cbranch_scc1 1b\n"
                     "2:\n\ts_mov_b64 exec, -1"
                     : "+s"(have), "=&v"(v), "=&s"(t), "=&s"(n) : "s"(need), "s"(at), "v"(f), "s"(tok) : "memory", "vcc", "scc");
    }
}
__device__ __forceinline__ void look(u64& v, u64 at, const unsigned* f) {
    asm volatile("s_mov_b64 exec, %1\n\tglobal_load_dwordx2 %0, %2, off sc1\n\ts_mov_b64 exec, -1" : "=v"(v) : "s"(at), "v"(f) : "memory");
}
__device__ __forceinline__ u64 seen(u64& v, u64 at, u64 tok) {
    asm volatile("" : "+v"(v));
    return __ballot(v == tok) & at;
}
__device__ __forceinline__ void spin(u64& v, const void* base, unsigned off, u64 tok) {
    asm volatile("" : "+v"(v));
    if (__builtin_expect(__ballot(v != tok) != 0, 0)) {
        unsigned n;
        asm volatile("s_mov_b32 %1, 0x400000\n"
                     "1:\n\tglobal_load_dwordx2 %0, %2, %3 sc1\n\ts_sub_u32 %1, %1, 1\n\ts_waitcnt vmcnt(0)\n\ts_cbranch_scc1 2f\n\t"
                     "v_cmp_ne_u64 vcc, %4, %0\n\ts_cbranch_vccnz 1b\n2:"
                     : "=&v"(v), "=&s"(n) : "v"(off), "s"(base), "s"(tok) : "memory", "vcc", "scc");
    }
}
template <class M> __device__ __forceinline__ void await(M& have, M need, const unsigned* f, u64 tok) {
    if (__builtin_expect((have & need) != need, 0)) {
#pragma unroll 1
        for (unsigned spins = 0; spins < (1u << 22); ++spins) {
            have = (M)__ballot(__hip_atomic_load((gu64*)f, __ATOMIC_RELAXED, __HIP_MEMORY_SCOPE_AGENT) == tok);
            if ((have & need) == need) break;
            __builtin_amdgcn_s_sleep(1);
        }
        asm volatile("" ::: "memory");
    }
}
__device__ __forceinline__ unsigned gu_bsrc(int p, unsigned lane, int tn) {
    const unsigned rem = lane + (p < 14 ? p : p - 14) * 64, rowp = __umul24(rem, 4682u) >> 16;
    return __umul24(rowp ^ (rowp >> 1 & 4u), F_ * 2) + (rem * 16u + tn * 224u) - __umul24(rowp, 224u);
}
template <bool PERM = false>
__device__ __forceinline__ unsigned b_off(int h, int lane, int ld, int col) {
    const int row = h * 8 + (lane >> 3), cp = lane & 7;
    const int jj = (cp >> 1) ^ (((row >> 1) & 1) | ((row >> 2) & 2));
    return (unsigned)((PERM ? (lane >> 5) * 16 + h * 4 + (row & 3) : row) * ld + col + jj * 16 + (cp & 1) * 8) * 2u;
}

constexpr int DNO = F_ / 64 - NSOLO - DNE;
constexpr int dn_first(int n) { return (2 - n) & 3; }
static_assert(NSOLO == 4 && DNE < 28 && DNO < 28 && ((dn_first(DNE) ^ dn_first(DNO)) & 1) == 0, "k-tile program");
constexpr int DPX = DPAR == 0 ? 4 : (DPAR == 1 ? 1 : (DPAR == 2 ? 5 : (DPAR == 3 ? 7 : 3)));
__device__ __forceinline__ int dn_grp(int x) {
    return DPAR == 0 ? x & 3 : (DPAR == 1 ? x >> 1 : (DPAR == 4 ? (x >> 2) * 2 + ((x ^ x >> 1) & 1) : (x & 3) ^ (x >> 2) * (DPX & 3)));
}
constexpr int DNA = 65536, DNR = 98304;

constexpr int GBR = 3 * 32768, GBS = 224 * 128, GCS = GBR + 2 * GBS, GST = GCS + 224 * 4, GMX = DNR + 16384, GCN = 161024;
constexpr unsigned CSO = 4u << 20;
struct GuC { unsigned aoff[4], boff[4], tof, ab[2][2], bb[2][2], sto, cca, cc6, sta, gof, gofm, hsw, hsr, fo, cw, f1, f2; };
constexpr int gu_k(int i) { return i == 3 ? 4 : (i == 4 ? 3 : i); }
__device__ __forceinline__ void gu_setup(GuC& c, unsigned lds0, int wid, int id, bool first, unsigned fl) {
    const int tid = (int)opaque(threadIdx.x), lane = tid & 63;
    const int wm = wid >> 1, wn = wid & 1, tm = id & 7, tn = id >> 3, m0 = tm * 256;
    const int l15 = lane & 15, g = lane >> 4, q = l15 >> 2, p4 = l15 & 3;
#pragma unroll
    for (int i = 0; i < 4; ++i)
        c.aoff[i] = opaque((unsigned)((m0 + i * 64 + (tid >> 3)) * D_ + ((tid & 7) ^ ((tid >> 4) & 7)) * 8) * 2u);
    const int dk = DPAR ? (id >> 5) & 1 : id >> 7;
    if (wid < 4) {
        const unsigned j = (id & 31) * 256 + tid;
        unsigned rw = j >> 2;
        rw += rw < 1792 ? 0 : (dk ? DNE * 64 : DNO * 64);
        rw = (rw < F_ ? rw : 0) + (id >> 6) * 896;
        rw = rw < F_ ? rw : rw - F_;
        c.tof = opaque(rw * 2048 + 256 + (j & 1) * 128 + (j & 2) * 512);
    } else {
        c.tof = opaque((id * 224 + (tid < 480 ? tid - 256 : 0u)) * (first ? 128u : 0u));
    }
#pragma unroll
    for (int kk = 0; kk < 2; ++kk) {
        const unsigned sw = ((kk * 4 + g) ^ (l15 >> 1)) * 16;
        c.ab[kk][0] = opaque(lds0 + (wm * 64 + l15) * 128 + sw);
        c.ab[kk][1] = opaque(c.ab[kk][0] + 65536);
        const unsigned r = lds0 + GBR + ((g * 8 + kk * 4 + q) ^ (4 * (g & 1))) * 224;
        c.bb[kk][0] = opaque(r + wn * 128 + p4 * 8);
        c.bb[kk][1] = opaque(r + 96 + wn * 16 + (p4 & 1) * 8 + (p4 >> 1) * (GBS / 2));
    }
    c.sto = opaque(wid * 4096 + g * 1024 + l15 * 16);
    c.cca = opaque(lds0 + GCS + (wn * 64 + g * 4) * 4);
    c.cc6 = opaque(lds0 + GCS + (48 + wn * 8 + (g & 1) * 4 + (g >> 1) * 112) * 4);
    c.sta = opaque(lds0 + GST + wm * 1024 + (l15 & 1) * 256 + (l15 >> 1) * 16);
    c.gof = opaque(((m0 + wm * 64) / 4 + (g & 1) * 4 + q) * (8 * F_) + (tn * 7 + wn * 4) * 128 + p4 * 32 + (g >> 1) * 16);
    c.gofm = opaque(((m0 + wm * 64 + wn * 32) / 4 + (lane >> 3)) * (8 * F_) + (tn * 7 + 3) * 128 + (lane & 7) * 16);
    c.hsw = opaque(lds0 + GMX + (wm * 64 + (g >> 1) * 16 + l15) * 32 + wn * 16 + (g & 1) * 8);
    c.hsr = opaque(lds0 + GMX + wid * 1024 + lane * 16);
    const int pid = id ^ (DPX * 32), own = ((id / 64) * 4 + id % 4) * 16 + (id % 64) / 4, set = (first ? 7 : 5) * 256 + (own & ~15);
    const int w1 = (tm * 2 + wn) * 16 + wm + (wm == 3), j = wid * 4 + (lane & 3);
    c.f1 = opaque(fl + (w1 == own ? set : w1) * 128u);
    const int w2 = j < 24 ? (tm * 2 + (j & 1)) * 16 + 4 + j / 2 - (j < 2)
                          : (j & 1 ? ((pid / 64) * 4 + pid % 4) * 16 + (pid % 64) / 4 : (dn_grp(id >> 5) * 4 + (id & 3)) * 16 + ((id >> 2) & 7) * 2 + dk);
    c.f2 = opaque(fl + w2 * 128u);
    const int col = (wid < 7 ? wid : 6) * 32 + (lane & 7) * 4;
    c.fo = opaque((unsigned)((lane >> 3) * 2 * F_ + (col < 112 ? col : F_ + col - 112) + tn * 112) * 4u);
    c.cw = opaque(lds0 + GCS + (col + g) * 4);
}

constexpr int WOB = 65536, WONB = 12, QCN = 57088;
constexpr unsigned BOXO = 36u << 20;
__device__ __forceinline__ int wo_k(int i) { return (int)(0xb3fedca987654210ull >> (4 * i)) & 15; }
__device__ __forceinline__ unsigned wo_boff(int h, int lane) {
    return b_off<true>(h, lane, D_, (((wg_id()) % 64) / 4) * 64);
}
__device__ __forceinline__ void wo_stage_b(unsigned lds0, const u16* B, unsigned boff, int h, int step) {
#pragma unroll
    for (int i = 0; i < 2; ++i)
        glds16(uni((const char*)B + (wo_k(step) * 64 + i * 32) * D_ * 2), boff,
               lds0 + WOB + (step < WONB ? step : step - WONB) * 8192 + h * 1024 + i * 4096);
}
__device__ __forceinline__ void wo_phase(char* smem, const u16* A, const u16* __restrict__ B, u16* C, float* sp,
                                         unsigned* pflags, bool first, const u16* __restrict__ wg,
                                         const u16* __restrict__ wu, const float* cs, unsigned fl, GuC& gc, f32x4& cp,
                                         u64& lk, u64 tok) {
    constexpr int NK = 16, ASZ = 16384;
    const int tid = (int)opaque(threadIdx.x), lane = tid & 63;
    const int wid = __builtin_amdgcn_readfirstlane(tid >> 6);
    const int id = wg_id();
    const int tm = (id / 64) * 4 + id % 4, tn = (id % 64) / 4;
    const int m0 = tm * 128, n0 = tn * 64;
    const unsigned lds0 = __builtin_amdgcn_readfirstlane((unsigned)(size_t)(LDS_AS char*)smem);
    ResC rc;
    unsigned goff[7] = {0u, 0u, 0u, 0u, 0u, 0u, 0u};
    auto stage_g = [&](int i, int kt) INL {
        const int p = wid - 4 + 4 * i;
        glds16(uni((const char*)(p >= 14 ? wu : wg) + kt * 64 * F_ * 2), goff[i], lds0 + 98304 + kt * 28672 + p * 1024);
    };
    if (wid >= 4) {
        const int h = wid - 4;
        const unsigned aoff = (unsigned)(m0 * D_ * 2 + (h & 1) * 4096 + (h >> 1) * 1024 + lane * 16);
        const unsigned boff = wo_boff(h, lane);
#pragma unroll
        for (int i = 0; i < 7; ++i) goff[i] = opaque(gu_bsrc(h + 4 * i, opaque(lane), id >> 3));
        res_setup(rc, lds0 + ASZ, m0, n0, tid);
        gu_setup(gc, lds0, wid, id, first, fl);
        const int l = lane & 15, cb = (id >> 3) * 7 / 4 + l % 3;
        const int job = ((cb < 56 ? cb : 55) + (l >= 6 ? 56 : 0)) * 2 + (l / 3 & 1), oid = job + 8 * ((job + 32) / 56);
        const unsigned* const f = pflags + (lane < 16 ? tm * 16 + l : ((oid / 128) * 8 + oid % 8) * 16 + (oid % 128) / 8) * 32;
        const u64 all = first && h == 0 ? 0x0fffffffu : 0xffffu;
        u64 have = 0, at = WLK == 2 ? 3 : 0x17, lk2;
        auto stage = [&](int st, auto bc) INL {
            poll(have, 1u << wo_k(st), at, f, tok);
#pragma unroll
            for (int i = 0; i < 4; ++i)
                glds16_dev(uni((const char*)A + wo_k(st) * 8192 + (i >> 1) * 64 * D_ * 2 + (i & 1) * 2048), aoff,
                           lds0 + ((st + 1) & 3) * ASZ + h * 1024 + i * 4096);
            if constexpr (decltype(bc)::value) wo_stage_b(lds0, B, boff, h, st);
        };
        stage(0, CT<0>{});
        if constexpr (WL2 == 0) look(lk2, all, f);
        stage(1, CT<0>{});
        wait_vm<4>();
        BARRIER();
        if constexpr (WL2 == 0) { at = all; have |= seen(lk2, at, tok); }
        if constexpr (WL2 == 1) look(lk2, all, f);
        stage(2, CT<0>{});
        stage(3, CT<0>{});
        if constexpr (WL2 == 1) { wait_vm<8>(); at = all; have |= seen(lk2, at, tok); }
#pragma unroll 1
        for (int st = 4; st < WONB; ++st) {
            wait_vm<8>();
            BARRIER();
            stage(st, CT<0>{});
        }
#pragma unroll 1
        for (int st = WONB; st < NK; ++st) {
            wait_vm<8>();
            BARRIER();
            stage(st, CT<1>{});
        }
        poll(have, all, at, f, tok);
        wait_vm<12>();
        BARRIER();
        stage_g(0, 0); stage_g(1, 0); stage_g(2, 0);
        wait_vm<9>();
        BARRIER();
        stage_g(3, 0); stage_g(4, 0);
        wait_vm<5>();
        BARRIER();
        stage_g(5, 0); stage_g(6, 0);
        if (h == 3) code_touch(SINK, lane);
#pragma unroll
        for (int i = 0; i < 4; ++i) gc.boff[i] = goff[i < 3 ? 2 * i + 1 : 0];

    } else {
        const int wm = wid >> 1, wn = wid & 1;
        const int l15 = lane & 15, g = lane >> 4, q = l15 >> 2, p4 = l15 & 3;
        const unsigned ab = opaque(lds0 + wm * 8192 + lane * 16);
        unsigned bb[4];
#pragma unroll
        for (int x = 0; x < 4; ++x)
            bb[x] = opaque(lds0 + WOB + (g * 8 + q + ((x & 1) ^ wm) * 4) * 128 + p4 * 8 + (((wn * 2 + x / 2) ^ ((q >> 1) | ((g & 1) << 1))) << 5));
        f32x4 acc[4][2];
        {
            const unsigned ra = lds0 + (wm * 64 + l15) * 128 + (((wn * 4 + g) ^ (l15 >> 1)) * 16);
            const bf16x8 sel[2] = {pick(g == (l15 >> 3), l15 & 7), pick(g == 2 + (l15 >> 3), l15 & 7)};
            static_for<0, 4>([&](auto ic) INL {
                constexpr int i = decltype(ic)::value;
                const bf16x8 rr = lds_a<i * 2048>(ra);
#pragma unroll
                for (int j = 0; j < 2; ++j) {
                    acc[i][j] = mma(sel[j], rr, f32x4{0.f, 0.f, 0.f, 0.f});
                    asm volatile("" : "+v"(acc[i][j]));
                }
            });
        }
        res_setup(rc, lds0 + ASZ, m0, n0, tid);
        const unsigned cw = opaque(lds0 + ASZ + (wm * 64 + l15) * CSTR + (wn * 32 + g * 4) * 2);
        SCHED();
        pubc<0>(pflags + (((id / 128) * 8 + id % 8) * 16 + (id % 128) / 8) * 32, tok, lds0 + QCN);
        gu_setup(gc, lds0, wid, id, first, fl);
#pragma unroll
        for (int i = 0; i < 4; ++i) gc.boff[i] = opaque(gu_bsrc(wid + 8 * i, lane, id >> 3));
        auto ldop = [&](bf16x8 (&af)[8], s16x4 (&bf)[8], auto ktc, auto oc) INL {
            constexpr int KT = decltype(ktc)::value, O = decltype(oc)::value;
            if constexpr (O == 2 || (O >= 5 && O < 12)) {
                constexpr int X = O == 2 ? 0 : O - 4;
                af[(X & 1) * 4 + X / 2] = lds_a<((KT + 1) % 4) * ASZ + X * 1024>(ab);
            } else {
                constexpr int X = O < 2 ? O : (O < 5 ? O - 1 : O - 8), J = X / 4;
                bf[X] = lds_h<(KT % 4) * 8192 + ((X >> 1) & 1) * 4096>(bb[J * 2 + (X & 1)]);
            }
        };
        auto ktile = [&](bf16x8 (&af)[8], s16x4 (&bf)[8], bf16x8 (&an)[8], s16x4 (&bn)[8], auto nktc) INL {
            static_for<0, 16>([&](auto tc) INL {
                constexpr int T = decltype(tc)::value, j = T / 8, i = j ? 3 - (T / 2) % 4 : (T / 2) % 4, kk = T & 1;
                if constexpr (T == WOBAR && decltype(nktc)::value < NK) {
                    WAIT_LGKM0();
                    BARRIER();
                }
                mma_ip(cat(bf[j * 4 + kk * 2], bf[j * 4 + kk * 2 + 1]), af[kk * 4 + i], acc[i][j]);
                SCHED();
                if constexpr (decltype(nktc)::value < NK && T >= WOBAR) {
                    constexpr int U = T - WOBAR, lo_ = (U * 16 + WOLD - 1) / WOLD, hi_ = ((U + 1) * 16 + WOLD - 1) / WOLD;
                    static_for<lo_, (hi_ < 16 ? hi_ : 16)>([&](auto oc) INL { ldop(an, bn, nktc, oc); });
                }
                SCHED();
            });
        };
        bf16x8 a0[8], a1[8];
        s16x4 b0[8], b1[8];
        BARRIER();
        static_for<0, 16>([&](auto oc) INL { ldop(a0, b0, CT<0>{}, oc); });
#pragma unroll 1
        for (int it = 0; it < 3; ++it) {
            ktile(a0, b0, a1, b1, CT<1>{});
            ktile(a1, b1, a0, b0, CT<2>{});
            ktile(a0, b0, a1, b1, CT<3>{});
            const unsigned d = it == 2 ? -65536 : 32768;
            bb[0] += d; bb[1] += d; bb[2] += d; bb[3] += d;
            ktile(a1, b1, a0, b0, CT<4>{});
        }
        ktile(a0, b0, a1, b1, CT<13>{});
        ktile(a1, b1, a0, b0, CT<14>{});
        ktile(a0, b0, a1, b1, CT<15>{});
        ktile(a1, b1, a0, b0, CT<16>{});
#pragma unroll
        for (int t = 0; t < 8; ++t) {
            const int j = t / 4, i = j ? 7 - t : t;
            *(LDS_AS u32x2*)(size_t)(cw + i * 16 * CSTR + j * 32) = u32x2{pk2(acc[i][j][0], acc[i][j][1]), pk2(acc[i][j][2], acc[i][j][3])};
            SCHED();
        }
    }
    WAIT_LGKM0();
    BARRIER();
    res_tail(rc, C, sp);
    asm volatile("global_load_dwordx2 %0, %2, %4 sc1\n\tglobal_load_dwordx4 %1, %3, %4 sc1"
                 : "=&{v[194:195]}"(lk), "=&{v[196:199]}"(cp) : "v"(gc.f1), "v"(gc.fo), "s"(cs) : "memory");
}

constexpr int QAS = 144 * 128, QBS = 192 * 128, QBR = 57344;
constexpr int QSK = 3 * QAS, QCS = 159744, QGR = QAS, QXC = 57344;
#define GL_STEP(V) "global_load_lds_dwordx4 %0, %1\n\tv_add_u32 %0, " V ", %0\n\ts_addk_i32 m0, 0x800\n\ts_nop 0\n\t"
__device__ __forceinline__ void qkv_half_a(const void* sbase, unsigned voff, unsigned lds_dst) {
    asm volatile("s_mov_b32 m0, %2\n\ts_nop 3\n\t" GL_STEP("%3") GL_STEP("%3") GL_STEP("%3") GL_STEP("%3") GL_STEP("%3") GL_STEP("%3") GL_STEP("%3")
                 "global_load_lds_dwordx4 %0, %1" : "+v"(voff) : "s"(sbase), "s"(__builtin_amdgcn_readfirstlane(lds_dst)), "s"(16 * D_ * 2) : "memory", "m0", "scc");
}
__device__ __forceinline__ void qkv_half_b(const void* sbase, unsigned voff, unsigned lds_dst) {
    asm volatile("s_mov_b32 m0, %2\n\ts_nop 3\n\t" GL_STEP("%3") GL_STEP("%3") GL_STEP("%3") GL_STEP("%4") GL_STEP("%3") GL_STEP("%3") GL_STEP("%3") GL_STEP("%4")
                 GL_STEP("%3") GL_STEP("%3") GL_STEP("%3") "global_load_lds_dwordx4 %0, %1"
                 : "+v"(voff) : "s"(sbase), "s"(__builtin_amdgcn_readfirstlane(lds_dst)), "s"(16 * 3 * D_ * 2), "s"(D_ * 2 - 48 * 3 * D_ * 2) : "memory", "m0", "scc");
}
constexpr int qsl(int kt) { return (kt + 2) % 3; }
__device__ __forceinline__ u64 qkv_ks() { return 0xb3a2fedc98765410ull; }
__device__ __forceinline__ int qkv_k(u64 ks, int i) { return (int)(ks >> (4 * i)) & 15; }
__device__ __forceinline__ unsigned qkv_boff(int h, int lane) {
    return b_off(h, lane, 3 * D_, (((wg_id()) % 128) / 8) * 64);
}
__device__ __forceinline__ void qkv_stage_b(unsigned lds0, const u16* B, unsigned boff, int h, int kt, int sl) {
#pragma unroll
    for (int i = 0; i < 6; ++i)
        glds16(uni((const char*)B + ((kt * 64 + (i & 1) * 32) * 3 * D_ + (i >> 1) * D_) * 2), boff,
               lds0 + QBR + sl * QBS + h * 1024 + i * 4096);
}
__device__ __forceinline__ void qkv_phase(char* smem, const u16* A, const u16* __restrict__ B, u16* C, float* cs,
                                          const float* sp, unsigned* pflags, unsigned* oflags,
                                          const u16* __restrict__ wo, bool first, const u16* __restrict__ wg,
                                          const u16* __restrict__ wu, u64* epoch, u64& tok) {
    constexpr int NK = 16;
    const int tid = (int)opaque(threadIdx.x), lane = tid & 63;
    const int wid = __builtin_amdgcn_readfirstlane(tid >> 6);
    const int id = wg_id();
    const int tm = (id / 128) * 8 + id % 8, tn = (id % 128) / 8;
    const int m0 = tm * 128;
    const unsigned lds0 = __builtin_amdgcn_readfirstlane((unsigned)(size_t)(LDS_AS char*)smem);
    float* const csl = (float*)(smem + QCS);
    float* const cso = cs + 16 * F_ + id * 192;
    auto token = [&]() INL {
        asm volatile("s_load_dwordx2 %0, %1, 0x0\n\ts_waitcnt lgkmcnt(0)" : "=s"(tok) : "s"(epoch) : "memory");
        tok += 0x9e3779b97f4a7c15ull;
    };
    if (wid >= 4) {
        const int h = wid - 4, hl = tid - 256;
        const int row = h * 8 + (lane >> 3), hr = h * 4 + ((lane >> 3) & 3);
        const unsigned la = (unsigned)((m0 + row) * D_ + ((lane & 7) ^ ((row >> 1) & 7)) * 8) * 2u;
        const unsigned lh = (unsigned)((tm ? m0 - 16 + hr : 0) * D_ + ((lane & 7) ^ ((hr >> 1) & 7)) * 8) * 2u;
        const unsigned lb = qkv_boff(h, lane);
        const unsigned wb = wo_boff(h, lane);
        constexpr auto wn = [](int i) { return i == 0 ? 2 : (i == 1 ? WN1 : (i == 2 ? WN2 : (i == 3 ? WN3 : (i == 4 ? WN4 : 10 - WN1 - WN2 - WN3 - WN4)))); };
        static_assert(WN1 + WN2 <= 6 && WN1 + WN2 + WN3 <= 9, "free slots");
        auto wo_pre = [&](int i) INL {
            int e0 = 0;
#pragma unroll
            for (int x = 0; x < i; ++x) e0 += wn(x);
#pragma unroll
            for (int e = e0; e < e0 + wn(i); ++e) wo_stage_b(lds0, wo, wb, h, (int)(0xb765a9843210ull >> (4 * e)) & 15);
        };
        const int wtile = ((id / 64) * 4 + id % 4) * 16 + (id % 64) / 4;
        auto wo_res = [&]() INL {
#pragma unroll
            for (int i = 0; i < 4; ++i)
                glds16_dev(uni((const char*)A + (((wtile >> 4) - tm) * 128 + i * 32) * D_ * 2 + (wtile & 15) * 128), la, lds0 + h * 1024 + i * 4096);
        };
        auto wo_slow = [&]() INL {
            const unsigned bx = bid_();
            if (h == 1) touch1(SINK, wo, ((bx >> 3) * 32 + (lane & 31)) * 2048 + ((bx & 1) * 8 + 3) * 128);
        };
        auto marks = [&]() INL {
            const unsigned em = opaque(~0u), bo = BOXO + bid_() * 16384u + (unsigned)hl * 16u;
            const __amdgpu_buffer_rsrc_t r = __builtin_amdgcn_make_buffer_rsrc(C, 0, 0x7fffffff, 0x00020000);
#pragma unroll
            for (int i = 0; i < 4; ++i) __builtin_amdgcn_raw_buffer_store_b128(u32x4{em, em, em, em}, r, bo, i * 4096, 16);
        };
        auto put_stat = [&](float sum, float sq, int row, bool on) INL {
            const float mean = mulf(sum, 1.f / D_);
            const float var = fmaxf(subf(mulf(sq, 1.f / D_), mulf(mean, mean)), 0.f);
            const float rs = __builtin_amdgcn_rcpf(rbf(__builtin_amdgcn_sqrtf(rbf(addf(rbf(var), 1e-5f))))), nm = -rbf(mean);
            if (on) {
                *(float*)(smem + QSK + row * 4) = nm;
                *(float*)(smem + QSK + 576 + row * 4) = rs * (0.17677669529663687f * 1.44269504088896341f);
                *(float*)(smem + QSK + 1152 + row * 4) = rs;
            }
        };
        if (first) {
            const unsigned long long hmask = h == 0 ? 0xffffff00ull : 0xffffffffull;
            auto piece = [&](int kt, int p) INL {
                const unsigned da = lds0 + qsl(kt) * QAS;
                if (p < 4) glds16(uni((const char*)A + kt * 128 + p * 32 * D_ * 2), la, da + h * 1024 + p * 4096);
                else if (p == 4) glds16_mask(uni((const char*)A + kt * 128), lh, da + 16384 + h * 512, hmask);
                else glds16(uni((const char*)B + ((kt * 64 + ((p - 5) & 1) * 32) * 3 * D_ + ((p - 5) >> 1) * D_) * 2), lb,
                            lds0 + QBR + qsl(kt) * QBS + h * 1024 + (p - 5) * 4096);
            };
            __builtin_amdgcn_s_setprio(HPRIO);
            constexpr unsigned tw = SINK;
            const unsigned one2 = opaque(0x3f803f80u);
            if (hl < 24) *(u32x4*)(smem + (hl >> 3) * QAS + 128 * 128 + (hl & 7) * 16) = u32x4{one2, one2, one2, one2};
            const bool idle = (tn & 7) == 3;
            const int job = idle ? 0 : id - 8 * ((id + 32) >> 6);
            const int cb = job >> 1, kh = job & 1;
            const char* const wp = uni((const char*)((cb < 56 ? wg : wu) + (long long)kh * 512 * F_ + (cb % 56) * 64));
            const unsigned wpo = (unsigned)((idle ? 0 : hl >> 3) * F_ + (hl & 7) * 8) * 2u;
            const int wst = idle ? 0 : 64 * F_;
            f32x4 wv[NK];
            float sm[8];
#pragma unroll
            for (int e = 0; e < 8; ++e) sm[e] = 0.f;
            auto wadd1 = [&](int r, int e) INL {
                float t0, t1;
                asm volatile("v_lshlrev_b32 %2, 16, %4\n\tv_and_b32 %3, 0xffff0000, %4\n\tv_add_f32 %0, %0, %2\n\tv_add_f32 %1, %1, %3"
                             : "+v"(sm[2 * e]), "+v"(sm[2 * e + 1]), "=&v"(t0), "=&v"(t1) : "v"(wv[r][e]));
            };
            auto wadd = [&](int r) INL {
#pragma unroll
                for (int e = 0; e < 4; ++e) wadd1(r, e);
            };
            const unsigned bx = bid_();
            const int kq = bx >> 4;
            const unsigned xto = (unsigned)(((bx & 15) * 128 + (hl & 127)) * 2048 + kq * 128);
            auto sweep = [&]() INL {
                touch1(tw, B, (unsigned)(bx * 192 + (hl < 192 ? hl : 0)) * 128u);
                touch1(tw, A, xto);
            };
            token();
            if (h >= 2) {
                if (kq < KIN && kq >= KSKIP) {
                    for (int i = (kq - KSKIP + 1) * SLP; i > 0; --i) __builtin_amdgcn_s_sleep(1);
                    touch1(tw, B, (unsigned)(bx * 192 + (hl & 127)) * 128u);
                    touch1(tw, A, xto);
                    if (h == 2) touch1(tw, B, (unsigned)(bx * 192 + hl) * 128u);
                }
                qkv_half_a(uni((const char*)A + 128 - 16 * D_ * 2), la, lds0 + qsl(1) * QAS + (h - 2) * 1024);
                qkv_half_b(uni((const char*)B + 48 * 3 * D_ * 2), lb, lds0 + QBR + qsl(1) * QBS + (h - 2) * 1024);
            } else {
                wait_vm<1>();
            }
            constexpr int WR = NK / WNB;
            constexpr auto wat = [](int r) { return WS0 + (r / WR) * WSD + WNG - 1 + 3 + (r % WR) / WPS; };
            static_assert(WSD >= 2 && WS0 + (WNB - 1) * WSD + WNG - 1 <= NK - 3, "bursts");
            unsigned wbm = 0;
#pragma unroll
            for (int b = 0; b < WNB; ++b) wbm |= 1u << (WS0 + b * WSD);
            const int wg0 = (bx >> 3) % WNG;
            wbm <<= wg0;
            const unsigned wwm = wbm * 6u;
            unsigned wvo = wpo;
#pragma unroll
            for (int r = 0; r < NK; ++r) asm volatile("" : "=v"(wv[r]));
            static_for<0, NK>([&](auto kc) INL {
                constexpr int KT = decltype(kc)::value;
                constexpr auto ex = [](int k) { return (k == PV0 ? 1 : 0) + (k == NK - 2 ? 8 : 0); };
                constexpr int CW = (KT + 1 < NK ? 11 : 0) + ex(KT - 2) + ex(KT - 1);
                if constexpr (KT > WS0 && KT <= WS0 + (WNB - 1) * WSD + WNG + 1)
                    wait_vm_if<CW + WR, CW, KT>(wwm);
                else if constexpr (KT > 0) wait_vm<CW>();
                BARRIER();
                static_for<0, 11>([&](auto pc) INL {
                    constexpr int P = decltype(pc)::value;
                    if constexpr (KT + 2 < NK) piece(KT + 2, P);
                    if constexpr (P < 8) {
#pragma unroll
                        for (int r = 0; r < NK; ++r) if (wat(r) == KT && (r & 1) == P / 4) wadd1(r, P % 4);
                    }
                    SCHED();
                });
                if constexpr (KT + KIN < NK) { if (kq == KT + KIN) sweep(); }
                static_for<0, NK / 4>([&](auto qc) INL {
                    constexpr int Q = decltype(qc)::value, S = WS0 + (Q * 4 / WR) * WSD;
                    if constexpr (KT >= S && KT < S + WNG)
                        gload4_if<KT - S>(wv[Q * 4], wv[Q * 4 + 1], wv[Q * 4 + 2], wv[Q * 4 + 3], wvo, wp, wst, wg0);
                });
                if constexpr (KT == PV0) touch1(tw, wo, (unsigned)(bx * 64 + (hl & 63)) * 128u);
                if constexpr (KT == WT0) wo_slow();
                if constexpr (KT == NK - 2) { wo_res(); wo_pre(0); }
                if constexpr (KT == 7) { if (h == 0) att_put(smem, lane, m0); }
                if constexpr (KT == 4) {
                    if (h == 0) touch1(tw, oflags - 7 * 8192, ((lane < 12 ? lane : 0) * 256 + bx) * 128u);
                }
                WAIT_LGKM0();
            });
            auto colsum = [&]() INL {
#pragma unroll
            for (int r = 0; r < NK; ++r) if (wat(r) >= NK) wadd(r);
            asm volatile("s_nop 1\n\tv_add_f32_dpp %0, %0, %0 row_ror:8 row_mask:0xf bank_mask:0xf\n\tv_add_f32_dpp %1, %1, %1 row_ror:8 row_mask:0xf bank_mask:0xf\n\t"
                         "v_add_f32_dpp %2, %2, %2 row_ror:8 row_mask:0xf bank_mask:0xf\n\tv_add_f32_dpp %3, %3, %3 row_ror:8 row_mask:0xf bank_mask:0xf\n\t"
                         "v_add_f32_dpp %4, %4, %4 row_ror:8 row_mask:0xf bank_mask:0xf\n\tv_add_f32_dpp %5, %5, %5 row_ror:8 row_mask:0xf bank_mask:0xf\n\t"
                         "v_add_f32_dpp %6, %6, %6 row_ror:8 row_mask:0xf bank_mask:0xf\n\tv_add_f32_dpp %7, %7, %7 row_ror:8 row_mask:0xf bank_mask:0xf\n\t"
                         "s_nop 1\n\tv_permlane16_swap_b32 %0, %4\n\tv_permlane16_swap_b32 %1, %5\n\tv_permlane16_swap_b32 %2, %6\n\t"
                         "v_permlane16_swap_b32 %3, %7\n\tv_add_f32 %0, %0, %4\n\tv_add_f32 %1, %1, %5\n\tv_add_f32 %2, %2, %6\n\t"
                         "v_add_f32 %3, %3, %7\n\ts_nop 1\n\tv_permlane32_swap_b32 %0, %2\n\tv_permlane32_swap_b32 %1, %3\n\t"
                         "v_add_f32 %0, %0, %2\n\tv_add_f32 %1, %1, %3"
                         : "+v"(sm[0]), "+v"(sm[1]), "+v"(sm[2]), "+v"(sm[3]), "+v"(sm[4]), "+v"(sm[5]), "+v"(sm[6]), "+v"(sm[7]));
            if ((lane & 8) == 0 && !idle) {
                const __amdgpu_buffer_rsrc_t r = __builtin_amdgcn_make_buffer_rsrc(cs, 0, 0x7fffffff, 0x00020000);
                __builtin_amdgcn_raw_buffer_store_b64(u32x2{__float_as_uint(sm[0]), __float_as_uint(sm[1])}, r,
                    (unsigned)((kh * 4 + h) * 2 * F_ + cb * 64 + (lane & 7) * 8 + ((lane >> 4) & 1) * 4 + (lane >> 5) * 2) * 4u, 0, 16);
            }
            };
            if constexpr (CSP == 1) { wait_vm<8>(); colsum(); }
            wo_pre(1);
            BARRIER();
            {
                const int l = lane & 15, gi = h * 3 + (lane < 48 ? lane >> 4 : 0), mr = gi * 15 + l - 1;
                const float* const raw = (const float*)(smem + QGR + gi * 1024);
                put_stat(raw[l * 4], raw[((l >> 2) * 16 + l) * 4 + (l & 3)], l == 0 ? 0 : (gi < 9 ? 16 + (mr < 128 ? mr : 127) : l), gi < 10);
            }
            __builtin_amdgcn_s_setprio(0);
            if constexpr (GW2 == 0) wo_pre(2);
            WAIT_LGKM0();
            BARRIER();
            if constexpr (GW2 == 1) wo_pre(2);
            if (hl < 48) store16_wt(cso, (unsigned)hl * 16u, *(const u32x4*)(csl + hl * 4));
            wo_pre(3);
            if constexpr (CSP == 2) { wait_vm<2 * (WN1 + WN2 + WN3)>(); colsum(); }
            marks();
            if (h == 3) code_touch(51200, lane);
            wait_vm<20>();
            BARRIER();
            if constexpr (CSP == 3) { wait_vm<2 * (WN1 + WN2 + WN3) + (MKP == 0 ? 4 : 0)>(); colsum(); }
        } else {
            const unsigned* const f = pflags + (lane < 32 ? (tm && (lane & 16) ? tm - 1 : tm) * 16 + (lane & 15) : wtile) * 32;
            const u64 ks = qkv_ks();
            u64 have, at = 0x10001u << qkv_k(ks, 0) | 0x10001u << qkv_k(ks, 1), lk;
            look(lk, at, f);
            auto stage = [&](int st, int sl, auto bc) INL {
                const int kt = qkv_k(ks, st);
                poll(have, 0x10001ull << kt, at, f, tok);
                const unsigned da = lds0 + sl * QAS;
#pragma unroll
                for (int i = 0; i < 4; ++i) glds16_dev(uni((const char*)A + kt * 128 + i * 32 * D_ * 2), la, da + h * 1024 + i * 4096);
                glds16_part<32>(uni((const char*)A + kt * 128), lh, da + 16384 + h * 512);
                if constexpr (decltype(bc)::value) qkv_stage_b(lds0, B, lb, h, kt, sl);
            };
            {
                const unsigned bx = bid_();
                asm volatile("" :: "v"(la), "v"(lh), "v"(lb), "s"(A));
                pubc<1, 8>(pflags + ((dn_grp(bx & 7) * 4 + (bx >> 3 & 3)) * 16 + (bx >> 5) * 2 + (DPAR ? bx & 1 : bx >> 2 & 1)) * 32,
                                            tok, lds0 + GCN + 8);
            }
            have = seen(lk, at, tok);
            constexpr u64 all = 0x1ffffffffull;
            stage(0, qsl(0), CT<0>{});
            look(lk, all, f);
            stage(1, qsl(1), CT<0>{});
            wait_vm<5>();
            BARRIER();
            at = all;
            have |= seen(lk, at, tok);
            stage(2, qsl(2), CT<1>{});
            if (h == 0) att_put(smem, lane, m0);
            wait_vm<11>();
            BARRIER();
            stage(3, qsl(3), CT<1>{});
            if (h == 0) glds16_dev(uni((const char*)cso), lane * 16u, lds0 + QCS);
            int sl = qsl(4);
#pragma unroll 1
            for (int st = 4; st < NK; ++st) {
                wait_vm<11>();
                BARRIER();
                stage(st, sl, CT<1>{});
                if (__builtin_expect(st == WT1, 0)) wo_slow();
                sl = sl == 2 ? 0 : sl + 1;
            }
            f32x4 spv[8];
            poll(have, 1ull << 32, at, f, tok);
            {
                const int srow = m0 - 16 + (hl < 144 ? hl & ~1 : 0);
                const int sr = srow < 0 ? 0 : srow;
                const unsigned so = (unsigned)((sr >> 5) * 512 + (hl & 1) * 256 + (sr & 31)) * 8u;
                gload16_dev(spv[0], sp, so);
                static_for<1, 8>([&](auto ic) INL { gload16_devo<decltype(ic)::value * 256>(spv[decltype(ic)::value], sp, so); });
            }
            wait_vm<19>();
            BARRIER();
            wo_res();
            wo_pre(0);
            wait_vm<16>();
            BARRIER();
            if constexpr (QSP) __builtin_amdgcn_s_setprio(QSP);
            wait_vm<8>();
            float sm[4];
#pragma unroll
            for (int i = 0; i < 8; ++i) {
                asm volatile("" : "+v"(spv[i]));
#pragma unroll
                for (int e = 0; e < 4; ++e) sm[e] = i ? addf(sm[e], spv[i][e]) : spv[i][e];
            }
#pragma unroll
            for (int e = 0; e < 4; ++e)
                sm[e] = addf(sm[e], __uint_as_float(__builtin_amdgcn_update_dpp(0u, __float_as_uint(sm[e]), 0xB1, 0xf, 0xf, false)));
            put_stat(hl & 1 ? sm[2] : sm[0], hl & 1 ? sm[3] : sm[1], hl, hl < 144);
            if constexpr (QSP) __builtin_amdgcn_s_setprio(0);
            if constexpr (QSO == 0) wo_pre(1);
            WAIT_LGKM0();
            BARRIER();
            if constexpr (QSO == 1) wo_pre(1); else wo_pre(2);
            BARRIER();
            if constexpr (QSO == 1) wo_pre(2);
            wo_pre(3);
            marks();
            if (h == 3) code_touch(51200, lane);
            wait_vm<20>();
            BARRIER();
        }
        static_assert(WN1 + WN2 + WN3 == 8 && WN4 == 2 && CSP == 1, "counts");
        wo_pre(4);
    } else {
        const int wm = wid >> 1, wn = wid & 1;
        const int l15 = lane & 15, g = lane >> 4, q = l15 >> 2, p4 = l15 & 3;
        unsigned ab[2], hb[2], bj[6];
#pragma unroll
        for (int kk = 0; kk < 2; ++kk) {
            const unsigned sw = ((kk * 4 + g) ^ (l15 >> 1)) * 16;
            ab[kk] = opaque(lds0 + (wm * 64 + l15) * 128 + sw);
            hb[kk] = opaque(lds0 + (128 + l15) * 128 + sw);
        }
#pragma unroll
        for (int j = 0; j < 6; ++j)
            bj[j] = opaque(lds0 + QBR + (j >> 1) * 8192 + (g * 8 + q) * 128 + p4 * 8 + (((wn * 2 + ((j & 1) ^ wm)) ^ ((q >> 1) | ((g & 1) << 1))) << 5));
        f32x4 acc[5][6];
        constexpr auto qcol = [](int c) { return c < 3 ? 2 * c + 1 : 2 * c - 6; };
        auto ldop = [&](bf16x8 (&af)[10], s16x4 (&bf)[24], auto ktc, auto oc) INL {
            constexpr int KT = decltype(ktc)::value, O = decltype(oc)::value;
            if constexpr (O == 2 || (O >= 5 && O < 12)) {
                constexpr int X = O == 2 ? 0 : O - 4;
                af[X] = lds_a<qsl(KT) * QAS + (X / 2) * 2048>(ab[X & 1]);
            } else if constexpr (O == 24 || O == 25) {
                af[O - 16] = lds_a<qsl(KT) * QAS>(hb[O & 1]);
            } else {
                constexpr int X = O < 2 ? O : (O < 5 ? O - 1 : (O < 24 ? O - 8 : O - 10));
                bf[X] = lds_h<qsl(KT) * QBS + ((X >> 1) & 1) * 4096 + (X & 1) * 512>(bj[qcol(X / 4)]);
            }
        };
        unsigned ha[3][2];
        f32x4 rg[3];
        bf16x8 gf[2];
        auto hrow = [&](int i, int l) INL {
            const int gi = wid * 3 + i, mr = gi * 15 + l - 1;
            return l == 0 ? 128 : (gi < 9 ? (mr < 128 ? mr : 127) : 128 + l);
        };
        unsigned ea[10], ma[2], xw[2], xa, oo, l16, p4_, g16, cwa, cwb;
        f32x4 vnm[5], ccv[4];
        f32x2 rnm[3], rsc[2], rk[4];
        float ccw[2];
        u32x4 am[4];
        constexpr int NEA = 15;
        auto eadr = [&](auto tc) INL {
            constexpr int T = decltype(tc)::value;
            if constexpr (T == 0) asm volatile("v_and_b32 %0, 63, %1\n\tv_lshlrev_b32 %0, 4, %0" : "=v"(l16) : "v"(threadIdx.x));
            if constexpr (T == 1) { p4_ = opaque(l16 >> 2 & 60u); ma[0] = opaque(l16 + lds0 + ATX); }
            if constexpr (T == 2) ma[1] = opaque(ma[0] + (1 - wm) * 2048);
            if constexpr (T == 3) { asm volatile("v_and_b32 %0, 48, %1" : "=v"(g16) : "v"(threadIdx.x)); ea[0] = opaque(p4_ + lds0 + QSK); ea[1] = opaque(g16 + lds0 + QSK); }
            if constexpr (T == 4) { ea[2] = opaque(ea[0] + wm * 256); ea[3] = opaque(ea[1] + wm * 256); }
            if constexpr (T == 5) ea[4] = opaque(g16 + lds0 + QCS + (wn * 2 + wm) * 64);
            if constexpr (T == 6) { ea[6] = opaque(ea[4] ^ 64u); ea[5] = opaque(ea[4] - g16 + p4_); }
            if constexpr (T == 7) { ea[7] = opaque(ea[5] ^ 64u); ea[9] = opaque((p4_ & 56u) - g16); }
            if constexpr (T == 8) ea[8] = opaque(ea[2] - ((ea[9] & 63u) | -1216u));
            if constexpr (T == 9) ea[9] = opaque(ea[2] - (((ea[9] - 8u) & 63u) | -1216u));
            if constexpr (T == 10) xa = opaque(l16 + lds0 + QXC + wn * 4096);
            if constexpr (T == 11) xw[1] = opaque(xa + (1 - wm * 5) * 2048);
            if constexpr (T == 12) { xw[0] = opaque(xa + wm * 8); xa = opaque(opaque(xa) + wm * 2048); }
            if constexpr (T == 13) oo = opaque(l16 + (tm * 32 + tn) * 8192);
            if constexpr (T == 14) oo = opaque(oo + (wm * 32 + wn) * 4096);
            if constexpr (T == 15) cwa = opaque(p4_ == 0 ? ea[4] : xa);
            if constexpr (T == 16) cwb = opaque(g16 == 0 ? ea[5] : xa);
        };
        auto ld2 = [&](unsigned a) INL { return f32x2{*(const LDS_AS float*)(size_t)a, *(const LDS_AS float*)(size_t)(a + 64)}; };
        auto eld = [&](auto oc) INL {
            constexpr int O = decltype(oc)::value;
            if constexpr (O < 4) am[O] = *(const LDS_AS u32x4*)(size_t)(O == 2 ? ma[1] : ma[0] + O * 1024);
            else if constexpr (O == 5 || O == 7) ccw[O / 2 - 2] = *(const LDS_AS float*)(size_t)(ea[O] + 512);
            else if constexpr (O < 8) ccv[O / 2] = *(const LDS_AS f32x4*)(size_t)(ea[O] + 256);
            else if constexpr (O < 10) ccv[O - 8] = *(const LDS_AS f32x4*)(size_t)(ea[2 * O - 12]);
            else if constexpr (O == 10) rnm[2].x = *(const LDS_AS float*)(size_t)ea[0];
            else if constexpr (O == 11) vnm[4] = *(const LDS_AS f32x4*)(size_t)ea[1];
            else if constexpr (O == 12 || O == 15) rnm[O == 12] = ld2(ea[2] + 64 + (O == 12) * 128);
            else if constexpr (O < 18) { constexpr int I = O < 15 ? 16 - O : 17 - O; vnm[I] = *(const LDS_AS f32x4*)(size_t)(ea[3] + (I + 1) * 64); }
            else if constexpr (O < 20) rsc[O - 18] = ld2(ea[2] + 576 + 64 + (O - 18) * 128);
            else rk[O - 20] = ld2(ea[8 + (O - 20) / 2] + (O & 1) * 128);
        };
        auto unit = [&](bf16x8 (&af)[10], s16x4 (&bf)[24], bf16x8 (&an)[10], s16x4 (&bn)[24], auto ktc, auto hc, auto fic) INL {
            constexpr bool FI = decltype(fic)::value;
            constexpr int KT = decltype(ktc)::value, HF = decltype(hc)::value;
            static_for<0, 26>([&](auto tc) INL {
                constexpr int T = decltype(tc)::value, X = HF * 26 + T, kk = X & 1;
                constexpr int c = X < 32 ? X / 8 : (X < 42 ? 4 : 5), p = (X < 32 ? X % 8 : (X < 42 ? X - 32 : X - 42)) / 2;
                constexpr int i = c == 5 ? 4 - p : (c & 1 ? 3 - p : p);
                if constexpr (qcol(c) >= 4) mma_ip(af[i * 2 + kk], cat(bf[c * 4 + kk * 2], bf[c * 4 + kk * 2 + 1]), acc[i][qcol(c)]);
                else mma_ip(cat(bf[c * 4 + kk * 2], bf[c * 4 + kk * 2 + 1]), af[i * 2 + kk], acc[i][qcol(c)]);
                SCHED();
                if constexpr (HF == 1) {
                    if constexpr (KT + 1 == NK && FI) {
                        if constexpr (T == GBT - 4) *(LDS_AS f32x4*)(size_t)(cwa + 2048) = rg[2];
                        else if constexpr (T == GBT) { WAIT_LGKM0(); BARRIER(); }
                        else if constexpr (T < 17) eadr(CT<T + 2 - (T > GBT - 4) - (T > GBT)>{});
                        else if constexpr (T < 21) eld(CT<T - 17>{});
                        else if constexpr (T == 21) *(LDS_AS f32x4*)(size_t)cwa = acc[4][0];
                        else if constexpr (T == 22) *(LDS_AS f32x4*)(size_t)(cwa + 256) = acc[4][2];
                        else if constexpr (T == 24) *(LDS_AS float*)(size_t)(cwb + 512) = acc[4][4][0];
                    }
                    if constexpr (KT + 1 == NK && !FI) {
                        static_assert(QBT >= 10, "order");
                        constexpr int EN = 26 - QBT < 14 ? 26 - QBT : 14;
                        if constexpr (T < 10) eld(tc);
                        if constexpr (T == QBT - 1) BARRIER();
                        if constexpr (T >= QBT)
                            static_for<(T - QBT) * 14 / EN, ((T + 1 - QBT) * 14 / EN < 14 ? (T + 1 - QBT) * 14 / EN : 14)>([&](auto oc) INL { eld(CT<10 + decltype(oc)::value>{}); });
                    }
                    if constexpr (KT + 1 < NK && T < QG1) ldop(an, bn, CT<KT + 1>{}, tc);
                    if constexpr (KT + 1 < NK && T >= QL1 && T < QL1 + 16 - QG1) ldop(an, bn, CT<KT + 1>{}, CT<T - QL1 + QG1>{});
                } else if constexpr (FI) {
                    if constexpr (T % 8 < 2 && T < 24) gf[T & 1] = lds_a<qsl(KT) * QAS>(ha[T / 8][T & 1]);
                    else if constexpr (T < 20) ldop(af, bf, ktc, CT<14 + T>{});
                    else if constexpr (KT + 1 == NK) {
                        if constexpr (T == 20) eadr(CT<0>{});
                        if constexpr (T == 25) eadr(CT<1>{});
                        if constexpr (T == 21) cwa = opaque(l16 + lds0 + QGR + wid * 3072);
                        if constexpr (T == 22 || T == 24) *(LDS_AS f32x4*)(size_t)(cwa + (T - 22) * 512) = rg[T / 2 - 11];
                    }
                } else {
                    if constexpr (T < 18) ldop(af, bf, ktc, CT<16 + T>{});
                }
                SCHED();
                if constexpr (FI && HF == 0 && T % 8 == 7) {
                    mma_ip(gf[0], gf[0], rg[T / 8]);
                    SCHED();
                    if constexpr (T < 16) ldop(af, bf, ktc, CT<16 + T - 1>{});
                    SCHED();
                    mma_ip(gf[1], gf[1], rg[T / 8]);
                    SCHED();
                    if constexpr (T < 16) ldop(af, bf, ktc, CT<16 + T>{});
                    SCHED();
                }
                if constexpr (FI && X == 31) {
                    mma_ip(cat(bf[12], bf[13]), af[8], acc[4][0]);
                    SCHED();
                    mma_ip(cat(bf[14], bf[15]), af[9], acc[4][0]);
                    SCHED();
                }
            });
        };
        if (first) token();
        if (!first && id == 0 && wid == 3) {
#pragma unroll 1
            for (unsigned spins = 0; spins < (1u << 22); ++spins) {
                bool ok = true;
#pragma unroll
                for (int i = 0; i < 4; ++i)
                    ok &= __hip_atomic_load((gu64*)(oflags + 8192 + (i * 64 + lane) * 32), __ATOMIC_RELAXED, __HIP_MEMORY_SCOPE_AGENT) == tok;
                if (__all(ok)) break;
            }
            if (lane == 0) __hip_atomic_store((gu64*)epoch, tok, __ATOMIC_RELAXED, __HIP_MEMORY_SCOPE_AGENT);
        }
        bf16x8 a0[10], a1[10], a2[10];
        s16x4 b0[24], b1[24], b2[24];
        auto loop = [&](auto fic) INL {
#pragma unroll
        for (int i = 0; i < 5; ++i)
#pragma unroll
            for (int j = 0; j < 6; ++j)
                if (i < 4 || j == 2 || j == 4) {
                    acc[i][j] = f32x4{0.f, 0.f, 0.f, 0.f};
                    asm volatile("" : "+v"(acc[i][j]));
                }
            if constexpr (decltype(fic)::value) wait_vm<0>();
            BARRIER();
            static_for<0, 16>([&](auto oc) INL { ldop(a0, b0, CT<0>{}, oc); });
            auto ktile = [&](bf16x8 (&af)[10], s16x4 (&bf)[24], bf16x8 (&an)[10], s16x4 (&bn)[24], auto kc) INL {
                unit(af, bf, an, bn, kc, CT<0>{}, fic);
                WAIT_LGKM0();
                BARRIER();
                unit(af, bf, an, bn, kc, CT<1>{}, fic);
            };
#pragma unroll 1
            for (int it = 0; it < 5; ++it) {
                ktile(a0, b0, a1, b1, CT<0>{});
                ktile(a1, b1, a2, b2, CT<1>{});
                ktile(a2, b2, a0, b0, CT<2>{});
            }
            unit(a0, b0, a1, b1, CT<NK - 1>{}, CT<0>{}, fic);
            unit(a0, b0, a1, b1, CT<NK - 1>{}, CT<1>{}, fic);
        };
        if (first) {
#pragma unroll
            for (int i = 0; i < 3; ++i) {
                const int hr = hrow(i, l15);
#pragma unroll
                for (int kk = 0; kk < 2; ++kk) ha[i][kk] = opaque(lds0 + hr * 128 + (((kk * 4 + g) ^ ((hr >> 1) & 7)) * 16));
                rg[i] = f32x4{0.f, 0.f, 0.f, 0.f};
                asm volatile("" : "+v"(rg[i]));
            }
            acc[4][0] = f32x4{0.f, 0.f, 0.f, 0.f};
            asm volatile("" : "+v"(acc[4][0]));
            loop(CT<1>{});
            WAIT_LGKM0();
            BARRIER();
            static_for<0, 20>([&](auto oc) INL {
                constexpr int O = decltype(oc)::value;
                eld(CT<(O < 10 ? (int)(0x98d7c6b5a4ull >> (4 * O)) & 15 : O + 4)>{});
                SCHED();
            });
        } else {
            static_for<0, NEA>([&](auto tc) INL { eadr(tc); });
            loop(CT<0>{});
            BARRIER();
        }
        SCHED();
        auto ln2 = [&](int i, int j0, int j1) INL {
            f32x2 y[4];
#pragma unroll
            for (int x = 0; x < 4; ++x) {
                const int j = x < 2 ? j0 : j1;
                const f32x2 a = x & 1 ? acc[i][j].zw : acc[i][j].xy;
                if (j < 4) y[x] = __builtin_elementwise_fma(i & 1 ? rnm[i / 2].yy : rnm[i / 2].xx, x & 1 ? ccv[j].zw : ccv[j].xy, a);
                else y[x] = __builtin_elementwise_fma(x & 1 ? vnm[i].zw : vnm[i].xy, f32x2{ccw[j - 4], ccw[j - 4]}, a);
            }
            SCHED();
            acc[i][j0] = f32x4{y[0].x, y[0].y, y[1].x, y[1].y};
            acc[i][j1] = f32x4{y[2].x, y[2].y, y[3].x, y[3].y};
        };
        auto rnd = [&](f32x4 a, f32x4 b) INL {
            u32x4 r = {pk2(a[0], a[1]), pk2(a[2], a[3]), pk2(b[0], b[1]), pk2(b[2], b[3])};
            asm volatile("" : "+v"(r));
            SCHED();
            return r;
        };
        u32x4 qf[4], kf[4];
        u32x2 v3[2];
        ln2(4, 2, 4);
        {
            const u32x4 hv = rnd(acc[4][2], acc[4][4]);
            *(LDS_AS u32x2*)(size_t)(xw[0]) = hv.xy;
            *(LDS_AS u32x2*)(size_t)(xw[0] + 1024) = hv.zw;
        }
        ln2(3, 2, 3);
        kf[3] = rnd(acc[3][2], acc[3][3]);
        *(LDS_AS u32x2*)(size_t)(xw[1] + 8) = kf[3].xy;
        *(LDS_AS u32x2*)(size_t)(xw[1]) = kf[3].zw;
        ln2(3, 4, 5);
        v3[0] = u32x2{pk2(acc[3][4][0], acc[3][4][1]), pk2(acc[3][4][2], acc[3][4][3])};
        v3[1] = u32x2{pk2(acc[3][5][0], acc[3][5][1]), pk2(acc[3][5][2], acc[3][5][3])};
        *(LDS_AS u32x2*)(size_t)(xw[1] + 1032) = v3[0];
        *(LDS_AS u32x2*)(size_t)(xw[1] + 1024) = v3[1];
        SCHED();
        f32x4 vf[3][2];
        f32x2 sc[4];
#pragma unroll
        for (int i = 3; i >= 0; --i) {
            ln2(i, 0, 1);
            qf[i] = rnd(acc[i][0], acc[i][1]);
            if (i < 3) {
                ln2(i, 2, 3);
                kf[i] = rnd(acc[i][2], acc[i][3]);
                ln2(i, 4, 5);
                vf[i][0] = acc[i][4];
                vf[i][1] = acc[i][5];
                asm volatile("" : "+v"(vf[i][0]), "+v"(vf[i][1]));
            }
        }
#pragma unroll
        for (int i = 0; i < 4; ++i) {
            sc[i] = rk[i] * rsc[i & 1];
            asm volatile("" : "+v"(sc[i]));
        }
        WAIT_LGKM0();
        BARRIER();
        SCHED();
        att_run(qf, kf, vf, v3, am, rk, sc, xa, oo, C);
        SCHED();
    }
}

__device__ __forceinline__ unsigned dn_boff(int h, int lane) { return b_off(h, lane, D_, (bid_() >> 5) * 128); }
__device__ __forceinline__ void dn_stage_b(unsigned lds0, const u16* B, unsigned boff, int kh, int h, int i, int kg, int sl) {
    glds16(uni((const char*)B + (long long)(kg * 64 + (i & 1) * 32) * D_ * 2 + ((i >> 1) ^ kh) * 128), boff,
           lds0 + h * 1024 + sl * 16384 + i * 4096);
}
#define BOX_LOOK(r) "global_load_dwordx4 v[" r ":" r "+3], %14, %15 sc1\n\tglobal_load_dwordx4 v[" r "+4:" r "+7], %14, %15 offset:1024 sc1\n\t" \
    "global_load_dwordx4 v[" r "+8:" r "+11], %14, %15 offset:2048 sc1\n\tglobal_load_dwordx4 v[" r "+12:" r "+15], %14, %15 offset:3072 sc1\n\t"
#define BOX_MAX(r, i) "v_max3_u32 %12, %12, v[" r "+" i "], v[" r "+" i "+1]\n\t"
#define BOX_TEST(r) "s_waitcnt vmcnt(4)\n\tv_max_u32 %12, v[" r "], v[" r "+1]\n\t" BOX_MAX(r, "2") BOX_MAX(r, "4") BOX_MAX(r, "6") \
    BOX_MAX(r, "8") BOX_MAX(r, "10") BOX_MAX(r, "12") BOX_MAX(r, "14") "v_cmp_eq_u32 vcc, -1, %12\n\t"
#define BOX_MOV(r, i) "v_mov_b64 v[200+" i ":201+" i "], v[" r "+" i ":" r "+1+" i "]\n\t"
#define BOX_TAKE(r) BOX_MOV(r, "0") BOX_MOV(r, "2") BOX_MOV(r, "4") BOX_MOV(r, "6") BOX_MOV(r, "8") BOX_MOV(r, "10") BOX_MOV(r, "12") BOX_MOV(r, "14")
__device__ __forceinline__ void box_wait(u32x4 (&a)[4], u32x4 (&b)[4], u32x4 (&c)[4], const void* box, unsigned off) {
    unsigned t, n;
    asm volatile(BOX_TEST("200") "s_cbranch_vccz 3f\n\ts_mov_b32 %13, 0x100000\n"
                 "1:\n\t" BOX_LOOK("232") BOX_TEST("216") "s_cbranch_vccz 2f\n\t" BOX_LOOK("216") BOX_TEST("232")
                 "s_cbranch_vccz 4f\n\ts_sub_u32 %13, %13, 1\n\ts_cbranch_scc0 1b\n"
                 "4:\n\t" BOX_TAKE("232") "s_branch 5f\n"
                 "2:\n\t" BOX_TAKE("216") "\n5:\n\ts_nop 1\n3:"
                 : "+{v[200:203]}"(a[0]), "+{v[204:207]}"(a[1]), "+{v[208:211]}"(a[2]), "+{v[212:215]}"(a[3]),
                   "+{v[216:219]}"(b[0]), "+{v[220:223]}"(b[1]), "+{v[224:227]}"(b[2]), "+{v[228:231]}"(b[3]),
                   "=&{v[232:235]}"(c[0]), "=&{v[236:239]}"(c[1]), "=&{v[240:243]}"(c[2]), "=&{v[244:247]}"(c[3]), "=&v"(t), "=&s"(n)
                 : "v"(off), "s"(box) : "memory", "vcc", "scc");
}
__device__ __forceinline__ void dn_phase(char* smem, const u16* A, const u16* __restrict__ B, u16* C, float* sp,
                                         float* slab, unsigned* gflags, unsigned* oflags,
                                         const u16* pf, u64 tok) {
    constexpr int BM = 128, NS = 4, NSO = NSOLO;
    constexpr int ASZ = BM * 128, SLOT = 2 * ASZ;
    constexpr int lda = F_;
    constexpr int BOX = 16384;

    const int tid = (int)opaque(threadIdx.x), lane = tid & 63;
    const int wid = __builtin_amdgcn_readfirstlane(tid >> 6);
    const unsigned bx = bid_();
    const int xcd = bx & 7, r_ = bx >> 3;
    const int kh = DPAR ? xcd & 1 : xcd >> 2, tm = dn_grp(xcd) * 4 + (r_ & 3), tn = r_ >> 2;
    const int m0 = tm * BM, n0 = tn * 128 + kh * 64;
    const unsigned lds0 = __builtin_amdgcn_readfirstlane((unsigned)(size_t)(LDS_AS char*)smem);
    ResC rc;
    u32x4 pb[4], pc[4];
    if (wid >= 4) {
        const int h = wid - 4;
        unsigned aoff = (unsigned)((m0 / 4 + h * 2 + (lane >> 5)) * (8 * F_) + (((lane >> 3) & 3) ^ (h & 1)) * 128 + (lane & 7) * 16);
        unsigned boff = dn_boff(h, lane);
        const unsigned* const f = gflags + (tm * 32 + (lane & 31)) * 32;
        u64 have, at = (u64)(((1u << DLK) - 1u) << (xcd & 6) * 4), lk;
        look(lk, at, f);
        asm volatile("" : "+v"(aoff), "+v"(boff), "+s"(A));
        pubc<1>(gflags + ((r_ & 7) * 64 + 32 + xcd * 4 + (r_ >> 3)) * 32, tok, lds0 + GCN + 4);
        have = seen(lk, at, tok);
        auto stage = [&](int kg, int sl, auto nbc) INL {
            poll(have, (1u << (kg * 4 / 7)) | (1u << ((kg * 64 + 63) / 112)), at, f, tok);
#pragma unroll
            for (int i = 0; i < 4; ++i)
                glds16_dev(uni((const char*)A + kg * 512 + i * 32 * lda * 2), aoff, lds0 + DNA + h * 1024 + sl * ASZ + i * 4096);
#pragma unroll
            for (int i = 0; i < decltype(nbc)::value; ++i) dn_stage_b(lds0, B, boff, kh, h, i, kg, sl);
        };
        const int kg0 = (xcd & 6) * 7;
        int kg = kg0, sl = kh ? dn_first(DNO) : dn_first(DNE);
        auto next = [&](auto nbc) INL {
            stage(kg, sl, nbc);
            kg = kg == 55 ? 0 : kg + 1;
            sl = (sl + 1) & 3;
        };
        const u64 DW2 = 0xffffffffull;
        next(CT<0>{});
        if constexpr (DNE != DNO) BARRIER();
        if constexpr (DL2 == 0) look(lk, DW2, f);
        next(CT<0>{});
        wait_vm<4>();
        BARRIER();
        if constexpr (DL2 == 0) { at = DW2; have |= seen(lk, at, tok); }
        if constexpr (DL2 == 1) look(lk, DW2, f);
        next(CT<0>{});
        next(CT<0>{});
        wait_vm<8>();
        BARRIER();
        if constexpr (DL2 == 1) { at = DW2; have |= seen(lk, at, tok); }
        next(CT<4>{});
        wait_vm<12>();
        BARRIER();
        next(CT<4>{});
#pragma unroll 1
        for (int n = (kh ? DNO : DNE) - 6; n > 0; --n) {
            wait_vm<16>();
            BARRIER();
            next(CT<4>{});
        }
        static_for<-NS, NSO - 1>([&](auto kc) INL {
            constexpr int KT = decltype(kc)::value;
            constexpr int P2 = KT + 2 < 0 ? 8 : (KT + 2 < NSO ? 6 : 0), P3 = KT + 3 < 0 ? 8 : (KT + 3 < NSO ? 6 : 0);
            wait_vm<P2 + P3>();
            BARRIER();
            if constexpr (KT < 0) {
                constexpr int J = KT + NS, RE = DNE + J < 28 ? DNE + J : 52 + J, RO = DNO + J < 28 ? DNO + J : 52 + J;
                const int ks = kg0 + (kh ? RO : RE);
                stage(ks < 56 ? ks : ks - 56, (KT + 2) & 3, CT<2>{});
            }
        });
        if (pf != nullptr) {
            for (int i = QPS; i > 0; --i) __builtin_amdgcn_s_sleep(1);
            qkv_stage_b(lds0, pf, qkv_boff(h, lane), h, qkv_k(qkv_ks(), 0), qsl(0));
            qkv_stage_b(lds0, pf, qkv_boff(h, lane), h, qkv_k(qkv_ks(), 1), qsl(1));
        }
        res_setup(rc, lds0 + NS * SLOT, m0, n0, tid);
        if (pf != nullptr && (bx & 1) == 0 && wid >= 6) {
            const unsigned ix = (bx >> 3) * 96 + (tid < 480 ? tid - 384 : 0);
            touch1(SINK, pf, ix * 2048 + ((bx & 3) * 4 + 3) * 128);
        }
#pragma unroll
        for (int i = 0; i < 4; ++i) asm volatile("" : "=v"(pb[i]), "=v"(pc[i]));
    } else {
        const int wm = wid >> 1, wn = wid & 1;
        const int l15 = lane & 15, g = lane >> 4, q = l15 >> 2, p4 = l15 & 3;
        unsigned ab[2], bb[2];
#pragma unroll
        for (int x = 0; x < 2; ++x) {
            ab[x] = opaque(lds0 + DNA + (wm * 16 + q) * 512 + (((x * 2 + (g >> 1)) ^ (l15 >> 3)) * 128) + p4 * 32 + (g & 1) * 16);
            bb[x] = opaque(lds0 + (g * 8 + q) * 128 + p4 * 8 + (((wn * 2 + x) ^ ((q >> 1) | ((g & 1) << 1))) << 5));
        }
        f32x4 acc[4][4];
        bf16x8 rr[4];
        const bf16x8 selr[2] = {pick(g == (l15 >> 3), l15 & 7), pick(g == 2 + (l15 >> 3), l15 & 7)};
        {
            const unsigned ra = lds0 + DNR + (wm * 64 + l15) * 128 + (((wn * 4 + g) ^ (l15 >> 1)) * 16);
            static_for<0, 4>([&](auto ic) INL {
                constexpr int i = decltype(ic)::value;
                rr[i] = lds_a<i * 2048>(ra);
#pragma unroll
                for (int j = 0; j < 2; ++j) {
                    acc[i][j] = f32x4{0.f, 0.f, 0.f, 0.f};
                    asm volatile("" : "+v"(acc[i][j]));
                }
            });
#pragma unroll
            for (int i = 0; i < 4; ++i) asm volatile("" : "+v"(rr[i]));
            if constexpr (DNE != DNO) BARRIER();
        }
        auto ldop = [&](bf16x8 (&af)[8], s16x4 (&bf)[16], auto slc, auto j0c, auto oc) INL {
            constexpr int SL = decltype(slc)::value, O = decltype(oc)::value;
            if constexpr (O == 2 || (O >= 5 && O < 12)) {
                constexpr int X = O == 2 ? 0 : O - 4;
                af[(X & 1) * 4 + X / 2] = lds_a<SL * ASZ + (X / 2) * 2048>(ab[X & 1]);
            } else {
                constexpr int X = decltype(j0c)::value * 4 + (O < 2 ? O : (O < 5 ? O - 1 : O - 8)), J = X / 4;
                bf[X] = lds_h<SL * ASZ + (J < 2 ? 8192 : 0) + ((X >> 1) & 1) * 4096 + (X & 1) * 512>(bb[J & 1]);
            }
        };
        auto ktile = [&](bf16x8 (&af)[8], s16x4 (&bf)[16], bf16x8 (&an)[8], s16x4 (&bn)[16], auto nslc, auto j0c, auto jnc, auto&& gap) INL {
            constexpr int J0 = decltype(j0c)::value, JN = decltype(jnc)::value;
            constexpr int NM = 8 * (4 - J0), NL = JN < 4 ? 8 + 4 * (4 - JN) : 0, TB = J0 ? DBAR2 : DBAR;
            constexpr int NG = NM - TB < NL ? NM - TB : NL;
            static_for<0, NM>([&](auto tc) INL {
                constexpr int T = decltype(tc)::value, j = J0 + T / 8, i = j & 1 ? 3 - (T / 2) % 4 : (T / 2) % 4, kk = T & 1;
                if constexpr (T == TB && NL > 0) {
                    WAIT_LGKM0();
                    BARRIER();
                }
                mma_ip(cat(bf[j * 4 + kk * 2], bf[j * 4 + kk * 2 + 1]), af[kk * 4 + i], acc[i][j]);
                SCHED();
                if constexpr (NL > 0 && T >= TB) {
                    constexpr int U = T - TB, lo_ = (U * NL + NG - 1) / NG, hi_ = ((U + 1) * NL + NG - 1) / NG;
                    static_for<lo_, (hi_ < NL ? hi_ : NL)>([&](auto oc) INL { ldop(an, bn, nslc, jnc, oc); });
                }
                gap(tc);
                SCHED();
            });
        };
        bf16x8 a0[8], a1[8];
        s16x4 b0[16], b1[16];
        const unsigned bo = opaque((unsigned)(wid * 4096 + lane * 16));
        const bf16x8 sel[2] = {pick(g == q, p4), pick(g == q, 4 + p4)};
        res_setup(rc, lds0 + NS * SLOT, m0, n0, tid);
        const unsigned cw = opaque(lds0 + NS * SLOT + (wm * 64 + l15) * CSTR + (wn * 32 + g * 4) * 2);
        const char* const rin = sopq((const char*)slab + (bx ^ DPX) * BOX);
        const __amdgpu_buffer_rsrc_t rout = __builtin_amdgcn_make_buffer_rsrc((char*)slab + bx * BOX, 0, BOX, 0x00020000);
        BARRIER();
        auto none = [&](auto) INL {};
        auto head = [&](auto nc) INL {
            constexpr int H = (decltype(nc)::value - 1) % NS;
            static_for<0, 24>([&](auto oc) INL {
                if constexpr (H & 1) ldop(a1, b1, CT<(1 - H) & 3>{}, CT<0>{}, oc);
                else ldop(a0, b0, CT<(1 - H) & 3>{}, CT<0>{}, oc);
            });
            SCHED();
#pragma unroll
            for (int t = 0; t < 8; ++t) mma_z(selr[t & 1], rr[t >> 1], acc[t >> 1][2 + (t & 1)]);
            SCHED();
            static_for<0, H>([&](auto jc) INL {
                constexpr int E = H - decltype(jc)::value;
                if constexpr (E & 1) ktile(a1, b1, a0, b0, CT<(2 - E) & 3>{}, CT<0>{}, CT<0>{}, none);
                else ktile(a0, b0, a1, b1, CT<(2 - E) & 3>{}, CT<0>{}, CT<0>{}, none);
            });
        };
        if ((DNE - DNO) % NS == 0 || kh) head(CT<DNO>{}); else head(CT<DNE>{});
#pragma unroll 1
        for (int n = (kh ? DNO - 1 : DNE - 1) / NS; n > 0; --n) {
            ktile(a0, b0, a1, b1, CT<2>{}, CT<0>{}, CT<0>{}, none);
            ktile(a1, b1, a0, b0, CT<3>{}, CT<0>{}, CT<0>{}, none);
            ktile(a0, b0, a1, b1, CT<0>{}, CT<0>{}, CT<0>{}, none);
            ktile(a1, b1, a0, b0, CT<1>{}, CT<0>{}, CT<0>{}, none);
        }
        u32x4 ow, pa[4];
        auto look = [&](u32x4& d, auto ic) INL { gload16_devo<decltype(ic)::value * 1024>(d, rin, bo); };
        static_for<-1, NSO>([&](auto sc) INL {
            constexpr int KT = decltype(sc)::value;
            constexpr int J0 = KT < 0 ? 0 : 2, JN = KT + 1 < NSO ? 2 : 4;
            auto gap = [&](auto tc) INL {
                constexpr int T = decltype(tc)::value;
                if constexpr (KT < 0 && T >= XO && T < XO + 16) {
                    constexpr int U = T - XO, j = U / 8, i = j ? 3 - (U / 2) % 4 : (U / 2) % 4;
                    asm volatile("v_cvt_pk_bf16_f32 %0, %1, %2\n\tv_min_u32 %0, -2, %0"
                                 : "=v"(ow[(i & 1) * 2 + (U & 1)]) : "v"(acc[i][j][(U & 1) * 2]), "v"(acc[i][j][(U & 1) * 2 + 1]));
                    if constexpr (U % 4 == 3) __builtin_amdgcn_raw_buffer_store_b128(ow, rout, bo + (j * 2 + i / 2) * 1024, 0, 16);
                }
                constexpr int X = KT < 0 ? -999 : (KT - NSO) * 16 + T;
                if constexpr (X >= -XA && X < -XA + 4) look(pa[X + XA], CT<X + XA>{});
                if constexpr (X >= -XB && X < -XB + 4) look(pb[X + XB], CT<X + XB>{});
            };
            if constexpr (KT & 1) ktile(a0, b0, a1, b1, CT<(KT + 3) % NS>{}, CT<J0>{}, CT<JN>{}, gap);
            else ktile(a1, b1, a0, b0, CT<(KT + 3) % NS>{}, CT<J0>{}, CT<JN>{}, gap);
        });
        box_wait(pa, pb, pc, rin, bo);
#pragma unroll
        for (int t = 0; t < 8; ++t) mma_ip(sel[(t >> 1) & 1], __builtin_bit_cast(bf16x8, pa[(t & 1) * 2 + t / 4]), acc[t >> 1][2 + (t & 1)]);
        asm volatile("s_nop 1" ::: "memory");
        SCHED();
#pragma unroll
        for (int i = 0; i < 4; ++i) {
#pragma unroll
            for (int x = 0; x < 2; ++x) {
                const f32x4 a = acc[i][2 + x];
                *(LDS_AS u32x2*)(size_t)(cw + i * 16 * CSTR + x * 32) = u32x2{pk2(a[0], a[1]), pk2(a[2], a[3])};
            }
            SCHED();
        }
    }
    WAIT_LGKM0();
    BARRIER();
    res_tail(rc, C, sp);
    if (pf == nullptr) wait_vm<0>();
    else if (wid < 4) {
        pubc<0, 8>(oflags + (tm * 16 + (n0 >> 6)) * 32, tok, lds0 + GCN + 8);
        if (wid == 2) code_touch(SINK, opaque(threadIdx.x) & 63u);
    }
#pragma unroll
    for (int i = 0; i < 4; ++i) asm volatile("" :: "v"(pb[i]), "v"(pc[i]));
}

__device__ __forceinline__ void gu_phase(char* smem, const u16* A, const u16* B0, const u16* B1, u16* C, const float* cs,
                                         const float* sp, const u16* wd, unsigned* wflags, unsigned* gflags, const GuC& gc, f32x4 cp,
                                         u64 lk, u64 tok) {
    constexpr int BM = 256, BN = 224, MI = 4, NI = 7, NK = D_ / 64;
    constexpr int ASZ = BM * 128, BSZ = GBS, NSA = 3, NSB = 2, BRING = GBR, STL = GST;
    constexpr auto asl = [](int kt) { return (kt + 2) % NSA; };

    const int wid = __builtin_amdgcn_readfirstlane(opaque(threadIdx.x) >> 6);
    const int id = wg_id();
    const int tm = id & 7, tn = id >> 3, m0 = tm * BM;
    const unsigned lds0 = __builtin_amdgcn_readfirstlane((unsigned)(size_t)(LDS_AS char*)smem);
    const unsigned ldsw = lds0 + wid * 1024;

    const auto& aoff = gc.aoff;
    const auto& boff = gc.boff;
    const __amdgpu_buffer_rsrc_t ra = __builtin_amdgcn_make_buffer_rsrc((void*)A, 0, 0x7fffffff, 0x00020000);
    const __amdgpu_buffer_rsrc_t rb[4] = {__builtin_amdgcn_make_buffer_rsrc((void*)B0, 0, 0x7fffffff, 0x00020000),
                                          __builtin_amdgcn_make_buffer_rsrc((void*)(wid >= 6 ? B1 : B0), 0, 0x7fffffff, 0x00020000),
                                          __builtin_amdgcn_make_buffer_rsrc((void*)B1, 0, 0x7fffffff, 0x00020000),
                                          __builtin_amdgcn_make_buffer_rsrc((void*)B1, 0, wid < 4 ? 0x7fffffff : 0, 0x00020000)};
    const unsigned b3d[2] = {wid < 4 ? ldsw + BRING + 24576 : lds0 + 161792, wid < 4 ? ldsw + BRING + BSZ + 24576 : lds0 + 161792};
    unsigned soff = 0u;
    auto prep = [&](auto bc, auto pc, auto ktc) INL {
        constexpr int P = decltype(pc)::value, KT = decltype(ktc)::value;
        constexpr bool ISB = decltype(bc)::value;
        if constexpr (P == 0) s_set<(ISB ? gu_k(KT) * 64 * F_ * 2 : gu_k(KT) * 128)>(soff);
        if constexpr (ISB && P == 3) m0_set<0>(b3d[KT % NSB]);
        else m0_set<(ISB ? BRING + (KT % NSB) * BSZ : asl(KT) * ASZ) + P * 8192>(ldsw);
    };
    const int dk = DPAR ? (id >> 5) & 1 : id >> 7;
    const int dtm = dn_grp(id >> 5) * 4 + (id & 3), dtn = ((id >> 2) & 7) * 2 + dk;
    const unsigned dkh = dk * (128 + (dn_first(DNE) ^ dn_first(DNO)) * 64 * D_ * 2);
    const __amdgpu_buffer_rsrc_t rd = __builtin_amdgcn_make_buffer_rsrc((char*)wd + (id >> 6) * (14 * 64 * D_ * 2) + (dtn >> 1) * 256, 0,
        0x7fffffff, 0x00020000);
    const __amdgpu_buffer_rsrc_t rr = __builtin_amdgcn_make_buffer_rsrc((char*)A + (dtm * 128 - m0) * D_ * 2 + dtn * 128, 0, 0x7fffffff, 0x00020000);
    unsigned doff;
    constexpr auto dreq = [](int sl) { return sl < 0 ? -1 : (int)((DSCH >> (4 * sl)) & 15) - 1; };
    constexpr int DV = (dreq(0) >= 0) + (dreq(1) >= 0) + (dreq(2) >= 0) + (dreq(3) >= 0) + (TCU <= 0);
    auto dprep = [&](auto xc) INL {
        constexpr int X = decltype(xc)::value;
        if constexpr (X == 0) {
            const int t = (int)opaque(threadIdx.x);
            doff = opaque(b_off(t >> 6, t & 63, D_, 0));
        }
        if constexpr (X < 8) asm volatile("s_xor_b32 %0, %1, %2" : "=s"(soff) : "s"(dkh), "n"((((X >> 1) - dn_first(DNE)) & 3) * 64 * D_ * 2 + (X & 1) * 128) : "scc");
        m0_set<(X < 8 ? 0 : DNR - 65536) + X * 8192>(ldsw);
    };
    auto dgo = [&](auto xc) INL {
        constexpr int X = decltype(xc)::value;
        if constexpr (X < 8) blds16<false>(rd, doff, soff);
        else blds16<true>(rr, aoff[X - 8], 0u);
    };
    auto go = [&](auto bc, auto pc) INL {
        if constexpr (decltype(bc)::value) blds16<false>(rb[decltype(pc)::value], boff[decltype(pc)::value], soff);
        else blds16<true>(ra, aoff[decltype(pc)::value], soff);
    };
    auto stage_a = [&](auto ktc) INL {
        static_for<0, 4>([&](auto pc) INL { prep(CT<0>{}, pc, ktc); asm volatile("s_nop 0"); go(CT<0>{}, pc); });
    };
    __builtin_amdgcn_s_setprio(2);
    const auto& ab = gc.ab;
    const auto& bb = gc.bb;
    f32x4 acc[MI][NI];
    bf16x8 af[2][2 * MI];
    s16x4 bf[2][4 * NI];
    auto ldop = [&](auto ktc, auto oc) INL {
        constexpr int KT = decltype(ktc)::value, O = decltype(oc)::value;
        if constexpr (O == 2 || (O >= 5 && O < 12)) {
            constexpr int X = O == 2 ? 0 : O - 4, off = asl(KT) * ASZ + (X / 2) * 2048;
            af[KT & 1][X] = lds_a<(off & 65535)>(ab[X & 1][off >> 16]);
        } else {
            constexpr int X = O < 2 ? O : (O < 5 ? O - 1 : O - 8), J = X / 4;
            bf[KT & 1][X] = lds_h<(KT % NSB) * BSZ + ((X >> 1) & 1) * 32 * BN + (J == 6 ? 0 : (J / 3) * (BSZ / 2) + (J % 3) * 32)>(bb[X & 1][J == 6]);
        }
    };
    f32x4 stn[4];
    float sx[8];
    const char* const spw = uni((const char*)sp + m0 * 128);
    auto stats = [&](auto zc) INL {
        constexpr int Z = decltype(zc)::value;
        if constexpr (Z == 0) {
#pragma unroll
            for (int i = 0; i < 4; ++i) asm volatile("" : "+v"(stn[i]));
        }
        if constexpr (Z < 8) asm volatile("v_add_f32 %0, %1, %2" : "=v"(sx[Z]) : "v"(stn[Z / 4 * 2][Z % 4]), "v"(stn[Z / 4 * 2 + 1][Z % 4]));
        else if constexpr (Z < 12) asm volatile("v_add_f32 %0, %0, %1" : "+v"(sx[(Z & 1) * 2 + (Z / 2 & 1)]) : "v"(sx[4 + (Z & 1) * 2 + (Z / 2 & 1)]));
        else if constexpr (Z < 14) asm volatile("v_permlane16_swap_b32 %0, %1" : "+v"(sx[Z - 12]), "+v"(sx[Z - 10]));
        else if constexpr (Z < 16) asm volatile("v_add_f32 %0, %0, %1" : "+v"(sx[Z - 14]) : "v"(sx[Z - 12]));
        else if constexpr (Z == 17 || Z == 22) asm volatile("v_permlane32_swap_b32 %0, %1" : "+v"(sx[0]), "+v"(sx[1]));
        else if constexpr (Z == 18) asm volatile("v_add_f32 %0, %0, %1" : "+v"(sx[0]) : "v"(sx[1]));
        else if constexpr (Z == 19) asm volatile("v_mul_f32 %0, 0x3a800000, %1" : "=v"(sx[1]) : "v"(sx[0]));
        else if constexpr (Z == 20) asm volatile("v_mul_f32 %0, 0x3a800000, %0" : "+v"(sx[0]));
        else if constexpr (Z == 23) asm volatile("v_mul_f32 %0, %1, %1" : "=v"(sx[2]) : "v"(sx[0]));
        else if constexpr (Z == 24) asm volatile("v_sub_f32 %0, %0, %1" : "+v"(sx[1]) : "v"(sx[2]));
        else if constexpr (Z == 25) asm volatile("v_max_f32 %0, 0, %0" : "+v"(sx[1]));
        else if constexpr (Z == 26) asm volatile("v_cvt_pk_bf16_f32 %0, %0, -%1" : "+v"(sx[1]) : "v"(sx[0]));
        else if constexpr (Z == 27) asm volatile("v_and_b32 %0, 0xffff0000, %1" : "=v"(sx[0]) : "v"(sx[1]));
        else if constexpr (Z == 28 || Z == 31) asm volatile("v_lshlrev_b32 %0, 16, %0" : "+v"(sx[1]));
        else if constexpr (Z == 29) asm volatile("v_add_f32 %0, 0x3727c5ac, %0" : "+v"(sx[1]));
        else if constexpr (Z == 30 || Z == 33) asm volatile("v_cvt_pk_bf16_f32 %0, %0, %0" : "+v"(sx[1]));
        else if constexpr (Z == 32) asm volatile("v_sqrt_f32 %0, %0" : "+v"(sx[1]));
        else if constexpr (Z == 34) asm volatile("v_lshlrev_b32 %0, 16, %1" : "=v"(sx[2]) : "v"(sx[1]));
        else if constexpr (Z == 35) asm volatile("v_rcp_f32 %0, %1" : "=v"(sx[1]) : "v"(sx[2]));
        else if constexpr (Z == 36) asm volatile("v_mul_f32 %0, %0, %0" : "+v"(sx[2]));
        else if constexpr (Z == 37) asm volatile("v_mul_f32 %0, 0xbfb8aa3b, %0" : "+v"(sx[1]));
        else if constexpr (Z == 16) asm volatile("v_and_b32 %0, 31, %1" : "=v"(sx[4]) : "v"(threadIdx.x));
        else if constexpr (Z == 21) asm volatile("v_lshl_add_u32 %0, %0, 4, %1" : "+v"(sx[4]) : "s"(lds0 + STL + wid * 512));
        else if constexpr (Z == 38) *(LDS_AS f32x4*)(size_t)(__float_as_uint(sx[4])) = f32x4{sx[0], sx[1], sx[2], sx[3]};
    };
    static_assert((2 * (NK - 2) + 1 - (2 * STK + 5)) * SPU >= 39, "statistics in time");
    f32x4 ccv[NI], stv[MI];
    const unsigned cca = gc.cca, cc6 = gc.cc6, sta = gc.sta, gof = gc.gof, gofm = gc.gofm, hsw = gc.hsw, hsr = gc.hsr;
    auto eld = [&](auto oc) INL {
        constexpr int O = decltype(oc)::value;
        constexpr int SI = O == 0 ? 0 : (O == 3 ? 1 : (O == 7 ? 2 : (O == 8 ? 3 : -1)));
        constexpr int J = O < 3 ? O * 3 - 3 : (O < 6 ? O * 3 - 11 : (O == 6 ? 6 : O * 3 - 25));
        if constexpr (SI >= 0) stv[SI] = *(const LDS_AS f32x4*)(size_t)(sta + (SI >> 1) * 512 + (SI & 1) * 128);
        else if constexpr (J == 6) ccv[6] = *(const LDS_AS f32x4*)(size_t)(cc6);
        else ccv[J] = *(const LDS_AS f32x4*)(size_t)(cca + ((J / 3) * 112 + (J % 3) * 16) * 4);
    };
    auto unit = [&](auto ktc, auto kkc) INL {
        constexpr int KT = decltype(ktc)::value, KK = decltype(kkc)::value;
        constexpr int NKT = KK ? KT + 1 : KT;
        static_for<0, MI * NI>([&](auto tc) INL {
            constexpr int T = decltype(tc)::value, X = KK * MI * NI + T, j = X / 8, i = j & 1 ? MI - 1 - (X / 2) % MI : (X / 2) % MI;
            if constexpr (KT == 0 && (X & 1) == 0) mma_z(cat(bf[0][4 * j], bf[0][4 * j + 1]), af[0][2 * i], acc[i][j]);
            else mma_ip(cat(bf[KT & 1][4 * j + 2 * (X & 1)], bf[KT & 1][4 * j + 2 * (X & 1) + 1]), af[KT & 1][2 * i + (X & 1)], acc[i][j]);
            SCHED();
            constexpr bool RQ = KT + 2 < NK && T < 4 * GGS;
            if constexpr (RQ && T % GGS == 0) prep(kkc, CT<T / GGS>{}, CT<KT + 2>{});
            if constexpr (RQ && T % GGS == 1) go(kkc, CT<T / GGS>{});
            constexpr int DX = T < 4 * GGS ? dreq((2 * (KT - NK + 2) + KK) * 4 + T / GGS) : -1;
            if constexpr (DX >= 0 && T % GGS == 0) dprep(CT<DX>{});
            if constexpr (DX >= 0 && T % GGS == 1) dgo(CT<DX>{});
            if constexpr (2 * (KT - NK + 2) + KK == TCU && T == TCT) touch1(SINK, wd, gc.tof);
            constexpr int O = T - (T + GGS - 2) / GGS;
            if constexpr (NKT < NK && T % GGS != 1 && O < (KK ? 12 : 36 - GL2)) ldop(CT<NKT>{}, CT<(KK ? O : O + GL2)>{});
            if constexpr (NKT < NK && KK == 1 && T >= 20 && T < 8 + GL2) ldop(CT<NKT>{}, CT<T - 8>{});
            if constexpr (KT == 0 && KK == 1 && T == GLT) asm volatile("global_load_dwordx2 %0, %1, %2 sc1" : "=v"(lk) : "v"(gc.f2), "s"(cs) : "memory");
            if constexpr (KT == STK && KK == 1 && T >= STT && T < STT + 4) gload16_devo<(T - STT) * 256>(stn[T - STT], spw, gc.sto);
            constexpr int Z = (2 * KT + KK - 2 * STK - 5) * SPU + T - (MI * NI - SPU);
            if constexpr (Z >= 0 && Z < 39 && T >= MI * NI - SPU) stats(CT<Z>{});
            if constexpr (NKT == NK && T < 11) eld(tc);
            if constexpr (KK == 0 && T == PRT && KT < NK - 1) __builtin_amdgcn_s_setprio(0);
            SCHED();
        });
    };

        asm volatile("" :: "s"(ra), "s"(rb[0]), "s"(rb[1]), "s"(rb[2]), "s"(rb[3]), "s"(b3d[0]), "s"(b3d[1]), "s"(rd), "s"(rr), "s"(dkh), "s"(spw), "s"(ldsw));
    static_for<0, 4>([&](auto pc) INL { prep(CT<1>{}, pc, CT<1>{}); asm volatile("s_nop 0"); go(CT<1>{}, pc); });
    asm volatile("s_cmp_eq_u32 %2, 0\n\ts_cselect_b64 exec, 1, 0\n\ts_waitcnt vmcnt(6)\n\ts_barrier\n\t"
                 "global_store_dwordx2 %3, %4, %5 sc1\n\ts_mov_b64 exec, -1\n\ts_cbranch_scc1 1f\n\ts_waitcnt vmcnt(4)\n"
                 "1:\n\ts_waitcnt vmcnt(5)"
                 : "+{v[194:195]}"(lk), "+{v[196:199]}"(cp) : "s"(wid), "v"(0u), "v"(tok),
                   "s"(wflags + (((id / 64) * 4 + id % 4) * 16 + (id % 64) / 4) * 32) : "memory", "scc");
    spin(lk, cs, gc.f1, tok);
    BARRIER();
    stage_a(CT<0>{});
    asm volatile("v_add_f32_dpp v196, v196, v196 row_ror:8 row_mask:0xf bank_mask:0xf\n\t"
                 "v_add_f32_dpp v197, v197, v197 row_ror:8 row_mask:0xf bank_mask:0xf\n\t"
                 "v_add_f32_dpp v198, v198, v198 row_ror:8 row_mask:0xf bank_mask:0xf\n\t"
                 "v_add_f32_dpp v199, v199, v199 row_ror:8 row_mask:0xf bank_mask:0xf\n\t"
                 "v_permlane16_swap_b32 v196, v197\n\ts_nop 0\n\tv_permlane16_swap_b32 v198, v199\n\t"
                 "v_add_f32 v196, v196, v197\n\tv_add_f32 v198, v198, v199\n\ts_nop 1\n\tv_permlane32_swap_b32 v196, v198\n\t"
                 "v_add_f32 v196, v196, v198\n\tds_write_b32 %1, v196" : "+{v[196:199]}"(cp) : "v"(gc.cw) : "memory");
    {
        u32x4 z; unsigned a;
        asm volatile("v_mov_b32 %1, %2\n\tv_mov_b64 v[200:201], 0\n\tv_mov_b64 v[202:203], 0\n\tds_write_b128 %1, %0\n\tds_write_b128 %1, %0 offset:16"
                     : "=&{v[200:203]}"(z), "=&v"(a) : "s"(lds0 + GCN) : "memory");
    }
    constexpr auto isa = [](int o) { return o == 2 || (o >= 5 && o < 12); };
    if constexpr (GPV >= 1) static_for<0, GL2>([&](auto oc) INL { if constexpr (!isa(decltype(oc)::value)) ldop(CT<0>{}, oc); });
    SCHED();
    if constexpr (GPV == 2) stage_a(CT<1>{});
    wait_vm<(GPV == 2 ? 4 : 0)>();
    BARRIER();
    if constexpr (GPV == 0) stage_a(CT<1>{});
    static_for<0, GL2>([&](auto oc) INL { if constexpr (GPV == 0 || isa(decltype(oc)::value)) { ldop(CT<0>{}, oc); SCHED(); } });
    SCHED();
    if constexpr (GPV == 1) stage_a(CT<1>{});
    static_for<0, NK>([&](auto kc) INL {
        constexpr int KT = decltype(kc)::value;
        unit(CT<KT>{}, CT<0>{});
        if constexpr (KT < NK - 1) {
            WAIT_LGKM0();
            wait_vm<(KT == STK + 1 ? 8 : (KT == NK - 2 ? DV : 4))>();
            if constexpr (KT == 1) spin(lk, cs, gc.f2, tok);
            BARRIER();
            if constexpr (PRT < 28) __builtin_amdgcn_s_setprio(2);
        }
        unit(CT<KT>{}, CT<1>{});
    });
    asm volatile("s_nop 15\n\ts_nop 7" ::: "memory");
    SCHED();
    const int tide = (int)opaque(threadIdx.x);
    {
        __builtin_amdgcn_s_setprio(0);
        auto center = [&](f32x4 a, f32x4 c, float nm) INL {
            float a0, a1, a2, a3;
            asm("v_fma_f32 %0, %4, %5, %9\n\tv_fma_f32 %1, %4, %6, %10\n\tv_fma_f32 %2, %4, %7, %11\n\tv_fma_f32 %3, %4, %8, %12"
                : "=&v"(a0), "=&v"(a1), "=&v"(a2), "=&v"(a3)
                : "v"(nm), "v"(c[0]), "v"(c[1]), "v"(c[2]), "v"(c[3]), "v"(a[0]), "v"(a[1]), "v"(a[2]), "v"(a[3]));
            return f32x4{a0, a1, a2, a3};
        };
        auto glu = [&](f32x4 gp, f32x4 up, float c, float k) INL {
            float t0, t1, t2, t3, u0 = up[0], u1 = up[1], u2 = up[2], u3 = up[3];
            asm("v_mul_f32 %0, %12, %8\n\tv_mul_f32 %1, %12, %9\n\tv_mul_f32 %2, %12, %10\n\tv_mul_f32 %3, %12, %11\n\t"
                "v_exp_f32 %0, %0\n\tv_exp_f32 %1, %1\n\tv_exp_f32 %2, %2\n\tv_exp_f32 %3, %3\n\t"
                "v_mul_f32 %4, %4, %8\n\tv_mul_f32 %5, %5, %9\n\t"
                "v_fma_f32 %0, %0, %13, %13\n\tv_fma_f32 %1, %1, %13, %13\n\t"
                "v_fma_f32 %2, %2, %13, %13\n\tv_fma_f32 %3, %3, %13, %13\n\t"
                "v_rcp_f32 %0, %0\n\tv_rcp_f32 %1, %1\n\tv_rcp_f32 %2, %2\n\tv_rcp_f32 %3, %3\n\t"
                "v_mul_f32 %6, %6, %10\n\tv_mul_f32 %7, %7, %11\n\t"
                "v_mul_f32 %4, %4, %0\n\tv_mul_f32 %5, %5, %1\n\tv_mul_f32 %6, %6, %2\n\tv_mul_f32 %7, %7, %3"
                : "=&v"(t0), "=&v"(t1), "=&v"(t2), "=&v"(t3), "+v"(u0), "+v"(u1), "+v"(u2), "+v"(u3)
                : "v"(gp[0]), "v"(gp[1]), "v"(gp[2]), "v"(gp[3]), "v"(c), "v"(k));
            return u32x2{pk2(u0, u1), pk2(u2, u3)};
        };
        auto centerp = [&](f32x4 a, f32x4 c, float nm) INL {
            const f32x2 n2 = {nm, nm};
            const f32x2 lo = __builtin_elementwise_fma(n2, c.xy, a.xy), hi = __builtin_elementwise_fma(n2, c.zw, a.zw);
            return f32x4{lo.x, lo.y, hi.x, hi.y};
        };
        auto glu2 = [&](f32x2 gp, f32x2 up, float c, float k) INL {
            const f32x2 ea = gp * f32x2{c, c}, k2 = {k, k};
            const f32x2 d = __builtin_elementwise_fma(f32x2{__builtin_amdgcn_exp2f(ea.x), __builtin_amdgcn_exp2f(ea.y)}, k2, k2);
            const f32x2 hv = (gp * up) * f32x2{__builtin_amdgcn_rcpf(d.x), __builtin_amdgcn_rcpf(d.y)};
            return pk2(hv.x, hv.y);
        };
        auto glup = [&](f32x4 gp, f32x4 up, float c, float k) INL {
            return u32x2{glu2(gp.xy, up.xy, c, k), glu2(gp.zw, up.zw, c, k)};
        };
        u32x2 hp[2];
        auto blk = [&](int i, int j, auto pkc) INL {
            if constexpr (decltype(pkc)::value)
                hp[i & 1] = glup(centerp(acc[i][j], ccv[j], stv[i][0]), centerp(acc[i][j + 3], ccv[j + 3], stv[i][0]), stv[i][1], stv[i][2]);
            else
                hp[i & 1] = glu(center(acc[i][j], ccv[j], stv[i][0]), center(acc[i][j + 3], ccv[j + 3], stv[i][0]), stv[i][1], stv[i][2]);
        };
        auto blk2 = [&](int i, int j) INL {
            f32x2 gp[4], up[4], ea[4];
#pragma unroll
            for (int x = 0; x < 4; ++x) {
                const f32x4 a = acc[i + (x >> 1)][j], c = ccv[j];
                const float nm = stv[i + (x >> 1)][0];
                gp[x] = __builtin_elementwise_fma(f32x2{nm, nm}, x & 1 ? c.zw : c.xy, x & 1 ? a.zw : a.xy);
            }
            SCHED();
#pragma unroll
            for (int x = 0; x < 4; ++x) { const float c = stv[i + (x >> 1)][1]; ea[x] = gp[x] * f32x2{c, c}; }
            SCHED();
#pragma unroll
            for (int x = 0; x < 4; ++x) {
                const f32x4 a = acc[i + (x >> 1)][j + 3], c = ccv[j + 3];
                const float nm = stv[i + (x >> 1)][0];
                ea[x] = f32x2{__builtin_amdgcn_exp2f(ea[x].x), __builtin_amdgcn_exp2f(ea[x].y)};
                up[x] = __builtin_elementwise_fma(f32x2{nm, nm}, x & 1 ? c.zw : c.xy, x & 1 ? a.zw : a.xy);
                SCHED();
            }
#pragma unroll
            for (int x = 0; x < 4; ++x) { const float k = stv[i + (x >> 1)][2]; ea[x] = __builtin_elementwise_fma(ea[x], f32x2{k, k}, f32x2{k, k}); }
            SCHED();
#pragma unroll
            for (int x = 0; x < 4; ++x) {
                ea[x] = f32x2{__builtin_amdgcn_rcpf(ea[x].x), __builtin_amdgcn_rcpf(ea[x].y)};
                up[x] = gp[x] * up[x];
                SCHED();
            }
#pragma unroll
            for (int x = 0; x < 4; ++x) up[x] = up[x] * ea[x];
            SCHED();
#pragma unroll
            for (int x = 0; x < 4; ++x) hp[x & 1][x >> 1] = pk2(up[(x & 1) * 2 + (x >> 1)].x, up[(x & 1) * 2 + (x >> 1)].y);
        };
        auto flush = [&](int hf, int j) INL {
            const auto t0 = __builtin_amdgcn_permlane16_swap(hp[0][0], hp[1][0], false, false);
            const auto t1 = __builtin_amdgcn_permlane16_swap(hp[0][1], hp[1][1], false, false);
            store16_wt(C, gof + hf * 8 * (8 * F_) + j * 128, u32x4{t0[0], t1[0], t0[1], t1[1]});
            SCHED();
        };
        auto head = [&](auto pkc) INL { blk(0, 0, pkc); blk(1, 0, pkc); flush(0, 0); blk(0, 1, pkc); };
        auto sync = [&]() INL {
            WAIT_LGKM0();
            BARRIER();
            asm volatile("" ::: "memory");
            SCHED();
        };
        const bool late = wid >= 4;
        if (!late) head(CT<0>{});
        sync();
#pragma unroll
        for (int i = 0; i < MI; ++i)
#pragma unroll
            for (int j = 0; j < NI; ++j) asm volatile("" : "+v"(acc[i][j]));
#pragma unroll
        for (int i = 0; i < MI; i += 2) {
            const f32x4 x = centerp(acc[i][6], ccv[6], stv[i][0]), y = centerp(acc[i + 1][6], ccv[6], stv[i + 1][0]);
            f32x4 gm, um;
#pragma unroll
            for (int r = 0; r < 4; ++r) {
                const auto t = __builtin_amdgcn_permlane32_swap(__float_as_uint(x[r]), __float_as_uint(y[r]), false, false);
                gm[r] = __uint_as_float(t[0]); um[r] = __uint_as_float(t[1]);
            }
            const bool hi = (tide & 32) != 0;
            *(LDS_AS u32x2*)(size_t)(hsw + i * 512) =
                glup(gm, um, hi ? stv[i + 1][1] : stv[i][1], hi ? stv[i + 1][2] : stv[i][2]);
        }
        u32x4 mv;
        auto mixed = [&]() INL {
            wait_vm<2>();
            sync();
            mv = *(const LDS_AS u32x4*)(size_t)(hsr);
            SCHED();
        };
        if constexpr (MXP == 0) { mixed(); store16_wt(C, gofm, mv); }
        if (late) {
            __builtin_amdgcn_s_setprio(LPR);
            blk2(0, 0);
            flush(0, 0);
            blk2(0, 1);
        } else {
            blk(1, 1, CT<1>{});
        }
        flush(0, 1);
        if constexpr (MXP == 1) mixed();
        blk2(0, 2);
        if constexpr (MXP == 1) store16_wt(C, gofm, mv);
        flush(0, 2);
#pragma unroll
        for (int j = 0; j < 3; ++j) {
            blk2(2, j);
            flush(1, j);
        }
        __builtin_amdgcn_s_setprio(0);
        if (!late) {
            pubc<0>(gflags + (tm * 64 + tn) * 32, tok, lds0 + GCN);
            if (wid == 2) code_touch(SINK, tide & 63);
        }
    }
}

constexpr long long W_OB = 12, W_H = 16, W_XA = 32, W_XB = 36, W_CS = 40, W_SPA = 42, W_SPB = 43, W_FL = 44, W_EP = 45, W_SL = 48, W_L1 = 48;
extern "C" __global__ __launch_bounds__(512, 2) void body_k(const u16* __restrict__ x0, const u16* __restrict__ wqkv,
                                                 const u16* __restrict__ wo, const u16* __restrict__ wg,
                                                 const u16* __restrict__ wu, const u16* __restrict__ wd, u16* out,
                                                 char* ws) {
    extern __shared__ __attribute__((aligned(16))) char smem[];
    {
        const int tid = threadIdx.x, lane = tid & 63, wid = __builtin_amdgcn_readfirstlane(tid >> 6);
        if (wid >= 2 && wid < 6) {
            const int id = (blockIdx.x & 7) * 32 + (blockIdx.x >> 3);
            const int tm = (id / 128) * 8 + id % 8, tn = (id % 128) / 8, h = wid & 1;
            const int row = h * 8 + (lane >> 3);
            const unsigned sw = ((lane & 7) ^ ((row >> 1) & 7)) * 16;
            if (wid < 4) {
                qkv_half_a(x0, (unsigned)((tm * 128 + row) * D_ * 2) + sw, qsl(0) * QAS + h * 1024);
            } else {
                const unsigned lh = (unsigned)((tm ? tm * 128 - 16 + row : 0) * D_ * 2) + sw;
                glds16_mask(x0, lh, qsl(0) * QAS + 16384 + h * 1024, h == 0 ? ~0xffull : ~0ull);
                qkv_half_b(wqkv, b_off(h, lane, 3 * D_, tn * 64), QBR + qsl(0) * QBS + h * 1024);
                glds16_mask((const char*)x0 + 128, lh, qsl(1) * QAS + 16384 + h * 1024, h == 0 ? ~0xffull : ~0ull);
            }
        } else if (wid == 1) {
            code_touch(SINK, lane);
        }
    }
    u64 tok = 0;
#pragma unroll 1
    for (int L = 0; L < 2; ++L) {
        auto at = [&](long long mb, int l) INL { return sopq(ws) + ((mb + l * W_L1) << 20); };
        auto fl = [&](int ph) INL { return (unsigned*)at(W_FL, 0) + 16384 + ph * 8192; };
        qkv_phase(smem, L ? (u16*)at(W_XB, 0) : x0, sopq(wqkv), (u16*)at(W_OB, L), (float*)at(W_CS, 0), (float*)at(W_SPB, 0), fl(4),
                  fl(7 - L), sopq(wo), L == 0, sopq(wg), sopq(wu), (u64*)at(W_EP, 0), tok);
        const float* const cs = (float*)at(W_CS, 0);
        GuC gc;
        f32x4 cp;
        u64 lk;
        wo_phase(smem, (u16*)at(W_OB, L), sopq(wo), (u16*)at(W_XA, L), (float*)at(W_SPA, L), fl(7 - L),
                 L == 0, sopq(wg), sopq(wu), cs, CSO + (2 + L) * 32768, gc, cp, lk, tok);
        gu_phase(smem, (u16*)at(W_XA, L), sopq(wg), sopq(wu), (u16*)at(W_H, L), cs, (float*)at(W_SPA, L), sopq(wd),
                 fl(L), fl(8 + 2 * L), gc, cp, lk, tok);
        dn_phase(smem, (u16*)at(W_H, L), sopq(wd), L ? out : (u16*)at(W_XB, 0), L ? nullptr : (float*)at(W_SPB, 0),
                 (float*)at(W_SL, L), fl(8 + 2 * L), fl(4 + L), L ? nullptr : sopq(wqkv), tok);
    }
    asm volatile(".Lbody_end:");
}

extern "C" void kernel_launch(void* const* d_in, const int* in_sizes, int n_in, void* d_out, int out_size,
                              void* d_ws, size_t ws_size, hipStream_t stream) {
    const u16* x0 = (const u16*)d_in[0];
    const u16* wqkv = (const u16*)d_in[1];
    const u16* wo = (const u16*)d_in[2];
    const u16* wg = (const u16*)d_in[4];
    const u16* wu = (const u16*)d_in[5];
    const u16* wd = (const u16*)d_in[6];
    constexpr int smem = 163840;
    (void)hipFuncSetAttribute((const void*)body_k, hipFuncAttributeMaxDynamicSharedMemorySize, smem);
    body_k<<<256, 512, smem, stream>>>(x0, wqkv, wo, wg, wu, wd, (u16*)d_out, (char*)d_ws);
}
```

```cpp
#include <hip/hip_runtime.h>
#include <hip/hip_bf16.h>
#include <stdint.h>

#pragma clang diagnostic ignored "-Winline-asm"
typedef unsigned short u16;
using bf16x8 = __attribute__((ext_vector_type(8))) short;
using s16x4  = __attribute__((ext_vector_type(4))) short;
using f32x4  = __attribute__((ext_vector_type(4))) float;
using f32x2  = __attribute__((ext_vector_type(2))) float;
using u32x2  = __attribute__((ext_vector_type(2))) unsigned;
using u32x4  = __attribute__((ext_vector_type(4))) unsigned;
typedef __bf16 bf2_t __attribute__((ext_vector_type(2)));
#define LDS_AS __attribute__((address_space(3)))
#define INL __attribute__((always_inline))

constexpr int FWT = 4, XS1 = 0, XS2 = 2, XS3 = 4, XLK = 11, WNB = 4, WS0 = 2, WSD = 3, WNG = 3, GLT = 20, WLK = 2,
              WL2 = 0, DLK = 2, DL2 = 0, GPV = 2, TCU = -1, TCT = 25, STT = 22, STK = 10, MXP = 1, LPR = 1, SPU = 10,
              PRT = 20, WOLD = 12, WOBAR = 4, GL2 = 16, QL1 = 16, QG1 = 10, GGS = 6, PV0 = 6, HPRIO = 2, SLP = 1,
              KIN = 9, KSKIP = 2, NSOLO = 4, QPS = 0, DNE = 26, DPAR = 0, XO = 5, XA = 28, XB = 8, DBAR = 4, DBAR2 = 4,
              WT0 = 11, WT1 = 12, QBT = 12, QSO = 0, QSP = 2, WN4 = 2, WN1 = 3, WN2 = 2, WN3 = 3;
constexpr unsigned long long DSCH = 0x87654321a9000000ull;
constexpr int S_ = 2048, D_ = 1024, F_ = 3584;

__device__ __forceinline__ float bf2f(u16 v) { return __uint_as_float(((unsigned)v) << 16); }
__device__ __forceinline__ u16 f2bf(float f) {
    __hip_bfloat16 h = __float2bfloat16(f);
    return *reinterpret_cast<u16*>(&h);
}
__device__ __forceinline__ float rbf(float f) { return bf2f(f2bf(f)); }
__device__ __forceinline__ unsigned pk2(float a, float b) {
    const f32x2 v = {a, b};
    return __builtin_bit_cast(unsigned, __builtin_convertvector(v, bf2_t));
}
__device__ __forceinline__ float addf(float a, float b) {
    float r;
    asm("v_add_f32 %0, %1, %2" : "=v"(r) : "v"(a), "v"(b));
    return r;
}
__device__ __forceinline__ float mulf(float a, float b) {
    float r;
    asm("v_mul_f32 %0, %1, %2" : "=v"(r) : "v"(a), "v"(b));
    return r;
}
__device__ __forceinline__ float subf(float a, float b) {
    float r;
    asm("v_sub_f32 %0, %1, %2" : "=v"(r) : "v"(a), "v"(b));
    return r;
}

template <int V> struct CT { static constexpr int value = V; };
template <int I, int N, class F> __device__ __forceinline__ void static_for(F&& f) {
    if constexpr (I < N) { f(CT<I>{}); static_for<I + 1, N>(f); }
}

#define WAIT_LGKM0() asm volatile("s_waitcnt lgkmcnt(0)" ::: "memory")
#define BARRIER() do { asm volatile("" ::: "memory"); __builtin_amdgcn_s_barrier(); asm volatile("" ::: "memory"); } while (0)
#define SCHED() __builtin_amdgcn_sched_barrier(0)
template <int N> __device__ __forceinline__ void wait_vm() {
    static_assert(N >= 0 && N < 64, "vmcnt range");
    asm volatile("s_waitcnt vmcnt(%0)" :: "n"(N) : "memory");
}

__device__ __forceinline__ void glds16(const void* sbase, unsigned voff, unsigned lds_dst) {
    asm volatile("s_mov_b32 m0, %2\n\ts_nop 3\n\tglobal_load_lds_dwordx4 %0, %1" :: "v"(voff), "s"(sbase), "s"(__builtin_amdgcn_readfirstlane(lds_dst)) : "memory", "m0");
}
template <int NLANE>
__device__ __forceinline__ void glds16_part(const void* sbase, unsigned voff, unsigned lds_dst) {
    unsigned long long ex;
    asm volatile("s_mov_b64 %0, exec\n\ts_mov_b32 m0, %3\n\ts_mov_b64 exec, %4\n\ts_nop 1\n\t"
                 "global_load_lds_dwordx4 %1, %2 sc1\n\ts_mov_b64 exec, %0"
                 : "=&s"(ex) : "v"(voff), "s"(sbase), "s"(__builtin_amdgcn_readfirstlane(lds_dst)), "n"((1ull << NLANE) - 1) : "memory", "m0");
}
__device__ __forceinline__ void glds16_mask(const void* sbase, unsigned voff, unsigned lds_dst, unsigned long long mask) {
    unsigned long long ex;
    asm volatile("s_mov_b64 %0, exec\n\ts_mov_b32 m0, %3\n\ts_mov_b64 exec, %4\n\ts_nop 1\n\t"
                 "global_load_lds_dwordx4 %1, %2\n\ts_mov_b64 exec, %0"
                 : "=&s"(ex) : "v"(voff), "s"(sbase), "s"(__builtin_amdgcn_readfirstlane(lds_dst)), "s"(mask) : "memory", "m0");
}
__device__ __forceinline__ void glds16_dev(const void* sbase, unsigned voff, unsigned lds_dst) {
    asm volatile("s_mov_b32 m0, %2\n\ts_nop 3\n\tglobal_load_lds_dwordx4 %0, %1 sc1" :: "v"(voff), "s"(sbase), "s"(__builtin_amdgcn_readfirstlane(lds_dst)) : "memory", "m0");
}
template <int V> __device__ __forceinline__ void s_set(unsigned& d) { asm volatile("s_mov_b32 %0, %1" : "=s"(d) : "n"(V)); }
template <int V> __device__ __forceinline__ void m0_set(unsigned base) { asm volatile("s_add_i32 m0, %0, %1" :: "s"(base), "n"(V) : "m0", "scc"); }
template <bool DEV> __device__ __forceinline__ void blds16(__amdgpu_buffer_rsrc_t r, unsigned voff, unsigned soff) {
    if constexpr (DEV) asm volatile("buffer_load_dwordx4 %0, %1, %2 offen sc1 lds" :: "v"(voff), "s"(r), "s"(soff) : "memory");
    else asm volatile("buffer_load_dwordx4 %0, %1, %2 offen lds" :: "v"(voff), "s"(r), "s"(soff) : "memory");
}
__device__ __forceinline__ void gload16_dev(f32x4& d, const void* sbase, unsigned voff) {
    asm volatile("s_nop 4\n\tglobal_load_dwordx4 %0, %1, %2 sc1" : "=v"(d) : "v"(voff), "s"(sbase) : "memory");
}
__device__ __forceinline__ void gload8_dev(unsigned long long& d, const void* p) {
    asm volatile("global_load_dwordx2 %0, %1, off sc1" : "=v"(d) : "v"(p) : "memory");
}
template <int OFF, class T> __device__ __forceinline__ void gload16_devo(T& d, const void* sbase, unsigned voff) {
    asm volatile("global_load_dwordx4 %0, %1, %2 offset:%3 sc1" : "=v"(d) : "v"(voff), "s"(sbase), "n"(OFF) : "memory");
}
template <int OFF> __device__ __forceinline__ void gload16(f32x4& d, const void* sbase, unsigned voff) {
    asm volatile("s_nop 4\n\tglobal_load_dwordx4 %0, %1, %2 offset:%3" : "=v"(d) : "v"(voff), "s"(sbase), "n"(OFF) : "memory");
}
template <int G> __device__ __forceinline__ void gload4_if(f32x4& a, f32x4& b, f32x4& c, f32x4& d, unsigned& voff, const void* sbase, int st, int g) {
    asm volatile("s_cmp_lg_u32 %7, %8\n\ts_cbranch_scc1 .Lwb%=\n\ts_nop 2\n\t"
                 "global_load_dwordx4 %0, %4, %5\n\tv_add_u32 %4, %6, %4\n\tglobal_load_dwordx4 %1, %4, %5\n\tv_add_u32 %4, %6, %4\n\t"
                 "global_load_dwordx4 %2, %4, %5\n\tv_add_u32 %4, %6, %4\n\tglobal_load_dwordx4 %3, %4, %5\n\tv_add_u32 %4, %6, %4\n.Lwb%=:"
                 : "+v"(a), "+v"(b), "+v"(c), "+v"(d), "+v"(voff) : "s"(sbase), "s"(st), "s"(g), "n"(G) : "memory", "scc");
}
template <int N1, int N0, int BIT> __device__ __forceinline__ void wait_vm_if(unsigned m) {
    asm volatile("s_waitcnt vmcnt(%1)\n\ts_bitcmp1_b32 %0, %3\n\ts_cbranch_scc1 .Lwq%=\n\ts_waitcnt vmcnt(%2)\n.Lwq%=:"
                 :: "s"(m), "n"(N1), "n"(N0), "n"(BIT) : "memory", "scc");
}
constexpr unsigned SINK = 163584;
__device__ __forceinline__ void touch1(unsigned sink, const void* sbase, unsigned voff) {
    asm volatile("s_mov_b32 m0, %2\n\ts_nop 3\n\tglobal_load_lds_dword %0, %1" :: "v"(voff), "s"(sbase), "s"(sink) : "memory", "m0");
}
__device__ __forceinline__ const char* uni(const char* p) {
    const unsigned long long v = (unsigned long long)p;
    const unsigned lo = __builtin_amdgcn_readfirstlane((unsigned)v), hi = __builtin_amdgcn_readfirstlane((unsigned)(v >> 32));
    return (const char*)(((unsigned long long)hi << 32) | lo);
}
__device__ __forceinline__ void store16_wt(void* base, unsigned off, u32x4 v) {
    const __amdgpu_buffer_rsrc_t r = __builtin_amdgcn_make_buffer_rsrc(base, 0, 0x7fffffff, 0x00020000);
    __builtin_amdgcn_raw_buffer_store_b128(v, r, off, 0, 16);
}
__device__ __forceinline__ unsigned opaque(unsigned v) { asm volatile("" : "+v"(v)); return v; }
__device__ __forceinline__ unsigned bid_() { unsigned b = blockIdx.x; asm volatile("" : "+s"(b)); return b; }
__device__ __forceinline__ int wg_id() { const unsigned b = bid_(); return (b & 7) * 32 + (b >> 3); }
template <class T> __device__ __forceinline__ T* sopq(T* p) { asm volatile("" : "+s"(p)); return p; }
__device__ __forceinline__ void code_touch(unsigned sink, unsigned lane) {
    const char* pc;
    int off;
    unsigned len;
    asm volatile("s_getpc_b64 %0\n.Lct%=:\n\ts_mov_b32 %1, body_k-.Lct%=\n\ts_mov_b32 %2, .Lbody_end-body_k"
                 : "=s"(pc), "=s"(off), "=s"(len));
    const unsigned o = ((bid_() >> 3) + lane * 32u) * 128u;
    touch1(sink, pc + off, o < len ? o : 0u);
}
template <int OFF> __device__ __forceinline__ bf16x8 lds_a(unsigned base) {
    static_assert(OFF >= 0 && OFF < 65536, "ds offset");
    return *(const LDS_AS bf16x8*)(size_t)(base + OFF);
}
template <int OFF> __device__ __forceinline__ s16x4 lds_h(unsigned base) {
    static_assert(OFF >= 0 && OFF < 65536, "ds offset");
    return __builtin_amdgcn_ds_read_tr16_b64_v4i16((LDS_AS s16x4*)(size_t)(base + OFF));
}
__device__ __forceinline__ bf16x8 cat(s16x4 lo, s16x4 hi) { return __builtin_shufflevector(lo, hi, 0, 1, 2, 3, 4, 5, 6, 7); }
__device__ __forceinline__ f32x4 mma(bf16x8 a, bf16x8 b, f32x4 c) {
    return __builtin_amdgcn_mfma_f32_16x16x32_bf16(a, b, c, 0, 0, 0);
}

__device__ __forceinline__ void mma_ip(bf16x8 a, bf16x8 b, f32x4& c) {
    asm volatile("v_mfma_f32_16x16x32_bf16 %0, %1, %2, %0" : "+v"(c) : "v"(a), "v"(b));
}
__device__ __forceinline__ void mma_z(bf16x8 a, bf16x8 b, f32x4& c) {
    asm volatile("v_mfma_f32_16x16x32_bf16 %0, %1, %2, 0" : "=&v"(c) : "v"(a), "v"(b));
}

constexpr int CSTR = 64 * 2 + 16;
struct ResC { unsigned rd, co, so; };
__device__ __forceinline__ void res_setup(ResC& c, unsigned lds, int m0, int n0, int tid) {
    const int row = (tid >> 6) * 16 + ((tid >> 3) & 7), ch = tid & 7;
    c.rd = opaque(lds + row * CSTR + ch * 16);
    c.co = opaque((unsigned)((m0 + row) * D_ + n0 + ch * 8) * 2u);
    c.so = opaque((unsigned)(((m0 + row) >> 5) * 512 + (n0 >> 6) * 32 + (row & 31) + (ch >> 2) * 8) * 8u);
}
__device__ __forceinline__ void res_tail(const ResC& c, u16* C, float* sp) {
    const u32x4 v = *(const LDS_AS u32x4*)(size_t)c.rd, w = *(const LDS_AS u32x4*)(size_t)(c.rd + 8 * CSTR);
    const __amdgpu_buffer_rsrc_t rc = __builtin_amdgcn_make_buffer_rsrc(C, 0, 0x7fffffff, 0x00020000);
    __builtin_amdgcn_raw_buffer_store_b128(v, rc, c.co, 0, 16);
    __builtin_amdgcn_raw_buffer_store_b128(w, rc, c.co, 8 * D_ * 2, 16);
    SCHED();
    if (sp == nullptr) return;
    unsigned s0, q0, s1, q1;
    asm("v_dot2_f32_bf16 %0, %4, %12, 0\n\tv_dot2_f32_bf16 %1, %4, %4, 0\n\tv_dot2_f32_bf16 %0, %5, %12, %0\n\tv_dot2_f32_bf16 %1, %5, %5, %1\n\t"
        "v_dot2_f32_bf16 %0, %6, %12, %0\n\tv_dot2_f32_bf16 %1, %6, %6, %1\n\tv_dot2_f32_bf16 %0, %7, %12, %0\n\tv_dot2_f32_bf16 %1, %7, %7, %1\n\t"
        "v_dot2_f32_bf16 %2, %8, %12, 0\n\tv_dot2_f32_bf16 %3, %8, %8, 0\n\tv_dot2_f32_bf16 %2, %9, %12, %2\n\tv_dot2_f32_bf16 %3, %9, %9, %3\n\t"
        "v_dot2_f32_bf16 %2, %10, %12, %2\n\tv_dot2_f32_bf16 %3, %10, %10, %3\n\tv_dot2_f32_bf16 %2, %11, %12, %2\n\tv_dot2_f32_bf16 %3, %11, %11, %3\n\t"
        "v_add_f32_dpp %0, %0, %0 row_half_mirror row_mask:0xf bank_mask:0x5\n\tv_add_f32_dpp %1, %1, %1 row_half_mirror row_mask:0xf bank_mask:0x5\n\t"
        "v_add_f32_dpp %0, %2, %2 row_half_mirror row_mask:0xf bank_mask:0xa\n\tv_add_f32_dpp %1, %3, %3 row_half_mirror row_mask:0xf bank_mask:0xa\n\t"
        "s_nop 0\n\tv_add_f32_dpp %0, %0, %0 quad_perm:[1,0,3,2] row_mask:0xf bank_mask:0xf\n\t"
        "v_add_f32_dpp %1, %1, %1 quad_perm:[1,0,3,2] row_mask:0xf bank_mask:0xf\n\t"
        "s_nop 0\n\tv_add_f32_dpp %0, %0, %0 quad_perm:[2,3,0,1] row_mask:0xf bank_mask:0xf\n\t"
        "v_add_f32_dpp %1, %1, %1 quad_perm:[2,3,0,1] row_mask:0xf bank_mask:0xf"
        : "=&v"(s0), "=&v"(q0), "=&v"(s1), "=&v"(q1)
        : "v"(v[0]), "v"(v[1]), "v"(v[2]), "v"(v[3]), "v"(w[0]), "v"(w[1]), "v"(w[2]), "v"(w[3]), "s"(0x3f803f80u));
    const __amdgpu_buffer_rsrc_t r = __builtin_amdgcn_make_buffer_rsrc(sp, 0, 0x7fffffff, 0x00020000);
    __builtin_amdgcn_raw_buffer_store_b64(u32x2{s0, q0}, r, c.so, 0, 16);
}
__device__ __forceinline__ bf16x8 pick(bool on, int e) {
    const unsigned one = on ? 0x3f80u << (e & 1) * 16 : 0u;
    bf16x8 m = __builtin_bit_cast(bf16x8, u32x4{(e >> 1) == 0 ? one : 0u, (e >> 1) == 1 ? one : 0u, (e >> 1) == 2 ? one : 0u, (e >> 1) == 3 ? one : 0u});
    asm volatile("" : "+v"(m));
    return m;
}

constexpr int ATX = 155648;
__device__ __forceinline__ void att_put(char* smem, int lane, int m0) {
    const int l15 = lane & 15, g = lane >> 4;
    u32x4 cx, cy, c0, ps;
#pragma unroll
    for (int r = 0; r < 4; ++r) {
        const int e = l15 - g * 4 - r, eh = ((l15 - g * 4) & ~1) - (r & 1) * 2;
        cx[r] = (e & 1) == 0 && e < 0 ? 0u : 0xff800000u;
        cy[r] = (e & 1) == 0 && e >= 0 ? 0u : 0xff800000u;
        c0[r] = m0 ? cx[r] : 0xff800000u;
        const unsigned hs = r & 1 ? 0x0302u : 0x0100u;
        ps[r] = (eh < 0) == (r < 2) ? (l15 & 1 ? hs << 16 | 0x0c0cu : 0x0c0c0000u | hs) : 0x0c0c0c0cu;
    }
    u32x4* const p = (u32x4*)(smem + ATX) + lane;
    p[0] = cx; p[64] = cy; p[128] = c0; p[192] = ps;
    *(unsigned*)(smem + 57088 + (lane & 1) * 4) = 0u;
}
using u32x8 = __attribute__((ext_vector_type(8))) unsigned;
#define AMM "v_mfma_f32_16x16x32_bf16 "
#define ACV "v_cvt_pk_bf16_f32 "
#define AX1(s) "v_max3_f32 v[" s "], v[" s "], v[" s "+1], v[" s "+4]\n\t"
#define AX2(s) "v_max3_f32 v[" s "+2], v[" s "+2], v[" s "+3], v[" s "+6]\n\t"
#define AX3(s) "v_max_f32 v[" s "], v[" s "], v[" s "+5]\n\t"
#define AX4(s) "v_max_f32 v[" s "+2], v[" s "+2], v[" s "+7]\n\t"
#define AOP(op, d, s) op " v" d ", v[" s "], v[" s "+2]\n\t"
#define AO2(op, d, e, a, b) op " v" d ", v" a ", v" b "\n\t" op " v" e ", v" a ", v" b "\n\t"
#define ASB(s, n) "v_sub_f32 v[" s "], v[" s "], v" n "\n\tv_sub_f32 v[" s "+2], v[" s "+2], v" n "\n\t"
#define AEX(s) "v_exp_f32 v[" s "], v[" s "]\n\t"
#define AMU(s, n) "v_mul_f32 v[" s "], v[" s "], v" n "\n\t"
#define APK(s) ACV "v[" s "+1], v[" s "], v[" s "+2]\n\t"
#define APM(s, i) "v_perm_b32 v[" s "+4+" i "], v[" s "+1], v[" s "+1], v[240+" i "]\n\t"
#define APV(o, v, s) AMM "v[" o ":" o "+3], v[" v ":" v "+3], v[" s "+4:" s "+7], 0\n\t"
#define AOC(s, o) ACV "v[" s "], v[" o "], v[" o "+1]\n\t" ACV "v[" s "+1], v[" o "+2], v[" o "+3]\n\t"
#define AST(s, off) "buffer_store_dwordx4 v[" s ":" s "+3], %[oo], %[rc], 0 offen offset:" off " sc1\n\t"
#define S16(a, b) "v_permlane16_swap_b32 v" a ", v" b "\n\t"
#define S32(a, b) "v_permlane32_swap_b32 v" a ", v" b "\n\t"
__device__ __forceinline__ void att_run(const u32x4 (&q)[4], const u32x4 (&k)[4], const f32x4 (&v)[3][2], const u32x2 (&v3)[2],
                                        const u32x4 (&am)[4], const f32x2 (&rk)[4], const f32x2 (&sc)[4], unsigned xa, unsigned oo, u16* C) {
    u32x4 kx;
    u32x8 s3, s2, w0, w1;
    asm volatile("ds_read_b128 %2, %3\n\tds_read_b64 v[164:165], %3 offset:1024\n\t"
                 AMM "v[228:231], %4, %6, %8\n\t" ACV "v166, %9, %10\n\t" ACV "v167, %11, %12\n\t" ACV "v168, %13, %14\n\t"
                 AMM "v[224:227], %5, %6, %7\n\t" ACV "v169, %15, %16\n\t" ACV "v170, %17, %18\n\t" ACV "v171, %19, %20"
                 : "=&{v[224:231]}"(s3), "=&{v[164:171]}"(w0), "=&v"(kx)
                 : "v"(xa), "v"(k[3]), "v"(k[2]), "v"(q[3]), "v"(am[0]), "v"(am[1]),
                   "v"(v[0][0][0]), "v"(v[0][0][1]), "v"(v[0][0][2]), "v"(v[0][0][3]), "v"(v[1][0][0]), "v"(v[1][0][1]),
                   "v"(v[1][0][2]), "v"(v[1][0][3]), "v"(v[2][0][0]), "v"(v[2][0][1]), "v"(v[2][0][2]), "v"(v[2][0][3]) : "memory");
    asm volatile("ds_read_b64 v[174:175], %2 offset:1032\n\t"
                 AMM "v[220:223], %3, %5, %7\n\t" ACV "v176, %8, %9\n\t" ACV "v177, %10, %11\n\t" ACV "v178, %12, %13\n\t"
                 AMM "v[216:219], %4, %5, %6\n\t" ACV "v179, %14, %15\n\t" ACV "v180, %16, %17\n\t" ACV "v181, %18, %19"
                 : "=&{v[216:223]}"(s2), "=&{v[174:181]}"(w1)
                 : "v"(xa), "v"(k[2]), "v"(k[1]), "v"(q[2]), "v"(am[0]), "v"(am[1]),
                   "v"(v[0][1][0]), "v"(v[0][1][1]), "v"(v[0][1][2]), "v"(v[0][1][3]), "v"(v[1][1][0]), "v"(v[1][1][1]),
                   "v"(v[1][1][2]), "v"(v[1][1][3]), "v"(v[2][1][0]), "v"(v[2][1][1]), "v"(v[2][1][2]), "v"(v[2][1][3]) : "memory");
    const __amdgpu_buffer_rsrc_t rc = __builtin_amdgcn_make_buffer_rsrc(C, 0, 0x7fffffff, 0x00020000);
    asm volatile(AMM "v[212:215], %[k1], %[q1], %[cy]\n\t" AX1("224") AX2("224") AX3("224")
                 AMM "v[208:211], %[k0], %[q1], %[cx]\n\t" AX4("224") AMU("224", "195") AMU("226", "199")
                 AMM "v[204:207], %[k0], %[q0], %[cy]\n\t" AOP("v_max_f32", "232", "224") AX1("216") AX2("216") "s_waitcnt lgkmcnt(0)\n\t"
                 AMM "v[200:203], %[kx], %[q0], %[c0]\n\t" AX3("216") AX4("216") AMU("216", "194") AMU("218", "198")
                 AOP("v_max_f32", "233", "216") AX1("208") AX2("208")
                 S16("232", "233") AX3("208") AO2("v_max_f32", "236", "237", "232", "233") AX4("208") AMU("208", "193")
                 S32("236", "237") AMU("210", "197") AO2("v_max_f32", "232", "233", "236", "237") AOP("v_max_f32", "234", "208") AX1("200")
                 S16("232", "233") AX2("200") AX3("200") AX4("200") ASB("224", "232") AMU("200", "192") AMU("202", "196")
                 AOP("v_max_f32", "235", "200") ASB("216", "233")
                 S16("234", "235") AEX("224") AEX("226") AO2("v_max_f32", "238", "239", "234", "235") AEX("216") AEX("218")
                 S32("238", "239") AOP("v_add_f32", "232", "224") AO2("v_max_f32", "234", "235", "238", "239")
                 AOP("v_add_f32", "233", "216") AMU("224", "187")
                 S16("234", "235") AMU("226", "191")
                 S16("232", "233") ASB("208", "234") AO2("v_add_f32", "236", "237", "232", "233") ASB("200", "235")
                 S32("236", "237") AEX("208") AEX("210") "v_add_f32 v232, v236, v237\n\t" AEX("200") "v_rcp_f32 v232, v232\n\t" AEX("202")
                 "v_mov_b32 v233, v232\n\t" AOP("v_add_f32", "234", "208") AOP("v_add_f32", "235", "200")
                 S16("232", "233") AMU("224", "232")
                 S16("234", "235") AMU("226", "232") AMU("216", "186") AO2("v_add_f32", "238", "239", "234", "235") AMU("218", "190") APK("224")
                 S32("238", "239") AMU("216", "233") "v_add_f32 v234, v238, v239\n\t" AMU("218", "233") "v_rcp_f32 v234, v234\n\t"
                 APM("224", "0") "v_mov_b32 v235, v234\n\t" APM("224", "1") APM("224", "2")
                 S16("234", "235") APM("224", "3") APK("216") AMU("208", "185")
                 APV("132", "170", "224") APM("216", "0") APM("216", "1") APM("216", "2")
                 APV("136", "180", "224") APM("216", "3") AMU("210", "189") AMU("208", "234")
                 APV("140", "168", "216") AMU("210", "234") AMU("200", "184") APK("208")
                 APV("144", "178", "216") APM("208", "0") APM("208", "1") APM("208", "2") APM("208", "3") AMU("202", "188") AMU("200", "235")
                 APV("148", "166", "208") AMU("202", "235") AOC("224", "132")
                 APV("152", "176", "208") APK("200") AOC("226", "136") AST("224", "3072")
                 APM("200", "0") APM("200", "1") APM("200", "2") APM("200", "3") AOC("216", "140")
                 APV("156", "164", "200") AOC("218", "144") AST("216", "2048")
                 APV("160", "174", "200") AOC("208", "148") AOC("210", "152") AST("208", "1024")
                 AOC("200", "156") "s_nop 0\n\t" AOC("202", "160") AST("200", "0") "s_nop 1"
                 : "+{v[224:231]}"(s3), "+{v[216:223]}"(s2)
                 : "{v[164:171]}"(w0), "{v[174:181]}"(w1), "{v[172:173]}"(v3[0]), "{v[182:183]}"(v3[1]),
                   [k1] "v"(k[1]), [k0] "v"(k[0]), [kx] "v"(kx), [q1] "v"(q[1]), [q0] "v"(q[0]),
                   [cx] "v"(am[0]), [cy] "v"(am[1]), [c0] "v"(am[2]), "{v[240:243]}"(am[3]),
                   "{v[184:185]}"(rk[0]), "{v[186:187]}"(rk[1]), "{v[188:189]}"(rk[2]), "{v[190:191]}"(rk[3]),
                   "{v[192:193]}"(sc[0]), "{v[194:195]}"(sc[1]), "{v[196:197]}"(sc[2]), "{v[198:199]}"(sc[3]), [oo] "v"(oo), [rc] "s"(rc)
                 : "memory", "v132", "v133", "v134", "v135", "v136", "v137", "v138", "v139", "v140", "v141", "v142", "v143", "v144", "v145",
                   "v146", "v147", "v148", "v149", "v150", "v151", "v152", "v153", "v154", "v155", "v156", "v157", "v158", "v159", "v160",
                   "v161", "v162", "v163", "v200", "v201", "v202", "v203", "v204", "v205", "v206", "v207", "v208", "v209", "v210",
                   "v211", "v212", "v213", "v214", "v215", "v232", "v233", "v234", "v235", "v236", "v237", "v238", "v239");
}

typedef unsigned long long u64;
typedef __attribute__((address_space(1))) u64 gu64;
template <class F> __device__ __forceinline__ void publish(unsigned* f, u64 tok, int wid, unsigned sink, F&& then) {
    asm volatile("s_cmp_eq_u32 %0, 0\n\ts_cselect_b64 exec, 1, 0\n\ts_waitcnt vmcnt(0)\n\ts_barrier\n\t"
                 "global_store_dwordx2 %1, %2, %3 sc1\n\ts_mov_b64 exec, -1" :: "s"(wid), "v"(0u), "v"(tok), "s"(f) : "memory", "scc");
    then();
    if (wid == 2) {
        code_touch(sink, opaque(threadIdx.x) & 63u);
        wait_vm<0>();
    }
}
template <int N, int W = 4> __device__ __forceinline__ void pubc(unsigned* f, u64 tok, unsigned cnt) {
    unsigned c = 1u;
    asm volatile("s_mov_b64 exec, 1\n\ts_waitcnt vmcnt(%5)\n\tds_add_rtn_u32 %0, %4, %0\n\ts_waitcnt lgkmcnt(0)\n\t"
                 "v_cmp_eq_u32 vcc, %6, %0\n\ts_cbranch_vccz 1f\n\tglobal_store_dwordx2 %1, %2, %3 sc1\n\ts_waitcnt vmcnt(1)\n\t"
                 "s_branch 2f\n1:\n\ts_waitcnt vmcnt(0)\n2:\n\ts_mov_b64 exec, -1"
                 : "+v"(c) : "v"(0u), "v"(tok), "s"(f), "v"(cnt), "n"(N), "n"(W - 1) : "memory", "vcc");
}
__device__ __forceinline__ void poll(u64& have, u64 need, u64 at, const unsigned* f, u64 tok) {
    if (__builtin_expect((have & need) != need, 0)) {
        u64 v, t;
        unsigned n;
        asm volatile("s_andn2_b64 exec, %5, %0\n\ts_mov_b32 %3, 0x400000\n"
                     "1:\n\tglobal_load_dwordx2 %1, %6, off sc1\n\ts_sub_u32 %3, %3, 1\n\ts_cbranch_scc1 2f\n\ts_waitcnt vmcnt(0)\n\t"
                     "v_cmp_eq_u64 vcc, %7, %1\n\ts_or_b64 %0, %0, vcc\n\ts_andn2_b64 %2, %4, %0\n\ts_cbranch_scc1 1b\n"
                     "2:\n\ts_mov_b64 exec, -1"
                     : "+s"(have), "=&v"(v), "=&s"(t), "=&s"(n) : "s"(need), "s"(at), "v"(f), "s"(tok) : "memory", "vcc", "scc");
    }
}
__device__ __forceinline__ void look(u64& v, u64 at, const unsigned* f) {
    asm volatile("s_mov_b64 exec, %1\n\tglobal_load_dwordx2 %0, %2, off sc1\n\ts_mov_b64 exec, -1" : "=v"(v) : "s"(at), "v"(f) : "memory");
}
__device__ __forceinline__ u64 seen(u64& v, u64 at, u64 tok) {
    asm volatile("" : "+v"(v));
    return __ballot(v == tok) & at;
}
__device__ __forceinline__ void spin(u64& v, const void* base, unsigned off, u64 tok) {
    asm volatile("" : "+v"(v));
    if (__builtin_expect(__ballot(v != tok) != 0, 0)) {
        unsigned n;
        asm volatile("s_mov_b32 %1, 0x400000\n"
                     "1:\n\tglobal_load_dwordx2 %0, %2, %3 sc1\n\ts_sub_u32 %1, %1, 1\n\ts_waitcnt vmcnt(0)\n\ts_cbranch_scc1 2f\n\t"
                     "v_cmp_ne_u64 vcc, %4, %0\n\ts_cbranch_vccnz 1b\n2:"
                     : "=&v"(v), "=&s"(n) : "v"(off), "s"(base), "s"(tok) : "memory", "vcc", "scc");
    }
}
template <class M> __device__ __forceinline__ void await(M& have, M need, const unsigned* f, u64 tok) {
    if (__builtin_expect((have & need) != need, 0)) {
#pragma unroll 1
        for (unsigned spins = 0; spins < (1u << 22); ++spins) {
            have = (M)__ballot(__hip_atomic_load((gu64*)f, __ATOMIC_RELAXED, __HIP_MEMORY_SCOPE_AGENT) == tok);
            if ((have & need) == need) break;
            __builtin_amdgcn_s_sleep(1);
        }
        asm volatile("" ::: "memory");
    }
}
__device__ __forceinline__ unsigned gu_bsrc(int p, unsigned lane, int tn) {
    const unsigned rem = lane + (p < 14 ? p : p - 14) * 64, rowp = __umul24(rem, 4682u) >> 16;
    return __umul24(rowp ^ (rowp >> 1 & 4u), F_ * 2) + (rem * 16u + tn * 224u) - __umul24(rowp, 224u);
}
template <bool PERM = false>
__device__ __forceinline__ unsigned b_off(int h, int lane, int ld, int col) {
    const int row = h * 8 + (lane >> 3), cp = lane & 7;
    const int jj = (cp >> 1) ^ (((row >> 1) & 1) | ((row >> 2) & 2));
    return (unsigned)((PERM ? (lane >> 5) * 16 + h * 4 + (row & 3) : row) * ld + col + jj * 16 + (cp & 1) * 8) * 2u;
}

constexpr int DNO = F_ / 64 - NSOLO - DNE;
constexpr int dn_first(int n) { return (2 - n) & 3; }
static_assert(NSOLO == 4 && DNE < 28 && DNO < 28 && ((dn_first(DNE) ^ dn_first(DNO)) & 1) == 0, "k-tile program");
constexpr int DPX = DPAR == 0 ? 4 : (DPAR == 1 ? 1 : (DPAR == 2 ? 5 : (DPAR == 3 ? 7 : 3)));
__device__ __forceinline__ int dn_grp(int x) {
    return DPAR == 0 ? x & 3 : (DPAR == 1 ? x >> 1 : (DPAR == 4 ? (x >> 2) * 2 + ((x ^ x >> 1) & 1) : (x & 3) ^ (x >> 2) * (DPX & 3)));
}
constexpr int DNA = 65536, DNR = 98304;

constexpr int GBR = 3 * 32768, GBS = 224 * 128, GCS = GBR + 2 * GBS, GST = GCS + 224 * 4, GMX = DNR + 16384, GCN = 161024;
constexpr unsigned CSO = 4u << 20;
struct GuC { unsigned aoff[4], boff[4], tof, ab[2][2], bb[2][2], sto, cca, cc6, sta, gof, gofm, hsw, hsr, fo, cw, f1, f2; };
constexpr int gu_k(int i) { return i == 3 ? 4 : (i == 4 ? 3 : i); }
__device__ __forceinline__ void gu_setup(GuC& c, unsigned lds0, int wid, int id, bool first, unsigned fl) {
    const int tid = (int)opaque(threadIdx.x), lane = tid & 63;
    const int wm = wid >> 1, wn = wid & 1, tm = id & 7, tn = id >> 3, m0 = tm * 256;
    const int l15 = lane & 15, g = lane >> 4, q = l15 >> 2, p4 = l15 & 3;
#pragma unroll
    for (int i = 0; i < 4; ++i)
        c.aoff[i] = opaque((unsigned)((m0 + i * 64 + (tid >> 3)) * D_ + ((tid & 7) ^ ((tid >> 4) & 7)) * 8) * 2u);
    const int dk = DPAR ? (id >> 5) & 1 : id >> 7;
    if (wid < 4) {
        const unsigned j = (id & 31) * 256 + tid;
        unsigned rw = j >> 2;
        rw += rw < 1792 ? 0 : (dk ? DNE * 64 : DNO * 64);
        rw = (rw < F_ ? rw : 0) + (id >> 6) * 896;
        rw = rw < F_ ? rw : rw - F_;
        c.tof = opaque(rw * 2048 + 256 + (j & 1) * 128 + (j & 2) * 512);
    } else {
        c.tof = opaque((id * 224 + (tid < 480 ? tid - 256 : 0u)) * (first ? 128u : 0u));
    }
#pragma unroll
    for (int kk = 0; kk < 2; ++kk) {
        const unsigned sw = ((kk * 4 + g) ^ (l15 >> 1)) * 16;
        c.ab[kk][0] = opaque(lds0 + (wm * 64 + l15) * 128 + sw);
        c.ab[kk][1] = opaque(c.ab[kk][0] + 65536);
        const unsigned r = lds0 + GBR + ((g * 8 + kk * 4 + q) ^ (4 * (g & 1))) * 224;
        c.bb[kk][0] = opaque(r + wn * 128 + p4 * 8);
        c.bb[kk][1] = opaque(r + 96 + wn * 16 + (p4 & 1) * 8 + (p4 >> 1) * (GBS / 2));
    }
    c.sto = opaque(wid * 4096 + g * 1024 + l15 * 16);
    c.cca = opaque(lds0 + GCS + (wn * 64 + g * 4) * 4);
    c.cc6 = opaque(lds0 + GCS + (48 + wn * 8 + (g & 1) * 4 + (g >> 1) * 112) * 4);
    c.sta = opaque(lds0 + GST + wm * 1024 + (l15 & 1) * 256 + (l15 >> 1) * 16);
    c.gof = opaque(((m0 + wm * 64) / 4 + (g & 1) * 4 + q) * (8 * F_) + (tn * 7 + wn * 4) * 128 + p4 * 32 + (g >> 1) * 16);
    c.gofm = opaque(((m0 + wm * 64 + wn * 32) / 4 + (lane >> 3)) * (8 * F_) + (tn * 7 + 3) * 128 + (lane & 7) * 16);
    c.hsw = opaque(lds0 + GMX + (wm * 64 + (g >> 1) * 16 + l15) * 32 + wn * 16 + (g & 1) * 8);
    c.hsr = opaque(lds0 + GMX + wid * 1024 + lane * 16);
    const int pid = id ^ (DPX * 32), own = ((id / 64) * 4 + id % 4) * 16 + (id % 64) / 4, set = (first ? 7 : 5) * 256 + (own & ~15);
    const int w1 = (tm * 2 + wn) * 16 + wm + (wm == 3), j = wid * 4 + (lane & 3);
    c.f1 = opaque(fl + (w1 == own ? set : w1) * 128u);
    const int w2 = j < 24 ? (tm * 2 + (j & 1)) * 16 + 4 + j / 2 - (j < 2)
                          : (j & 1 ? ((pid / 64) * 4 + pid % 4) * 16 + (pid % 64) / 4 : (dn_grp(id >> 5) * 4 + (id & 3)) * 16 + ((id >> 2) & 7) * 2 + dk);
    c.f2 = opaque(fl + w2 * 128u);
    const int col = (wid < 7 ? wid : 6) * 32 + (lane & 7) * 4;
    c.fo = opaque((unsigned)((lane >> 3) * 2 * F_ + (col < 112 ? col : F_ + col - 112) + tn * 112) * 4u);
    c.cw = opaque(lds0 + GCS + (col + g) * 4);
}

constexpr int WOB = 65536, WONB = 12, QCN = 57088;
constexpr unsigned BOXO = 36u << 20;
__device__ __forceinline__ int wo_k(int i) { return (int)(0xb3fedca987654210ull >> (4 * i)) & 15; }
__device__ __forceinline__ unsigned wo_boff(int h, int lane) {
    return b_off<true>(h, lane, D_, (((wg_id()) % 64) / 4) * 64);
}
__device__ __forceinline__ void wo_stage_b(unsigned lds0, const u16* B, unsigned boff, int h, int step) {
#pragma unroll
    for (int i = 0; i < 2; ++i)
        glds16(uni((const char*)B + (wo_k(step) * 64 + i * 32) * D_ * 2), boff,
               lds0 + WOB + (step < WONB ? step : step - WONB) * 8192 + h * 1024 + i * 4096);
}
__device__ __forceinline__ void wo_phase(char* smem, const u16* A, const u16* __restrict__ B, u16* C, float* sp,
                                         unsigned* pflags, bool first, const u16* __restrict__ wg,
                                         const u16* __restrict__ wu, const float* cs, unsigned fl, GuC& gc, f32x4& cp,
                                         u64& lk, u64 tok) {
    constexpr int NK = 16, ASZ = 16384;
    const int tid = (int)opaque(threadIdx.x), lane = tid & 63;
    const int wid = __builtin_amdgcn_readfirstlane(tid >> 6);
    const int id = wg_id();
    const int tm = (id / 64) * 4 + id % 4, tn = (id % 64) / 4;
    const int m0 = tm * 128, n0 = tn * 64;
    const unsigned lds0 = __builtin_amdgcn_readfirstlane((unsigned)(size_t)(LDS_AS char*)smem);
    ResC rc;
    unsigned goff[7] = {0u, 0u, 0u, 0u, 0u, 0u, 0u};
    auto stage_g = [&](int i, int kt) INL {
        const int p = wid - 4 + 4 * i;
        glds16(uni((const char*)(p >= 14 ? wu : wg) + kt * 64 * F_ * 2), goff[i], lds0 + 98304 + kt * 28672 + p * 1024);
    };
    if (wid >= 4) {
        const int h = wid - 4;
        const unsigned aoff = (unsigned)(m0 * D_ * 2 + (h & 1) * 4096 + (h >> 1) * 1024 + lane * 16);
        const unsigned boff = wo_boff(h, lane);
#pragma unroll
        for (int i = 0; i < 7; ++i) goff[i] = opaque(gu_bsrc(h + 4 * i, opaque(lane), id >> 3));
        res_setup(rc, lds0 + ASZ, m0, n0, tid);
        gu_setup(gc, lds0, wid, id, first, fl);
        const int l = lane & 15, cb = (id >> 3) * 7 / 4 + l % 3;
        const int job = ((cb < 56 ? cb : 55) + (l >= 6 ? 56 : 0)) * 2 + (l / 3 & 1), oid = job + 8 * ((job + 32) / 56);
        const unsigned* const f = pflags + (lane < 16 ? tm * 16 + l : ((oid / 128) * 8 + oid % 8) * 16 + (oid % 128) / 8) * 32;
        const u64 all = first && h == 0 ? 0x0fffffffu : 0xffffu;
        u64 have = 0, at = WLK == 2 ? 3 : 0x17, lk2;
        auto stage = [&](int st, auto bc) INL {
            poll(have, 1u << wo_k(st), at, f, tok);
#pragma unroll
            for (int i = 0; i < 4; ++i)
                glds16_dev(uni((const char*)A + wo_k(st) * 8192 + (i >> 1) * 64 * D_ * 2 + (i & 1) * 2048), aoff,
                           lds0 + ((st + 1) & 3) * ASZ + h * 1024 + i * 4096);
            if constexpr (decltype(bc)::value) wo_stage_b(lds0, B, boff, h, st);
        };
        stage(0, CT<0>{});
        if constexpr (WL2 == 0) look(lk2, all, f);
        stage(1, CT<0>{});
        wait_vm<4>();
        BARRIER();
        if constexpr (WL2 == 0) { at = all; have |= seen(lk2, at, tok); }
        if constexpr (WL2 == 1) look(lk2, all, f);
        stage(2, CT<0>{});
        stage(3, CT<0>{});
        if constexpr (WL2 == 1) { wait_vm<8>(); at = all; have |= seen(lk2, at, tok); }
#pragma unroll 1
        for (int st = 4; st < WONB; ++st) {
            wait_vm<8>();
            BARRIER();
            stage(st, CT<0>{});
        }
#pragma unroll 1
        for (int st = WONB; st < NK; ++st) {
            wait_vm<8>();
            BARRIER();
            stage(st, CT<1>{});
        }
        poll(have, all, at, f, tok);
        wait_vm<12>();
        BARRIER();
        stage_g(0, 0); stage_g(1, 0); stage_g(2, 0);
        wait_vm<9>();
        BARRIER();
        stage_g(3, 0); stage_g(4, 0);
        wait_vm<5>();
        BARRIER();
        stage_g(5, 0); stage_g(6, 0);
        if (h == 3) code_touch(SINK, lane);
#pragma unroll
        for (int i = 0; i < 4; ++i) gc.boff[i] = goff[i < 3 ? 2 * i + 1 : 0];

    } else {
        const int wm = wid >> 1, wn = wid & 1;
        const int l15 = lane & 15, g = lane >> 4, q = l15 >> 2, p4 = l15 & 3;
        const unsigned ab = opaque(lds0 + wm * 8192 + lane * 16);
        unsigned bb[4];
#pragma unroll
        for (int x = 0; x < 4; ++x)
            bb[x] = opaque(lds0 + WOB + (g * 8 + q + ((x & 1) ^ wm) * 4) * 128 + p4 * 8 + (((wn * 2 + x / 2) ^ ((q >> 1) | ((g & 1) << 1))) << 5));
        f32x4 acc[4][2];
        {
            const unsigned ra = lds0 + (wm * 64 + l15) * 128 + (((wn * 4 + g) ^ (l15 >> 1)) * 16);
            const bf16x8 sel[2] = {pick(g == (l15 >> 3), l15 & 7), pick(g == 2 + (l15 >> 3), l15 & 7)};
            static_for<0, 4>([&](auto ic) INL {
                constexpr int i = decltype(ic)::value;
                const bf16x8 rr = lds_a<i * 2048>(ra);
#pragma unroll
                for (int j = 0; j < 2; ++j) {
                    acc[i][j] = mma(sel[j], rr, f32x4{0.f, 0.f, 0.f, 0.f});
                    asm volatile("" : "+v"(acc[i][j]));
                }
            });
        }
        res_setup(rc, lds0 + ASZ, m0, n0, tid);
        const unsigned cw = opaque(lds0 + ASZ + (wm * 64 + l15) * CSTR + (wn * 32 + g * 4) * 2);
        SCHED();
        pubc<0>(pflags + (((id / 128) * 8 + id % 8) * 16 + (id % 128) / 8) * 32, tok, lds0 + QCN);
        gu_setup(gc, lds0, wid, id, first, fl);
#pragma unroll
        for (int i = 0; i < 4; ++i) gc.boff[i] = opaque(gu_bsrc(wid + 8 * i, lane, id >> 3));
        auto ldop = [&](bf16x8 (&af)[8], s16x4 (&bf)[8], auto ktc, auto oc) INL {
            constexpr int KT = decltype(ktc)::value, O = decltype(oc)::value;
            if constexpr (O == 2 || (O >= 5 && O < 12)) {
                constexpr int X = O == 2 ? 0 : O - 4;
                af[(X & 1) * 4 + X / 2] = lds_a<((KT + 1) % 4) * ASZ + X * 1024>(ab);
            } else {
                constexpr int X = O < 2 ? O : (O < 5 ? O - 1 : O - 8), J = X / 4;
                bf[X] = lds_h<(KT % 4) * 8192 + ((X >> 1) & 1) * 4096>(bb[J * 2 + (X & 1)]);
            }
        };
        auto ktile = [&](bf16x8 (&af)[8], s16x4 (&bf)[8], bf16x8 (&an)[8], s16x4 (&bn)[8], auto nktc) INL {
            static_for<0, 16>([&](auto tc) INL {
                constexpr int T = decltype(tc)::value, j = T / 8, i = j ? 3 - (T / 2) % 4 : (T / 2) % 4, kk = T & 1;
                if constexpr (T == WOBAR && decltype(nktc)::value < NK) {
                    WAIT_LGKM0();
                    BARRIER();
                }
                mma_ip(cat(bf[j * 4 + kk * 2], bf[j * 4 + kk * 2 + 1]), af[kk * 4 + i], acc[i][j]);
                SCHED();
                if constexpr (decltype(nktc)::value < NK && T >= WOBAR) {
                    constexpr int U = T - WOBAR, lo_ = (U * 16 + WOLD - 1) / WOLD, hi_ = ((U + 1) * 16 + WOLD - 1) / WOLD;
                    static_for<lo_, (hi_ < 16 ? hi_ : 16)>([&](auto oc) INL { ldop(an, bn, nktc, oc); });
                }
                SCHED();
            });
        };
        bf16x8 a0[8], a1[8];
        s16x4 b0[8], b1[8];
        BARRIER();
        static_for<0, 16>([&](auto oc) INL { ldop(a0, b0, CT<0>{}, oc); });
#pragma unroll 1
        for (int it = 0; it < 3; ++it) {
            ktile(a0, b0, a1, b1, CT<1>{});
            ktile(a1, b1, a0, b0, CT<2>{});
            ktile(a0, b0, a1, b1, CT<3>{});
            const unsigned d = it == 2 ? -65536 : 32768;
            bb[0] += d; bb[1] += d; bb[2] += d; bb[3] += d;
            ktile(a1, b1, a0, b0, CT<4>{});
        }
        ktile(a0, b0, a1, b1, CT<13>{});
        ktile(a1, b1, a0, b0, CT<14>{});
        ktile(a0, b0, a1, b1, CT<15>{});
        ktile(a1, b1, a0, b0, CT<16>{});
#pragma unroll
        for (int t = 0; t < 8; ++t) {
            const int j = t / 4, i = j ? 7 - t : t;
            *(LDS_AS u32x2*)(size_t)(cw + i * 16 * CSTR + j * 32) = u32x2{pk2(acc[i][j][0], acc[i][j][1]), pk2(acc[i][j][2], acc[i][j][3])};
            SCHED();
        }
    }
    WAIT_LGKM0();
    BARRIER();
    res_tail(rc, C, sp);
    asm volatile("global_load_dwordx2 %0, %2, %4 sc1\n\tglobal_load_dwordx4 %1, %3, %4 sc1"
                 : "=&{v[194:195]}"(lk), "=&{v[196:199]}"(cp) : "v"(gc.f1), "v"(gc.fo), "s"(cs) : "memory");
}

constexpr int QAS = 144 * 128, QBS = 192 * 128, QBR = 57344;
constexpr int QSK = 3 * QAS, QCS = 159744, QXC = 57344;
#define GL_STEP(V) "global_load_lds_dwordx4 %0, %1\n\tv_add_u32 %0, " V ", %0\n\ts_addk_i32 m0, 0x800\n\ts_nop 0\n\t"
__device__ __forceinline__ void qkv_half_a(const void* sbase, unsigned voff, unsigned lds_dst) {
    asm volatile("s_mov_b32 m0, %2\n\ts_nop 3\n\t" GL_STEP("%3") GL_STEP("%3") GL_STEP("%3") GL_STEP("%3") GL_STEP("%3") GL_STEP("%3") GL_STEP("%3")
                 "global_load_lds_dwordx4 %0, %1" : "+v"(voff) : "s"(sbase), "s"(__builtin_amdgcn_readfirstlane(lds_dst)), "s"(16 * D_ * 2) : "memory", "m0", "scc");
}
__device__ __forceinline__ void qkv_half_b(const void* sbase, unsigned voff, unsigned lds_dst) {
    asm volatile("s_mov_b32 m0, %2\n\ts_nop 3\n\t" GL_STEP("%3") GL_STEP("%3") GL_STEP("%3") GL_STEP("%4") GL_STEP("%3") GL_STEP("%3") GL_STEP("%3") GL_STEP("%4")
                 GL_STEP("%3") GL_STEP("%3") GL_STEP("%3") "global_load_lds_dwordx4 %0, %1"
                 : "+v"(voff) : "s"(sbase), "s"(__builtin_amdgcn_readfirstlane(lds_dst)), "s"(16 * 3 * D_ * 2), "s"(D_ * 2 - 48 * 3 * D_ * 2) : "memory", "m0", "scc");
}
constexpr int qsl(int kt) { return (kt + 2) % 3; }
__device__ __forceinline__ u64 qkv_ks() { return 0xb3a2fedc98765410ull; }
__device__ __forceinline__ int qkv_k(u64 ks, int i) { return (int)(ks >> (4 * i)) & 15; }
__device__ __forceinline__ unsigned qkv_boff(int h, int lane) {
    return b_off(h, lane, 3 * D_, (((wg_id()) % 128) / 8) * 64);
}
__device__ __forceinline__ void qkv_stage_b(unsigned lds0, const u16* B, unsigned boff, int h, int kt, int sl) {
#pragma unroll
    for (int i = 0; i < 6; ++i)
        glds16(uni((const char*)B + ((kt * 64 + (i & 1) * 32) * 3 * D_ + (i >> 1) * D_) * 2), boff,
               lds0 + QBR + sl * QBS + h * 1024 + i * 4096);
}
__device__ __forceinline__ void qkv_phase(char* smem, const u16* A, const u16* __restrict__ B, u16* C, float* cs,
                                          const float* sp, unsigned* pflags, unsigned* oflags,
                                          const u16* __restrict__ wo, bool first, const u16* __restrict__ wg,
                                          const u16* __restrict__ wu, u64* epoch, u64& tok) {
    constexpr int NK = 16;
    const int tid = (int)opaque(threadIdx.x), lane = tid & 63;
    const int wid = __builtin_amdgcn_readfirstlane(tid >> 6);
    const int id = wg_id();
    const int tm = (id / 128) * 8 + id % 8, tn = (id % 128) / 8;
    const int m0 = tm * 128;
    const unsigned lds0 = __builtin_amdgcn_readfirstlane((unsigned)(size_t)(LDS_AS char*)smem);
    float* const csl = (float*)(smem + QCS);
    float* const cso = cs + 16 * F_ + id * 192;
    auto token = [&]() INL {
        asm volatile("s_load_dwordx2 %0, %1, 0x0\n\ts_waitcnt lgkmcnt(0)" : "=s"(tok) : "s"(epoch) : "memory");
        tok += 0x9e3779b97f4a7c15ull;
    };
    if (wid >= 4) {
        const int h = wid - 4, hl = tid - 256;
        const int row = h * 8 + (lane >> 3), hr = h * 4 + ((lane >> 3) & 3);
        const unsigned la = (unsigned)((m0 + row) * D_ + ((lane & 7) ^ ((row >> 1) & 7)) * 8) * 2u;
        const unsigned lh = (unsigned)((tm ? m0 - 16 + hr : 0) * D_ + ((lane & 7) ^ ((hr >> 1) & 7)) * 8) * 2u;
        const unsigned lb = qkv_boff(h, lane);
        const unsigned wb = wo_boff(h, lane);
        constexpr auto wn = [](int i) { return i == 0 ? 2 : (i == 1 ? WN1 : (i == 2 ? WN2 : (i == 3 ? WN3 : (i == 4 ? WN4 : 10 - WN1 - WN2 - WN3 - WN4)))); };
        static_assert(WN1 + WN2 <= 6 && WN1 + WN2 + WN3 <= 9, "free slots");
        auto wo_pre = [&](int i) INL {
            int e0 = 0;
#pragma unroll
            for (int x = 0; x < i; ++x) e0 += wn(x);
#pragma unroll
            for (int e = e0; e < e0 + wn(i); ++e) wo_stage_b(lds0, wo, wb, h, (int)(0xb765a9843210ull >> (4 * e)) & 15);
        };
        const int wtile = ((id / 64) * 4 + id % 4) * 16 + (id % 64) / 4;
        auto wo_res = [&]() INL {
#pragma unroll
            for (int i = 0; i < 4; ++i)
                glds16_dev(uni((const char*)A + (((wtile >> 4) - tm) * 128 + i * 32) * D_ * 2 + (wtile & 15) * 128), la, lds0 + h * 1024 + i * 4096);
        };
        auto wo_slow = [&]() INL {
            const unsigned bx = bid_();
            if (h == 1) touch1(SINK, wo, ((bx >> 3) * 32 + (lane & 31)) * 2048 + ((bx & 1) * 8 + 3) * 128);
        };
        auto marks = [&]() INL {
            const unsigned em = opaque(~0u), bo = BOXO + bid_() * 16384u + (unsigned)hl * 16u;
            const __amdgpu_buffer_rsrc_t r = __builtin_amdgcn_make_buffer_rsrc(C, 0, 0x7fffffff, 0x00020000);
#pragma unroll
            for (int i = 0; i < 4; ++i) __builtin_amdgcn_raw_buffer_store_b128(u32x4{em, em, em, em}, r, bo, i * 4096, 16);
        };
        auto put_stat = [&](float sum, float sq, int row, bool on) INL {
            const float mean = mulf(sum, 1.f / D_);
            const float var = fmaxf(subf(mulf(sq, 1.f / D_), mulf(mean, mean)), 0.f);
            const float rs = __builtin_amdgcn_rcpf(rbf(__builtin_amdgcn_sqrtf(rbf(addf(rbf(var), 1e-5f))))), nm = -rbf(mean);
            if (on) {
                *(float*)(smem + QSK + row * 4) = nm;
                *(float*)(smem + QSK + 576 + row * 4) = rs * (0.17677669529663687f * 1.44269504088896341f);
                *(float*)(smem + QSK + 1152 + row * 4) = rs;
            }
        };
        if (first) {
            const unsigned long long hmask = h == 0 ? 0xffffff00ull : 0xffffffffull;
            auto piece = [&](int kt, int p) INL {
                const unsigned da = lds0 + qsl(kt) * QAS;
                if (p < 4) glds16(uni((const char*)A + kt * 128 + p * 32 * D_ * 2), la, da + h * 1024 + p * 4096);
                else if (p == 4) glds16_mask(uni((const char*)A + kt * 128), lh, da + 16384 + h * 512, hmask);
                else glds16(uni((const char*)B + ((kt * 64 + ((p - 5) & 1) * 32) * 3 * D_ + ((p - 5) >> 1) * D_) * 2), lb,
                            lds0 + QBR + qsl(kt) * QBS + h * 1024 + (p - 5) * 4096);
            };
            __builtin_amdgcn_s_setprio(HPRIO);
            constexpr unsigned tw = SINK;
            const unsigned one2 = opaque(0x3f803f80u);
            if (hl < 24) *(u32x4*)(smem + (hl >> 3) * QAS + 128 * 128 + (hl & 7) * 16) = u32x4{one2, one2, one2, one2};
            const bool idle = (tn & 7) == 3;
            const int job = idle ? 0 : id - 8 * ((id + 32) >> 6);
            const int cb = job >> 1, kh = job & 1;
            const char* const wp = uni((const char*)((cb < 56 ? wg : wu) + (long long)kh * 512 * F_ + (cb % 56) * 64));
            const unsigned wpo = (unsigned)((idle ? 0 : hl >> 3) * F_ + (hl & 7) * 8) * 2u;
            const int wst = idle ? 0 : 64 * F_;
            f32x4 wv[NK];
            f32x4 sm = {0.f, 0.f, 0.f, 0.f};
            u32x4 pk;
#pragma unroll
            for (int d = 0; d < 4; ++d) pk[d] = opaque((lane & 15) >> 1 == d ? (lane & 1 ? 0x3f800000u : 0x3f80u) : 0u);
            asm volatile("" : "+v"(sm), "+v"(pk));
            auto wadd = [&](int r) INL { asm volatile("v_mfma_f32_16x16x32_bf16 %0, %1, %2, %0" : "+v"(sm) : "v"(pk), "v"(wv[r])); };
            constexpr auto xst = [](int j) { return j == 0 ? -1 : (j == 1 ? XS1 : (j == 2 ? XS2 : XS3)); };
            constexpr auto xat = [xst](int j) { return xst(j) < 0 ? 1 + j : xst(j) + 3; };
            static_assert(XS1 <= XS2 && XS2 <= XS3 && xat(0) < xat(1) && xat(1) < xat(2) && xat(2) < xat(3) && xat(3) < XLK && XLK + 3 < NK - 1, "steps");
            const unsigned xo = opaque((unsigned)((m0 + tn * 8 + h * 2 + (lane >> 5)) * D_ * 2 + (lane & 31) * 16));
            const char* const xw = uni((const char*)(oflags - 5 * 8192));
            f32x4 xv[4], xg;
            float xs[4];
#pragma unroll
            for (int e = 0; e < 4; ++e) xs[e] = 0.f;
            auto xreq = [&](auto kc) INL {
                static_for<0, 4>([&](auto jc) INL {
                    constexpr int J = decltype(jc)::value;
                    if constexpr (xst(J) == decltype(kc)::value) gload16<J * 512>(xv[J], A, xo);
                });
            };
            auto xadd = [&](int j, int e) INL {
                float t0, t1;
                asm volatile("v_lshlrev_b32 %4, 16, %6\n\tv_and_b32 %5, 0xffff0000, %6\n\tv_add_f32 %0, %0, %4\n\tv_add_f32 %1, %1, %5\n\t"
                             "v_fmac_f32 %2, %4, %4\n\tv_fmac_f32 %3, %5, %5"
                             : "+v"(xs[0]), "+v"(xs[1]), "+v"(xs[2]), "+v"(xs[3]), "=&v"(t0), "=&v"(t1) : "v"(xv[j][e]));
            };
            auto xput = [&]() INL {
#define XROR(N) "v_add_f32_dpp %0, %0, %0 row_ror:" #N " row_mask:0xf bank_mask:0xf\n\tv_add_f32_dpp %1, %1, %1 row_ror:" #N " row_mask:0xf bank_mask:0xf\n\ts_nop 0\n\t"
                asm volatile("v_add_f32 %0, %0, %2\n\tv_add_f32 %1, %1, %3\n\ts_nop 0\n\t" XROR(8) XROR(4) XROR(2) XROR(1)
                             "s_nop 0\n\tv_permlane16_swap_b32 %0, %1\n\ts_nop 1\n\tv_add_f32 %0, %0, %1"
                             : "+v"(xs[0]), "+v"(xs[2]) : "v"(xs[1]), "v"(xs[3]));
                const u32x2 d = {__float_as_uint(xs[0]), (unsigned)(lane & 16 ? tok >> 32 : tok)};
                asm volatile("s_mov_b32 exec_lo, 0x10001\n\ts_mov_b32 exec_hi, 0x10001\n\ts_nop 2\n\tglobal_store_dwordx2 %0, %1, %2 sc1\n\ts_mov_b64 exec, -1"
                             :: "v"((xo >> 11 << 4) + (unsigned)(lane & 16) / 2), "v"(d), "s"(xw) : "memory");
            };
            const int xr = m0 + (hl < 144 ? hl - 16 : 0);
            const unsigned xi = opaque((unsigned)(xr < 0 ? 0 : xr) * 16u);
            auto xget = [&]() INL {
                asm volatile("" : "+v"(xg));
                auto late = [&]() INL { return __ballot(__float_as_uint(xg[1]) != (unsigned)tok || __float_as_uint(xg[3]) != (unsigned)(tok >> 32)) != 0; };
                if (__builtin_expect(late(), 0)) {
#pragma unroll 1
                    for (unsigned n = 1u << 22; n; --n) {
                        asm volatile("s_nop 4\n\tglobal_load_dwordx4 %0, %1, %2 sc1\n\ts_waitcnt vmcnt(0)" : "=v"(xg) : "v"(xi), "s"(xw) : "memory");
                        if (!late()) break;
                    }
                }
                put_stat(xg[0], xg[2], hl, hl < 144);
            };
            const unsigned bx = bid_();
            const int kq = bx >> 4;
            auto sweep = [&]() INL { touch1(tw, B, (unsigned)(bx * 192 + (hl < 192 ? hl : 0)) * 128u); };
            xreq(CT<-1>{});
            token();
            if (h >= 2) {
                if (kq < KIN && kq >= KSKIP) {
                    for (int i = (kq - KSKIP + 1) * SLP; i > 0; --i) __builtin_amdgcn_s_sleep(1);
                    touch1(tw, B, (unsigned)(bx * 192 + (hl & 127)) * 128u);
                    if (h == 2) touch1(tw, B, (unsigned)(bx * 192 + hl) * 128u);
                }
                qkv_half_a(uni((const char*)A + 128 - 16 * D_ * 2), la, lds0 + qsl(1) * QAS + (h - 2) * 1024);
                qkv_half_b(uni((const char*)B + 48 * 3 * D_ * 2), lb, lds0 + QBR + qsl(1) * QBS + (h - 2) * 1024);
            }
            if (h < 2) wait_vm<2 + (XS1 < 0) + (XS2 < 0) + (XS3 < 0)>();
            constexpr int WR = NK / WNB;
            constexpr auto wat = [](int r) { return WS0 + (r / WR) * WSD + WNG - 1 + 3; };
            static_assert(WSD >= 2 && WS0 + (WNB - 1) * WSD + WNG - 1 <= NK - 3, "bursts");
            unsigned wbm = 0;
#pragma unroll
            for (int b = 0; b < WNB; ++b) wbm |= 1u << (WS0 + b * WSD);
            const int wg0 = (bx >> 3) % WNG;
            wbm <<= wg0;
            const unsigned wwm = wbm * 6u;
            unsigned wvo = wpo;
#pragma unroll
            for (int r = 0; r < NK; ++r) asm volatile("" : "=v"(wv[r]));
            static_for<0, NK>([&](auto kc) INL {
                constexpr int KT = decltype(kc)::value;
                constexpr auto ex = [xat](int k) {
                    return (k == PV0 ? 1 : 0) + (k == NK - 2 ? 8 : 0) + (k >= 0 ? (k == XS1) + (k == XS2) + (k == XS3) : 0) + (k == xat(3)) + (k == XLK);
                };
                constexpr int CW = (KT + 1 < NK ? 11 : 0) + ex(KT - 2) + ex(KT - 1);
                if constexpr (KT > WS0 && KT <= WS0 + (WNB - 1) * WSD + WNG + 1)
                    wait_vm_if<CW + WR, CW, KT>(wwm);
                else if constexpr (KT > 0) wait_vm<CW>();
                BARRIER();
                static_for<0, 11>([&](auto pc) INL {
                    constexpr int P = decltype(pc)::value;
                    if constexpr (KT + 2 < NK) piece(KT + 2, P);
#pragma unroll
                    for (int r = 0; r < NK; ++r) if (wat(r) == KT && (r & 3) == P) wadd(r);
                    if constexpr (P >= 6 && P < 10) {
#pragma unroll
                        for (int j = 0; j < 4; ++j) if (xat(j) == KT) xadd(j, P - 6);
                    }
                    if constexpr (P == 9 && KT == XLK + 3) xget();
                    SCHED();
                });
                xreq(kc);
                if constexpr (KT == xat(3)) xput();
                if constexpr (KT == XLK) gload16_dev(xg, xw, xi);
                if constexpr (KT + KIN < NK) { if (kq == KT + KIN) sweep(); }
                static_for<0, NK / 4>([&](auto qc) INL {
                    constexpr int Q = decltype(qc)::value, S = WS0 + (Q * 4 / WR) * WSD;
                    if constexpr (KT >= S && KT < S + WNG)
                        gload4_if<KT - S>(wv[Q * 4], wv[Q * 4 + 1], wv[Q * 4 + 2], wv[Q * 4 + 3], wvo, wp, wst, wg0);
                });
                if constexpr (KT == PV0) touch1(tw, wo, (unsigned)(bx * 64 + (hl & 63)) * 128u);
                if constexpr (KT == WT0) wo_slow();
                if constexpr (KT == NK - 2) { wo_res(); wo_pre(0); }
                if constexpr (KT == 7) { if (h == 0) att_put(smem, lane, m0); }
                if constexpr (KT == FWT) {
                    if (h == 0) touch1(tw, oflags - 7 * 8192, ((lane < 12 ? lane : 0) * 256 + bx) * 128u);
                }
                WAIT_LGKM0();
            });
            wait_vm<8>();
#pragma unroll
            for (int r = 0; r < NK; ++r) if (wat(r) >= NK) wadd(r);
            asm volatile("s_nop 7\n\ts_nop 3" : "+v"(sm));
            float s0 = sm[0], s1 = sm[1], s2 = sm[2], s3 = sm[3];
            asm volatile("s_nop 1\n\tv_add_f32_dpp %0, %0, %0 row_ror:8 row_mask:0xf bank_mask:0xf\n\tv_add_f32_dpp %1, %1, %1 row_ror:8 row_mask:0xf bank_mask:0xf\n\t"
                         "v_add_f32_dpp %2, %2, %2 row_ror:8 row_mask:0xf bank_mask:0xf\n\tv_add_f32_dpp %3, %3, %3 row_ror:8 row_mask:0xf bank_mask:0xf"
                         : "+v"(s0), "+v"(s1), "+v"(s2), "+v"(s3));
            if ((lane & 40) == 0)
                store16_wt(cs, (unsigned)((kh * 4 + h) * 2 * F_ + (idle ? 1 << 18 : cb * 64) + (lane & 7) * 8 + (lane >> 4) * 4) * 4u,
                           u32x4{__float_as_uint(s0), __float_as_uint(s1), __float_as_uint(s2), __float_as_uint(s3)});
            wo_pre(1);
            wo_pre(2);
            __builtin_amdgcn_s_setprio(0);
            WAIT_LGKM0();
            BARRIER();
            if (hl < 48) store16_wt(cso, (unsigned)hl * 16u, *(const u32x4*)(csl + hl * 4));
            wo_pre(3);
            marks();
            if (h == 3) code_touch(51200, lane);
            wait_vm<20>();
            BARRIER();
        } else {
            const unsigned* const f = pflags + (lane < 32 ? (tm && (lane & 16) ? tm - 1 : tm) * 16 + (lane & 15) : wtile) * 32;
            const u64 ks = qkv_ks();
            u64 have, at = 0x10001u << qkv_k(ks, 0) | 0x10001u << qkv_k(ks, 1), lk;
            look(lk, at, f);
            auto stage = [&](int st, int sl, auto bc) INL {
                const int kt = qkv_k(ks, st);
                poll(have, 0x10001ull << kt, at, f, tok);
                const unsigned da = lds0 + sl * QAS;
#pragma unroll
                for (int i = 0; i < 4; ++i) glds16_dev(uni((const char*)A + kt * 128 + i * 32 * D_ * 2), la, da + h * 1024 + i * 4096);
                glds16_part<32>(uni((const char*)A + kt * 128), lh, da + 16384 + h * 512);
                if constexpr (decltype(bc)::value) qkv_stage_b(lds0, B, lb, h, kt, sl);
            };
            {
                const unsigned bx = bid_();
                asm volatile("" :: "v"(la), "v"(lh), "v"(lb), "s"(A));
                pubc<1, 8>(pflags + ((dn_grp(bx & 7) * 4 + (bx >> 3 & 3)) * 16 + (bx >> 5) * 2 + (DPAR ? bx & 1 : bx >> 2 & 1)) * 32,
                                            tok, lds0 + GCN + 8);
            }
            have = seen(lk, at, tok);
            constexpr u64 all = 0x1ffffffffull;
            stage(0, qsl(0), CT<0>{});
            look(lk, all, f);
            stage(1, qsl(1), CT<0>{});
            wait_vm<5>();
            BARRIER();
            at = all;
            have |= seen(lk, at, tok);
            stage(2, qsl(2), CT<1>{});
            if (h == 0) att_put(smem, lane, m0);
            wait_vm<11>();
            BARRIER();
            stage(3, qsl(3), CT<1>{});
            if (h == 0) glds16_dev(uni((const char*)cso), lane * 16u, lds0 + QCS);
            int sl = qsl(4);
#pragma unroll 1
            for (int st = 4; st < NK; ++st) {
                wait_vm<11>();
                BARRIER();
                stage(st, sl, CT<1>{});
                if (__builtin_expect(st == WT1, 0)) wo_slow();
                sl = sl == 2 ? 0 : sl + 1;
            }
            f32x4 spv[8];
            poll(have, 1ull << 32, at, f, tok);
            {
                const int srow = m0 - 16 + (hl < 144 ? hl & ~1 : 0);
                const int sr = srow < 0 ? 0 : srow;
                const unsigned so = (unsigned)((sr >> 5) * 512 + (hl & 1) * 256 + (sr & 31)) * 8u;
                gload16_dev(spv[0], sp, so);
                static_for<1, 8>([&](auto ic) INL { gload16_devo<decltype(ic)::value * 256>(spv[decltype(ic)::value], sp, so); });
            }
            wait_vm<19>();
            BARRIER();
            wo_res();
            wo_pre(0);
            wait_vm<16>();
            BARRIER();
            if constexpr (QSP) __builtin_amdgcn_s_setprio(QSP);
            wait_vm<8>();
            float sm[4];
#pragma unroll
            for (int i = 0; i < 8; ++i) {
                asm volatile("" : "+v"(spv[i]));
#pragma unroll
                for (int e = 0; e < 4; ++e) sm[e] = i ? addf(sm[e], spv[i][e]) : spv[i][e];
            }
#pragma unroll
            for (int e = 0; e < 4; ++e)
                sm[e] = addf(sm[e], __uint_as_float(__builtin_amdgcn_update_dpp(0u, __float_as_uint(sm[e]), 0xB1, 0xf, 0xf, false)));
            put_stat(hl & 1 ? sm[2] : sm[0], hl & 1 ? sm[3] : sm[1], hl, hl < 144);
            if constexpr (QSP) __builtin_amdgcn_s_setprio(0);
            if constexpr (QSO == 0) wo_pre(1);
            WAIT_LGKM0();
            BARRIER();
            if constexpr (QSO == 1) wo_pre(1); else wo_pre(2);
            BARRIER();
            if constexpr (QSO == 1) wo_pre(2);
            wo_pre(3);
            marks();
            if (h == 3) code_touch(51200, lane);
            wait_vm<20>();
            BARRIER();
        }
        static_assert(WN1 + WN2 + WN3 == 8 && WN4 == 2, "counts");
        wo_pre(4);
    } else {
        const int wm = wid >> 1, wn = wid & 1;
        const int l15 = lane & 15, g = lane >> 4, q = l15 >> 2, p4 = l15 & 3;
        unsigned ab[2], hb[2], bj[6];
#pragma unroll
        for (int kk = 0; kk < 2; ++kk) {
            const unsigned sw = ((kk * 4 + g) ^ (l15 >> 1)) * 16;
            ab[kk] = opaque(lds0 + (wm * 64 + l15) * 128 + sw);
            hb[kk] = opaque(lds0 + (128 + l15) * 128 + sw);
        }
#pragma unroll
        for (int j = 0; j < 6; ++j)
            bj[j] = opaque(lds0 + QBR + (j >> 1) * 8192 + (g * 8 + q) * 128 + p4 * 8 + (((wn * 2 + ((j & 1) ^ wm)) ^ ((q >> 1) | ((g & 1) << 1))) << 5));
        f32x4 acc[5][6];
        constexpr auto qcol = [](int c) { return c < 3 ? 2 * c + 1 : 2 * c - 6; };
        auto ldop = [&](bf16x8 (&af)[10], s16x4 (&bf)[24], auto ktc, auto oc) INL {
            constexpr int KT = decltype(ktc)::value, O = decltype(oc)::value;
            if constexpr (O == 2 || (O >= 5 && O < 12)) {
                constexpr int X = O == 2 ? 0 : O - 4;
                af[X] = lds_a<qsl(KT) * QAS + (X / 2) * 2048>(ab[X & 1]);
            } else if constexpr (O == 24 || O == 25) {
                af[O - 16] = lds_a<qsl(KT) * QAS>(hb[O & 1]);
            } else {
                constexpr int X = O < 2 ? O : (O < 5 ? O - 1 : (O < 24 ? O - 8 : O - 10));
                bf[X] = lds_h<qsl(KT) * QBS + ((X >> 1) & 1) * 4096 + (X & 1) * 512>(bj[qcol(X / 4)]);
            }
        };
        unsigned ea[10], ma[2], xw[2], xa, oo, l16, p4_, g16, cwa, cwb;
        f32x4 vnm[5], ccv[4];
        f32x2 rnm[3], rsc[2], rk[4];
        float ccw[2];
        u32x4 am[4];
        constexpr int NEA = 15;
        auto eadr = [&](auto tc) INL {
            constexpr int T = decltype(tc)::value;
            if constexpr (T == 0) asm volatile("v_and_b32 %0, 63, %1\n\tv_lshlrev_b32 %0, 4, %0" : "=v"(l16) : "v"(threadIdx.x));
            if constexpr (T == 1) { p4_ = opaque(l16 >> 2 & 60u); ma[0] = opaque(l16 + lds0 + ATX); }
            if constexpr (T == 2) ma[1] = opaque(ma[0] + (1 - wm) * 2048);
            if constexpr (T == 3) { asm volatile("v_and_b32 %0, 48, %1" : "=v"(g16) : "v"(threadIdx.x)); ea[0] = opaque(p4_ + lds0 + QSK); ea[1] = opaque(g16 + lds0 + QSK); }
            if constexpr (T == 4) { ea[2] = opaque(ea[0] + wm * 256); ea[3] = opaque(ea[1] + wm * 256); }
            if constexpr (T == 5) ea[4] = opaque(g16 + lds0 + QCS + (wn * 2 + wm) * 64);
            if constexpr (T == 6) { ea[6] = opaque(ea[4] ^ 64u); ea[5] = opaque(ea[4] - g16 + p4_); }
            if constexpr (T == 7) { ea[7] = opaque(ea[5] ^ 64u); ea[9] = opaque((p4_ & 56u) - g16); }
            if constexpr (T == 8) ea[8] = opaque(ea[2] - ((ea[9] & 63u) | -1216u));
            if constexpr (T == 9) ea[9] = opaque(ea[2] - (((ea[9] - 8u) & 63u) | -1216u));
            if constexpr (T == 10) xa = opaque(l16 + lds0 + QXC + wn * 4096);
            if constexpr (T == 11) xw[1] = opaque(xa + (1 - wm * 5) * 2048);
            if constexpr (T == 12) { xw[0] = opaque(xa + wm * 8); xa = opaque(opaque(xa) + wm * 2048); }
            if constexpr (T == 13) oo = opaque(l16 + (tm * 32 + tn) * 8192);
            if constexpr (T == 14) oo = opaque(oo + (wm * 32 + wn) * 4096);
            if constexpr (T == 15) cwa = opaque(p4_ == 0 ? ea[4] : xa);
            if constexpr (T == 16) cwb = opaque(g16 == 0 ? ea[5] : xa);
        };
        auto ld2 = [&](unsigned a) INL { return f32x2{*(const LDS_AS float*)(size_t)a, *(const LDS_AS float*)(size_t)(a + 64)}; };
        auto eld = [&](auto oc) INL {
            constexpr int O = decltype(oc)::value;
            if constexpr (O < 4) am[O] = *(const LDS_AS u32x4*)(size_t)(O == 2 ? ma[1] : ma[0] + O * 1024);
            else if constexpr (O == 5 || O == 7) ccw[O / 2 - 2] = *(const LDS_AS float*)(size_t)(ea[O] + 512);
            else if constexpr (O < 8) ccv[O / 2] = *(const LDS_AS f32x4*)(size_t)(ea[O] + 256);
            else if constexpr (O < 10) ccv[O - 8] = *(const LDS_AS f32x4*)(size_t)(ea[2 * O - 12]);
            else if constexpr (O == 10) rnm[2].x = *(const LDS_AS float*)(size_t)ea[0];
            else if constexpr (O == 11) vnm[4] = *(const LDS_AS f32x4*)(size_t)ea[1];
            else if constexpr (O == 12 || O == 15) rnm[O == 12] = ld2(ea[2] + 64 + (O == 12) * 128);
            else if constexpr (O < 18) { constexpr int I = O < 15 ? 16 - O : 17 - O; vnm[I] = *(const LDS_AS f32x4*)(size_t)(ea[3] + (I + 1) * 64); }
            else if constexpr (O < 20) rsc[O - 18] = ld2(ea[2] + 576 + 64 + (O - 18) * 128);
            else rk[O - 20] = ld2(ea[8 + (O - 20) / 2] + (O & 1) * 128);
        };
        auto unit = [&](bf16x8 (&af)[10], s16x4 (&bf)[24], bf16x8 (&an)[10], s16x4 (&bn)[24], auto ktc, auto hc, auto fic) INL {
            constexpr bool FI = decltype(fic)::value;
            constexpr int KT = decltype(ktc)::value, HF = decltype(hc)::value;
            static_for<0, 26>([&](auto tc) INL {
                constexpr int T = decltype(tc)::value, X = HF * 26 + T, kk = X & 1;
                constexpr int c = X < 32 ? X / 8 : (X < 42 ? 4 : 5), p = (X < 32 ? X % 8 : (X < 42 ? X - 32 : X - 42)) / 2;
                constexpr int i = c == 5 ? 4 - p : (c & 1 ? 3 - p : p);
                if constexpr (qcol(c) >= 4) mma_ip(af[i * 2 + kk], cat(bf[c * 4 + kk * 2], bf[c * 4 + kk * 2 + 1]), acc[i][qcol(c)]);
                else mma_ip(cat(bf[c * 4 + kk * 2], bf[c * 4 + kk * 2 + 1]), af[i * 2 + kk], acc[i][qcol(c)]);
                SCHED();
                if constexpr (HF == 1) {
                    if constexpr (KT + 1 == NK && FI) {
                        if constexpr (T < 18) eld(CT<(T < 4 ? T : T + 6)>{});
                        else if constexpr (T == 21) *(LDS_AS f32x4*)(size_t)cwa = acc[4][0];
                        else if constexpr (T == 22) *(LDS_AS f32x4*)(size_t)(cwa + 256) = acc[4][2];
                        else if constexpr (T == 24) *(LDS_AS float*)(size_t)(cwb + 512) = acc[4][4][0];
                    }
                    if constexpr (KT + 1 == NK && !FI) {
                        static_assert(QBT >= 10, "order");
                        constexpr int EN = 26 - QBT < 14 ? 26 - QBT : 14;
                        if constexpr (T < 10) eld(tc);
                        if constexpr (T == QBT - 1) BARRIER();
                        if constexpr (T >= QBT)
                            static_for<(T - QBT) * 14 / EN, ((T + 1 - QBT) * 14 / EN < 14 ? (T + 1 - QBT) * 14 / EN : 14)>([&](auto oc) INL { eld(CT<10 + decltype(oc)::value>{}); });
                    }
                    if constexpr (KT + 1 < NK && T < QG1) ldop(an, bn, CT<KT + 1>{}, tc);
                    if constexpr (KT + 1 < NK && T >= QL1 && T < QL1 + 16 - QG1) ldop(an, bn, CT<KT + 1>{}, CT<T - QL1 + QG1>{});
                } else {
                    if constexpr (T < 18) ldop(af, bf, ktc, CT<16 + T>{});
                }
                SCHED();
                if constexpr (FI && X == 31) {
                    mma_ip(cat(bf[12], bf[13]), af[8], acc[4][0]);
                    SCHED();
                    mma_ip(cat(bf[14], bf[15]), af[9], acc[4][0]);
                    SCHED();
                }
            });
        };
        if (first) token();
        if (!first && id == 0 && wid == 3) {
#pragma unroll 1
            for (unsigned spins = 0; spins < (1u << 22); ++spins) {
                bool ok = true;
#pragma unroll
                for (int i = 0; i < 4; ++i)
                    ok &= __hip_atomic_load((gu64*)(oflags + 8192 + (i * 64 + lane) * 32), __ATOMIC_RELAXED, __HIP_MEMORY_SCOPE_AGENT) == tok;
                if (__all(ok)) break;
            }
            if (lane == 0) __hip_atomic_store((gu64*)epoch, tok, __ATOMIC_RELAXED, __HIP_MEMORY_SCOPE_AGENT);
        }
        bf16x8 a0[10], a1[10], a2[10];
        s16x4 b0[24], b1[24], b2[24];
        auto loop = [&](auto fic) INL {
#pragma unroll
        for (int i = 0; i < 5; ++i)
#pragma unroll
            for (int j = 0; j < 6; ++j)
                if (i < 4 || j == 2 || j == 4) {
                    acc[i][j] = f32x4{0.f, 0.f, 0.f, 0.f};
                    asm volatile("" : "+v"(acc[i][j]));
                }
            if constexpr (decltype(fic)::value) wait_vm<0>();
            BARRIER();
            static_for<0, 16>([&](auto oc) INL { ldop(a0, b0, CT<0>{}, oc); });
            auto ktile = [&](bf16x8 (&af)[10], s16x4 (&bf)[24], bf16x8 (&an)[10], s16x4 (&bn)[24], auto kc) INL {
                unit(af, bf, an, bn, kc, CT<0>{}, fic);
                WAIT_LGKM0();
                BARRIER();
                unit(af, bf, an, bn, kc, CT<1>{}, fic);
            };
#pragma unroll 1
            for (int it = 0; it < 5; ++it) {
                ktile(a0, b0, a1, b1, CT<0>{});
                ktile(a1, b1, a2, b2, CT<1>{});
                ktile(a2, b2, a0, b0, CT<2>{});
            }
            unit(a0, b0, a1, b1, CT<NK - 1>{}, CT<0>{}, fic);
            unit(a0, b0, a1, b1, CT<NK - 1>{}, CT<1>{}, fic);
        };
        if (first) {
            static_for<0, NEA + 2>([&](auto tc) INL { eadr(tc); });
            acc[4][0] = f32x4{0.f, 0.f, 0.f, 0.f};
            asm volatile("" : "+v"(acc[4][0]));
            loop(CT<1>{});
            WAIT_LGKM0();
            BARRIER();
            static_for<4, 10>([&](auto oc) INL {
                eld(oc);
                SCHED();
            });
            __builtin_amdgcn_s_waitcnt(0xc07f);
        } else {
            static_for<0, NEA>([&](auto tc) INL { eadr(tc); });
            loop(CT<0>{});
            BARRIER();
        }
        SCHED();
        auto ln2 = [&](int i, int j0, int j1) INL {
            f32x2 y[4];
#pragma unroll
            for (int x = 0; x < 4; ++x) {
                const int j = x < 2 ? j0 : j1;
                const f32x2 a = x & 1 ? acc[i][j].zw : acc[i][j].xy;
                if (j < 4) y[x] = __builtin_elementwise_fma(i & 1 ? rnm[i / 2].yy : rnm[i / 2].xx, x & 1 ? ccv[j].zw : ccv[j].xy, a);
                else y[x] = __builtin_elementwise_fma(x & 1 ? vnm[i].zw : vnm[i].xy, f32x2{ccw[j - 4], ccw[j - 4]}, a);
            }
            SCHED();
            acc[i][j0] = f32x4{y[0].x, y[0].y, y[1].x, y[1].y};
            acc[i][j1] = f32x4{y[2].x, y[2].y, y[3].x, y[3].y};
        };
        auto rnd = [&](f32x4 a, f32x4 b) INL {
            u32x4 r = {pk2(a[0], a[1]), pk2(a[2], a[3]), pk2(b[0], b[1]), pk2(b[2], b[3])};
            asm volatile("" : "+v"(r));
            SCHED();
            return r;
        };
        u32x4 qf[4], kf[4];
        u32x2 v3[2];
        ln2(4, 2, 4);
        {
            const u32x4 hv = rnd(acc[4][2], acc[4][4]);
            *(LDS_AS u32x2*)(size_t)(xw[0]) = hv.xy;
            *(LDS_AS u32x2*)(size_t)(xw[0] + 1024) = hv.zw;
        }
        ln2(3, 2, 3);
        kf[3] = rnd(acc[3][2], acc[3][3]);
        *(LDS_AS u32x2*)(size_t)(xw[1] + 8) = kf[3].xy;
        *(LDS_AS u32x2*)(size_t)(xw[1]) = kf[3].zw;
        ln2(3, 4, 5);
        v3[0] = u32x2{pk2(acc[3][4][0], acc[3][4][1]), pk2(acc[3][4][2], acc[3][4][3])};
        v3[1] = u32x2{pk2(acc[3][5][0], acc[3][5][1]), pk2(acc[3][5][2], acc[3][5][3])};
        *(LDS_AS u32x2*)(size_t)(xw[1] + 1032) = v3[0];
        *(LDS_AS u32x2*)(size_t)(xw[1] + 1024) = v3[1];
        SCHED();
        f32x4 vf[3][2];
        f32x2 sc[4];
#pragma unroll
        for (int i = 3; i >= 0; --i) {
            ln2(i, 0, 1);
            qf[i] = rnd(acc[i][0], acc[i][1]);
            if (i < 3) {
                ln2(i, 2, 3);
                kf[i] = rnd(acc[i][2], acc[i][3]);
                ln2(i, 4, 5);
                vf[i][0] = acc[i][4];
                vf[i][1] = acc[i][5];
                asm volatile("" : "+v"(vf[i][0]), "+v"(vf[i][1]));
            }
        }
#pragma unroll
        for (int i = 0; i < 4; ++i) {
            sc[i] = rk[i] * rsc[i & 1];
            asm volatile("" : "+v"(sc[i]));
        }
        WAIT_LGKM0();
        BARRIER();
        SCHED();
        att_run(qf, kf, vf, v3, am, rk, sc, xa, oo, C);
        SCHED();
    }
}

__device__ __forceinline__ unsigned dn_boff(int h, int lane) { return b_off(h, lane, D_, (bid_() >> 5) * 128); }
__device__ __forceinline__ void dn_stage_b(unsigned lds0, const u16* B, unsigned boff, int kh, int h, int i, int kg, int sl) {
    glds16(uni((const char*)B + (long long)(kg * 64 + (i & 1) * 32) * D_ * 2 + ((i >> 1) ^ kh) * 128), boff,
           lds0 + h * 1024 + sl * 16384 + i * 4096);
}
#define BOX_LOOK(r) "global_load_dwordx4 v[" r ":" r "+3], %14, %15 sc1\n\tglobal_load_dwordx4 v[" r "+4:" r "+7], %14, %15 offset:1024 sc1\n\t" \
    "global_load_dwordx4 v[" r "+8:" r "+11], %14, %15 offset:2048 sc1\n\tglobal_load_dwordx4 v[" r "+12:" r "+15], %14, %15 offset:3072 sc1\n\t"
#define BOX_MAX(r, i) "v_max3_u32 %12, %12, v[" r "+" i "], v[" r "+" i "+1]\n\t"
#define BOX_TEST(r) "s_waitcnt vmcnt(4)\n\tv_max_u32 %12, v[" r "], v[" r "+1]\n\t" BOX_MAX(r, "2") BOX_MAX(r, "4") BOX_MAX(r, "6") \
    BOX_MAX(r, "8") BOX_MAX(r, "10") BOX_MAX(r, "12") BOX_MAX(r, "14") "v_cmp_eq_u32 vcc, -1, %12\n\t"
#define BOX_MOV(r, i) "v_mov_b64 v[200+" i ":201+" i "], v[" r "+" i ":" r "+1+" i "]\n\t"
#define BOX_TAKE(r) BOX_MOV(r, "0") BOX_MOV(r, "2") BOX_MOV(r, "4") BOX_MOV(r, "6") BOX_MOV(r, "8") BOX_MOV(r, "10") BOX_MOV(r, "12") BOX_MOV(r, "14")
__device__ __forceinline__ void box_wait(u32x4 (&a)[4], u32x4 (&b)[4], u32x4 (&c)[4], const void* box, unsigned off) {
    unsigned t, n;
    asm volatile(BOX_TEST("200") "s_cbranch_vccz 3f\n\ts_mov_b32 %13, 0x100000\n"
                 "1:\n\t" BOX_LOOK("232") BOX_TEST("216") "s_cbranch_vccz 2f\n\t" BOX_LOOK("216") BOX_TEST("232")
                 "s_cbranch_vccz 4f\n\ts_sub_u32 %13, %13, 1\n\ts_cbranch_scc0 1b\n"
                 "4:\n\t" BOX_TAKE("232") "s_branch 5f\n"
                 "2:\n\t" BOX_TAKE("216") "\n5:\n\ts_nop 1\n3:"
                 : "+{v[200:203]}"(a[0]), "+{v[204:207]}"(a[1]), "+{v[208:211]}"(a[2]), "+{v[212:215]}"(a[3]),
                   "+{v[216:219]}"(b[0]), "+{v[220:223]}"(b[1]), "+{v[224:227]}"(b[2]), "+{v[228:231]}"(b[3]),
                   "=&{v[232:235]}"(c[0]), "=&{v[236:239]}"(c[1]), "=&{v[240:243]}"(c[2]), "=&{v[244:247]}"(c[3]), "=&v"(t), "=&s"(n)
                 : "v"(off), "s"(box) : "memory", "vcc", "scc");
}
__device__ __forceinline__ void dn_phase(char* smem, const u16* A, const u16* __restrict__ B, u16* C, float* sp,
                                         float* slab, unsigned* gflags, unsigned* oflags,
                                         const u16* pf, u64 tok) {
    constexpr int BM = 128, NS = 4, NSO = NSOLO;
    constexpr int ASZ = BM * 128, SLOT = 2 * ASZ;
    constexpr int lda = F_;
    constexpr int BOX = 16384;

    const int tid = (int)opaque(threadIdx.x), lane = tid & 63;
    const int wid = __builtin_amdgcn_readfirstlane(tid >> 6);
    const unsigned bx = bid_();
    const int xcd = bx & 7, r_ = bx >> 3;
    const int kh = DPAR ? xcd & 1 : xcd >> 2, tm = dn_grp(xcd) * 4 + (r_ & 3), tn = r_ >> 2;
    const int m0 = tm * BM, n0 = tn * 128 + kh * 64;
    const unsigned lds0 = __builtin_amdgcn_readfirstlane((unsigned)(size_t)(LDS_AS char*)smem);
    ResC rc;
    u32x4 pb[4], pc[4];
    if (wid >= 4) {
        const int h = wid - 4;
        unsigned aoff = (unsigned)((m0 / 4 + h * 2 + (lane >> 5)) * (8 * F_) + (((lane >> 3) & 3) ^ (h & 1)) * 128 + (lane & 7) * 16);
        unsigned boff = dn_boff(h, lane);
        const unsigned* const f = gflags + (tm * 32 + (lane & 31)) * 32;
        u64 have, at = (u64)(((1u << DLK) - 1u) << (xcd & 6) * 4), lk;
        look(lk, at, f);
        asm volatile("" : "+v"(aoff), "+v"(boff), "+s"(A));
        pubc<1>(gflags + ((r_ & 7) * 64 + 32 + xcd * 4 + (r_ >> 3)) * 32, tok, lds0 + GCN + 4);
        have = seen(lk, at, tok);
        auto stage = [&](int kg, int sl, auto nbc) INL {
            poll(have, (1u << (kg * 4 / 7)) | (1u << ((kg * 64 + 63) / 112)), at, f, tok);
#pragma unroll
            for (int i = 0; i < 4; ++i)
                glds16_dev(uni((const char*)A + kg * 512 + i * 32 * lda * 2), aoff, lds0 + DNA + h * 1024 + sl * ASZ + i * 4096);
#pragma unroll
            for (int i = 0; i < decltype(nbc)::value; ++i) dn_stage_b(lds0, B, boff, kh, h, i, kg, sl);
        };
        const int kg0 = (xcd & 6) * 7;
        int kg = kg0, sl = kh ? dn_first(DNO) : dn_first(DNE);
        auto next = [&](auto nbc) INL {
            stage(kg, sl, nbc);
            kg = kg == 55 ? 0 : kg + 1;
            sl = (sl + 1) & 3;
        };
        const u64 DW2 = 0xffffffffull;
        next(CT<0>{});
        if constexpr (DNE != DNO) BARRIER();
        if constexpr (DL2 == 0) look(lk, DW2, f);
        next(CT<0>{});
        wait_vm<4>();
        BARRIER();
        if constexpr (DL2 == 0) { at = DW2; have |= seen(lk, at, tok); }
        if constexpr (DL2 == 1) look(lk, DW2, f);
        next(CT<0>{});
        next(CT<0>{});
        wait_vm<8>();
        BARRIER();
        if constexpr (DL2 == 1) { at = DW2; have |= seen(lk, at, tok); }
        next(CT<4>{});
        wait_vm<12>();
        BARRIER();
        next(CT<4>{});
#pragma unroll 1
        for (int n = (kh ? DNO : DNE) - 6; n > 0; --n) {
            wait_vm<16>();
            BARRIER();
            next(CT<4>{});
        }
        static_for<-NS, NSO - 1>([&](auto kc) INL {
            constexpr int KT = decltype(kc)::value;
            constexpr int P2 = KT + 2 < 0 ? 8 : (KT + 2 < NSO ? 6 : 0), P3 = KT + 3 < 0 ? 8 : (KT + 3 < NSO ? 6 : 0);
            wait_vm<P2 + P3>();
            BARRIER();
            if constexpr (KT < 0) {
                constexpr int J = KT + NS, RE = DNE + J < 28 ? DNE + J : 52 + J, RO = DNO + J < 28 ? DNO + J : 52 + J;
                const int ks = kg0 + (kh ? RO : RE);
                stage(ks < 56 ? ks : ks - 56, (KT + 2) & 3, CT<2>{});
            }
        });
        if (pf != nullptr) {
            for (int i = QPS; i > 0; --i) __builtin_amdgcn_s_sleep(1);
            qkv_stage_b(lds0, pf, qkv_boff(h, lane), h, qkv_k(qkv_ks(), 0), qsl(0));
            qkv_stage_b(lds0, pf, qkv_boff(h, lane), h, qkv_k(qkv_ks(), 1), qsl(1));
        }
        res_setup(rc, lds0 + NS * SLOT, m0, n0, tid);
        if (pf != nullptr && (bx & 1) == 0 && wid >= 6) {
            const unsigned ix = (bx >> 3) * 96 + (tid < 480 ? tid - 384 : 0);
            touch1(SINK, pf, ix * 2048 + ((bx & 3) * 4 + 3) * 128);
        }
#pragma unroll
        for (int i = 0; i < 4; ++i) asm volatile("" : "=v"(pb[i]), "=v"(pc[i]));
    } else {
        const int wm = wid >> 1, wn = wid & 1;
        const int l15 = lane & 15, g = lane >> 4, q = l15 >> 2, p4 = l15 & 3;
        unsigned ab[2], bb[2];
#pragma unroll
        for (int x = 0; x < 2; ++x) {
            ab[x] = opaque(lds0 + DNA + (wm * 16 + q) * 512 + (((x * 2 + (g >> 1)) ^ (l15 >> 3)) * 128) + p4 * 32 + (g & 1) * 16);
            bb[x] = opaque(lds0 + (g * 8 + q) * 128 + p4 * 8 + (((wn * 2 + x) ^ ((q >> 1) | ((g & 1) << 1))) << 5));
        }
        f32x4 acc[4][4];
        bf16x8 rr[4];
        const bf16x8 selr[2] = {pick(g == (l15 >> 3), l15 & 7), pick(g == 2 + (l15 >> 3), l15 & 7)};
        {
            const unsigned ra = lds0 + DNR + (wm * 64 + l15) * 128 + (((wn * 4 + g) ^ (l15 >> 1)) * 16);
            static_for<0, 4>([&](auto ic) INL {
                constexpr int i = decltype(ic)::value;
                rr[i] = lds_a<i * 2048>(ra);
#pragma unroll
                for (int j = 0; j < 2; ++j) {
                    acc[i][j] = f32x4{0.f, 0.f, 0.f, 0.f};
                    asm volatile("" : "+v"(acc[i][j]));
                }
            });
#pragma unroll
            for (int i = 0; i < 4; ++i) asm volatile("" : "+v"(rr[i]));
            if constexpr (DNE != DNO) BARRIER();
        }
        auto ldop = [&](bf16x8 (&af)[8], s16x4 (&bf)[16], auto slc, auto j0c, auto oc) INL {
            constexpr int SL = decltype(slc)::value, O = decltype(oc)::value;
            if constexpr (O == 2 || (O >= 5 && O < 12)) {
                constexpr int X = O == 2 ? 0 : O - 4;
                af[(X & 1) * 4 + X / 2] = lds_a<SL * ASZ + (X / 2) * 2048>(ab[X & 1]);
            } else {
                constexpr int X = decltype(j0c)::value * 4 + (O < 2 ? O : (O < 5 ? O - 1 : O - 8)), J = X / 4;
                bf[X] = lds_h<SL * ASZ + (J < 2 ? 8192 : 0) + ((X >> 1) & 1) * 4096 + (X & 1) * 512>(bb[J & 1]);
            }
        };
        auto ktile = [&](bf16x8 (&af)[8], s16x4 (&bf)[16], bf16x8 (&an)[8], s16x4 (&bn)[16], auto nslc, auto j0c, auto jnc, auto&& gap) INL {
            constexpr int J0 = decltype(j0c)::value, JN = decltype(jnc)::value;
            constexpr int NM = 8 * (4 - J0), NL = JN < 4 ? 8 + 4 * (4 - JN) : 0, TB = J0 ? DBAR2 : DBAR;
            constexpr int NG = NM - TB < NL ? NM - TB : NL;
            static_for<0, NM>([&](auto tc) INL {
                constexpr int T = decltype(tc)::value, j = J0 + T / 8, i = j & 1 ? 3 - (T / 2) % 4 : (T / 2) % 4, kk = T & 1;
                if constexpr (T == TB && NL > 0) {
                    WAIT_LGKM0();
                    BARRIER();
                }
                mma_ip(cat(bf[j * 4 + kk * 2], bf[j * 4 + kk * 2 + 1]), af[kk * 4 + i], acc[i][j]);
                SCHED();
                if constexpr (NL > 0 && T >= TB) {
                    constexpr int U = T - TB, lo_ = (U * NL + NG - 1) / NG, hi_ = ((U + 1) * NL + NG - 1) / NG;
                    static_for<lo_, (hi_ < NL ? hi_ : NL)>([&](auto oc) INL { ldop(an, bn, nslc, jnc, oc); });
                }
                gap(tc);
                SCHED();
            });
        };
        bf16x8 a0[8], a1[8];
        s16x4 b0[16], b1[16];
        const unsigned bo = opaque((unsigned)(wid * 4096 + lane * 16));
        const bf16x8 sel[2] = {pick(g == q, p4), pick(g == q, 4 + p4)};
        res_setup(rc, lds0 + NS * SLOT, m0, n0, tid);
        const unsigned cw = opaque(lds0 + NS * SLOT + (wm * 64 + l15) * CSTR + (wn * 32 + g * 4) * 2);
        const char* const rin = sopq((const char*)slab + (bx ^ DPX) * BOX);
        const __amdgpu_buffer_rsrc_t rout = __builtin_amdgcn_make_buffer_rsrc((char*)slab + bx * BOX, 0, BOX, 0x00020000);
        BARRIER();
        auto none = [&](auto) INL {};
        auto head = [&](auto nc) INL {
            constexpr int H = (decltype(nc)::value - 1) % NS;
            static_for<0, 24>([&](auto oc) INL {
                if constexpr (H & 1) ldop(a1, b1, CT<(1 - H) & 3>{}, CT<0>{}, oc);
                else ldop(a0, b0, CT<(1 - H) & 3>{}, CT<0>{}, oc);
            });
            SCHED();
#pragma unroll
            for (int t = 0; t < 8; ++t) mma_z(selr[t & 1], rr[t >> 1], acc[t >> 1][2 + (t & 1)]);
            SCHED();
            static_for<0, H>([&](auto jc) INL {
                constexpr int E = H - decltype(jc)::value;
                if constexpr (E & 1) ktile(a1, b1, a0, b0, CT<(2 - E) & 3>{}, CT<0>{}, CT<0>{}, none);
                else ktile(a0, b0, a1, b1, CT<(2 - E) & 3>{}, CT<0>{}, CT<0>{}, none);
            });
        };
        if ((DNE - DNO) % NS == 0 || kh) head(CT<DNO>{}); else head(CT<DNE>{});
#pragma unroll 1
        for (int n = (kh ? DNO - 1 : DNE - 1) / NS; n > 0; --n) {
            ktile(a0, b0, a1, b1, CT<2>{}, CT<0>{}, CT<0>{}, none);
            ktile(a1, b1, a0, b0, CT<3>{}, CT<0>{}, CT<0>{}, none);
            ktile(a0, b0, a1, b1, CT<0>{}, CT<0>{}, CT<0>{}, none);
            ktile(a1, b1, a0, b0, CT<1>{}, CT<0>{}, CT<0>{}, none);
        }
        u32x4 ow, pa[4];
        auto look = [&](u32x4& d, auto ic) INL { gload16_devo<decltype(ic)::value * 1024>(d, rin, bo); };
        static_for<-1, NSO>([&](auto sc) INL {
            constexpr int KT = decltype(sc)::value;
            constexpr int J0 = KT < 0 ? 0 : 2, JN = KT + 1 < NSO ? 2 : 4;
            auto gap = [&](auto tc) INL {
                constexpr int T = decltype(tc)::value;
                if constexpr (KT < 0 && T >= XO && T < XO + 16) {
                    constexpr int U = T - XO, j = U / 8, i = j ? 3 - (U / 2) % 4 : (U / 2) % 4;
                    asm volatile("v_cvt_pk_bf16_f32 %0, %1, %2\n\tv_min_u32 %0, -2, %0"
                                 : "=v"(ow[(i & 1) * 2 + (U & 1)]) : "v"(acc[i][j][(U & 1) * 2]), "v"(acc[i][j][(U & 1) * 2 + 1]));
                    if constexpr (U % 4 == 3) __builtin_amdgcn_raw_buffer_store_b128(ow, rout, bo + (j * 2 + i / 2) * 1024, 0, 16);
                }
                constexpr int X = KT < 0 ? -999 : (KT - NSO) * 16 + T;
                if constexpr (X >= -XA && X < -XA + 4) look(pa[X + XA], CT<X + XA>{});
                if constexpr (X >= -XB && X < -XB + 4) look(pb[X + XB], CT<X + XB>{});
            };
            if constexpr (KT & 1) ktile(a0, b0, a1, b1, CT<(KT + 3) % NS>{}, CT<J0>{}, CT<JN>{}, gap);
            else ktile(a1, b1, a0, b0, CT<(KT + 3) % NS>{}, CT<J0>{}, CT<JN>{}, gap);
        });
        box_wait(pa, pb, pc, rin, bo);
#pragma unroll
        for (int t = 0; t < 8; ++t) mma_ip(sel[(t >> 1) & 1], __builtin_bit_cast(bf16x8, pa[(t & 1) * 2 + t / 4]), acc[t >> 1][2 + (t & 1)]);
        asm volatile("s_nop 1" ::: "memory");
        SCHED();
#pragma unroll
        for (int i = 0; i < 4; ++i) {
#pragma unroll
            for (int x = 0; x < 2; ++x) {
                const f32x4 a = acc[i][2 + x];
                *(LDS_AS u32x2*)(size_t)(cw + i * 16 * CSTR + x * 32) = u32x2{pk2(a[0], a[1]), pk2(a[2], a[3])};
            }
            SCHED();
        }
    }
    WAIT_LGKM0();
    BARRIER();
    res_tail(rc, C, sp);
    if (pf == nullptr) wait_vm<0>();
    else if (wid < 4) {
        pubc<0, 8>(oflags + (tm * 16 + (n0 >> 6)) * 32, tok, lds0 + GCN + 8);
        if (wid == 2) code_touch(SINK, opaque(threadIdx.x) & 63u);
    }
#pragma unroll
    for (int i = 0; i < 4; ++i) asm volatile("" :: "v"(pb[i]), "v"(pc[i]));
}

__device__ __forceinline__ void gu_phase(char* smem, const u16* A, const u16* B0, const u16* B1, u16* C, const float* cs,
                                         const float* sp, const u16* wd, unsigned* wflags, unsigned* gflags, const GuC& gc, f32x4 cp,
                                         u64 lk, u64 tok) {
    constexpr int BM = 256, BN = 224, MI = 4, NI = 7, NK = D_ / 64;
    constexpr int ASZ = BM * 128, BSZ = GBS, NSA = 3, NSB = 2, BRING = GBR, STL = GST;
    constexpr auto asl = [](int kt) { return (kt + 2) % NSA; };

    const int wid = __builtin_amdgcn_readfirstlane(opaque(threadIdx.x) >> 6);
    const int id = wg_id();
    const int tm = id & 7, tn = id >> 3, m0 = tm * BM;
    const unsigned lds0 = __builtin_amdgcn_readfirstlane((unsigned)(size_t)(LDS_AS char*)smem);
    const unsigned ldsw = lds0 + wid * 1024;

    const auto& aoff = gc.aoff;
    const auto& boff = gc.boff;
    const __amdgpu_buffer_rsrc_t ra = __builtin_amdgcn_make_buffer_rsrc((void*)A, 0, 0x7fffffff, 0x00020000);
    const __amdgpu_buffer_rsrc_t rb[4] = {__builtin_amdgcn_make_buffer_rsrc((void*)B0, 0, 0x7fffffff, 0x00020000),
                                          __builtin_amdgcn_make_buffer_rsrc((void*)(wid >= 6 ? B1 : B0), 0, 0x7fffffff, 0x00020000),
                                          __builtin_amdgcn_make_buffer_rsrc((void*)B1, 0, 0x7fffffff, 0x00020000),
                                          __builtin_amdgcn_make_buffer_rsrc((void*)B1, 0, wid < 4 ? 0x7fffffff : 0, 0x00020000)};
    const unsigned b3d[2] = {wid < 4 ? ldsw + BRING + 24576 : lds0 + 161792, wid < 4 ? ldsw + BRING + BSZ + 24576 : lds0 + 161792};
    unsigned soff = 0u;
    auto prep = [&](auto bc, auto pc, auto ktc) INL {
        constexpr int P = decltype(pc)::value, KT = decltype(ktc)::value;
        constexpr bool ISB = decltype(bc)::value;
        if constexpr (P == 0) s_set<(ISB ? gu_k(KT) * 64 * F_ * 2 : gu_k(KT) * 128)>(soff);
        if constexpr (ISB && P == 3) m0_set<0>(b3d[KT % NSB]);
        else m0_set<(ISB ? BRING + (KT % NSB) * BSZ : asl(KT) * ASZ) + P * 8192>(ldsw);
    };
    const int dk = DPAR ? (id >> 5) & 1 : id >> 7;
    const int dtm = dn_grp(id >> 5) * 4 + (id & 3), dtn = ((id >> 2) & 7) * 2 + dk;
    const unsigned dkh = dk * (128 + (dn_first(DNE) ^ dn_first(DNO)) * 64 * D_ * 2);
    const __amdgpu_buffer_rsrc_t rd = __builtin_amdgcn_make_buffer_rsrc((char*)wd + (id >> 6) * (14 * 64 * D_ * 2) + (dtn >> 1) * 256, 0,
        0x7fffffff, 0x00020000);
    const __amdgpu_buffer_rsrc_t rr = __builtin_amdgcn_make_buffer_rsrc((char*)A + (dtm * 128 - m0) * D_ * 2 + dtn * 128, 0, 0x7fffffff, 0x00020000);
    unsigned doff;
    constexpr auto dreq = [](int sl) { return sl < 0 ? -1 : (int)((DSCH >> (4 * sl)) & 15) - 1; };
    constexpr int DV = (dreq(0) >= 0) + (dreq(1) >= 0) + (dreq(2) >= 0) + (dreq(3) >= 0) + (TCU <= 0);
    auto dprep = [&](auto xc) INL {
        constexpr int X = decltype(xc)::value;
        if constexpr (X == 0) {
            const int t = (int)opaque(threadIdx.x);
            doff = opaque(b_off(t >> 6, t & 63, D_, 0));
        }
        if constexpr (X < 8) asm volatile("s_xor_b32 %0, %1, %2" : "=s"(soff) : "s"(dkh), "n"((((X >> 1) - dn_first(DNE)) & 3) * 64 * D_ * 2 + (X & 1) * 128) : "scc");
        m0_set<(X < 8 ? 0 : DNR - 65536) + X * 8192>(ldsw);
    };
    auto dgo = [&](auto xc) INL {
        constexpr int X = decltype(xc)::value;
        if constexpr (X < 8) blds16<false>(rd, doff, soff);
        else blds16<true>(rr, aoff[X - 8], 0u);
    };
    auto go = [&](auto bc, auto pc) INL {
        if constexpr (decltype(bc)::value) blds16<false>(rb[decltype(pc)::value], boff[decltype(pc)::value], soff);
        else blds16<true>(ra, aoff[decltype(pc)::value], soff);
    };
    auto stage_a = [&](auto ktc) INL {
        static_for<0, 4>([&](auto pc) INL { prep(CT<0>{}, pc, ktc); asm volatile("s_nop 0"); go(CT<0>{}, pc); });
    };
    __builtin_amdgcn_s_setprio(2);
    const auto& ab = gc.ab;
    const auto& bb = gc.bb;
    f32x4 acc[MI][NI];
    bf16x8 af[2][2 * MI];
    s16x4 bf[2][4 * NI];
    auto ldop = [&](auto ktc, auto oc) INL {
        constexpr int KT = decltype(ktc)::value, O = decltype(oc)::value;
        if constexpr (O == 2 || (O >= 5 && O < 12)) {
            constexpr int X = O == 2 ? 0 : O - 4, off = asl(KT) * ASZ + (X / 2) * 2048;
            af[KT & 1][X] = lds_a<(off & 65535)>(ab[X & 1][off >> 16]);
        } else {
            constexpr int X = O < 2 ? O : (O < 5 ? O - 1 : O - 8), J = X / 4;
            bf[KT & 1][X] = lds_h<(KT % NSB) * BSZ + ((X >> 1) & 1) * 32 * BN + (J == 6 ? 0 : (J / 3) * (BSZ / 2) + (J % 3) * 32)>(bb[X & 1][J == 6]);
        }
    };
    f32x4 stn[4];
    float sx[8];
    const char* const spw = uni((const char*)sp + m0 * 128);
    auto stats = [&](auto zc) INL {
        constexpr int Z = decltype(zc)::value;
        if constexpr (Z == 0) {
#pragma unroll
            for (int i = 0; i < 4; ++i) asm volatile("" : "+v"(stn[i]));
        }
        if constexpr (Z < 8) asm volatile("v_add_f32 %0, %1, %2" : "=v"(sx[Z]) : "v"(stn[Z / 4 * 2][Z % 4]), "v"(stn[Z / 4 * 2 + 1][Z % 4]));
        else if constexpr (Z < 12) asm volatile("v_add_f32 %0, %0, %1" : "+v"(sx[(Z & 1) * 2 + (Z / 2 & 1)]) : "v"(sx[4 + (Z & 1) * 2 + (Z / 2 & 1)]));
        else if constexpr (Z < 14) asm volatile("v_permlane16_swap_b32 %0, %1" : "+v"(sx[Z - 12]), "+v"(sx[Z - 10]));
        else if constexpr (Z < 16) asm volatile("v_add_f32 %0, %0, %1" : "+v"(sx[Z - 14]) : "v"(sx[Z - 12]));
        else if constexpr (Z == 17 || Z == 22) asm volatile("v_permlane32_swap_b32 %0, %1" : "+v"(sx[0]), "+v"(sx[1]));
        else if constexpr (Z == 18) asm volatile("v_add_f32 %0, %0, %1" : "+v"(sx[0]) : "v"(sx[1]));
        else if constexpr (Z == 19) asm volatile("v_mul_f32 %0, 0x3a800000, %1" : "=v"(sx[1]) : "v"(sx[0]));
        else if constexpr (Z == 20) asm volatile("v_mul_f32 %0, 0x3a800000, %0" : "+v"(sx[0]));
        else if constexpr (Z == 23) asm volatile("v_mul_f32 %0, %1, %1" : "=v"(sx[2]) : "v"(sx[0]));
        else if constexpr (Z == 24) asm volatile("v_sub_f32 %0, %0, %1" : "+v"(sx[1]) : "v"(sx[2]));
        else if constexpr (Z == 25) asm volatile("v_max_f32 %0, 0, %0" : "+v"(sx[1]));
        else if constexpr (Z == 26) asm volatile("v_cvt_pk_bf16_f32 %0, %0, -%1" : "+v"(sx[1]) : "v"(sx[0]));
        else if constexpr (Z == 27) asm volatile("v_and_b32 %0, 0xffff0000, %1" : "=v"(sx[0]) : "v"(sx[1]));
        else if constexpr (Z == 28 || Z == 31) asm volatile("v_lshlrev_b32 %0, 16, %0" : "+v"(sx[1]));
        else if constexpr (Z == 29) asm volatile("v_add_f32 %0, 0x3727c5ac, %0" : "+v"(sx[1]));
        else if constexpr (Z == 30 || Z == 33) asm volatile("v_cvt_pk_bf16_f32 %0, %0, %0" : "+v"(sx[1]));
        else if constexpr (Z == 32) asm volatile("v_sqrt_f32 %0, %0" : "+v"(sx[1]));
        else if constexpr (Z == 34) asm volatile("v_lshlrev_b32 %0, 16, %1" : "=v"(sx[2]) : "v"(sx[1]));
        else if constexpr (Z == 35) asm volatile("v_rcp_f32 %0, %1" : "=v"(sx[1]) : "v"(sx[2]));
        else if constexpr (Z == 36) asm volatile("v_mul_f32 %0, %0, %0" : "+v"(sx[2]));
        else if constexpr (Z == 37) asm volatile("v_mul_f32 %0, 0xbfb8aa3b, %0" : "+v"(sx[1]));
        else if constexpr (Z == 16) asm volatile("v_and_b32 %0, 31, %1" : "=v"(sx[4]) : "v"(threadIdx.x));
        else if constexpr (Z == 21) asm volatile("v_lshl_add_u32 %0, %0, 4, %1" : "+v"(sx[4]) : "s"(lds0 + STL + wid * 512));
        else if constexpr (Z == 38) *(LDS_AS f32x4*)(size_t)(__float_as_uint(sx[4])) = f32x4{sx[0], sx[1], sx[2], sx[3]};
    };
    static_assert((2 * (NK - 2) + 1 - (2 * STK + 5)) * SPU >= 39, "statistics in time");
    f32x4 ccv[NI], stv[MI];
    const unsigned cca = gc.cca, cc6 = gc.cc6, sta = gc.sta, gof = gc.gof, gofm = gc.gofm, hsw = gc.hsw, hsr = gc.hsr;
    auto eld = [&](auto oc) INL {
        constexpr int O = decltype(oc)::value;
        constexpr int SI = O == 0 ? 0 : (O == 3 ? 1 : (O == 7 ? 2 : (O == 8 ? 3 : -1)));
        constexpr int J = O < 3 ? O * 3 - 3 : (O < 6 ? O * 3 - 11 : (O == 6 ? 6 : O * 3 - 25));
        if constexpr (SI >= 0) stv[SI] = *(const LDS_AS f32x4*)(size_t)(sta + (SI >> 1) * 512 + (SI & 1) * 128);
        else if constexpr (J == 6) ccv[6] = *(const LDS_AS f32x4*)(size_t)(cc6);
        else ccv[J] = *(const LDS_AS f32x4*)(size_t)(cca + ((J / 3) * 112 + (J % 3) * 16) * 4);
    };
    auto unit = [&](auto ktc, auto kkc) INL {
        constexpr int KT = decltype(ktc)::value, KK = decltype(kkc)::value;
        constexpr int NKT = KK ? KT + 1 : KT;
        static_for<0, MI * NI>([&](auto tc) INL {
            constexpr int T = decltype(tc)::value, X = KK * MI * NI + T, j = X / 8, i = j & 1 ? MI - 1 - (X / 2) % MI : (X / 2) % MI;
            if constexpr (KT == 0 && (X & 1) == 0) mma_z(cat(bf[0][4 * j], bf[0][4 * j + 1]), af[0][2 * i], acc[i][j]);
            else mma_ip(cat(bf[KT & 1][4 * j + 2 * (X & 1)], bf[KT & 1][4 * j + 2 * (X & 1) + 1]), af[KT & 1][2 * i + (X & 1)], acc[i][j]);
            SCHED();
            constexpr bool RQ = KT + 2 < NK && T < 4 * GGS;
            if constexpr (RQ && T % GGS == 0) prep(kkc, CT<T / GGS>{}, CT<KT + 2>{});
            if constexpr (RQ && T % GGS == 1) go(kkc, CT<T / GGS>{});
            constexpr int DX = T < 4 * GGS ? dreq((2 * (KT - NK + 2) + KK) * 4 + T / GGS) : -1;
            if constexpr (DX >= 0 && T % GGS == 0) dprep(CT<DX>{});
            if constexpr (DX >= 0 && T % GGS == 1) dgo(CT<DX>{});
            if constexpr (2 * (KT - NK + 2) + KK == TCU && T == TCT) touch1(SINK, wd, gc.tof);
            constexpr int O = T - (T + GGS - 2) / GGS;
            if constexpr (NKT < NK && T % GGS != 1 && O < (KK ? 12 : 36 - GL2)) ldop(CT<NKT>{}, CT<(KK ? O : O + GL2)>{});
            if constexpr (NKT < NK && KK == 1 && T >= 20 && T < 8 + GL2) ldop(CT<NKT>{}, CT<T - 8>{});
            if constexpr (KT == 0 && KK == 1 && T == GLT) asm volatile("global_load_dwordx2 %0, %1, %2 sc1" : "=v"(lk) : "v"(gc.f2), "s"(cs) : "memory");
            if constexpr (KT == STK && KK == 1 && T >= STT && T < STT + 4) gload16_devo<(T - STT) * 256>(stn[T - STT], spw, gc.sto);
            constexpr int Z = (2 * KT + KK - 2 * STK - 5) * SPU + T - (MI * NI - SPU);
            if constexpr (Z >= 0 && Z < 39 && T >= MI * NI - SPU) stats(CT<Z>{});
            if constexpr (NKT == NK && T < 11) eld(tc);
            if constexpr (KK == 0 && T == PRT && KT < NK - 1) __builtin_amdgcn_s_setprio(0);
            SCHED();
        });
    };

        asm volatile("" :: "s"(ra), "s"(rb[0]), "s"(rb[1]), "s"(rb[2]), "s"(rb[3]), "s"(b3d[0]), "s"(b3d[1]), "s"(rd), "s"(rr), "s"(dkh), "s"(spw), "s"(ldsw));
    static_for<0, 4>([&](auto pc) INL { prep(CT<1>{}, pc, CT<1>{}); asm volatile("s_nop 0"); go(CT<1>{}, pc); });
    asm volatile("s_cmp_eq_u32 %2, 0\n\ts_cselect_b64 exec, 1, 0\n\ts_waitcnt vmcnt(6)\n\ts_barrier\n\t"
                 "global_store_dwordx2 %3, %4, %5 sc1\n\ts_mov_b64 exec, -1\n\ts_cbranch_scc1 1f\n\ts_waitcnt vmcnt(4)\n"
                 "1:\n\ts_waitcnt vmcnt(5)"
                 : "+{v[194:195]}"(lk), "+{v[196:199]}"(cp) : "s"(wid), "v"(0u), "v"(tok),
                   "s"(wflags + (((id / 64) * 4 + id % 4) * 16 + (id % 64) / 4) * 32) : "memory", "scc");
    spin(lk, cs, gc.f1, tok);
    BARRIER();
    stage_a(CT<0>{});
    asm volatile("v_add_f32_dpp v196, v196, v196 row_ror:8 row_mask:0xf bank_mask:0xf\n\t"
                 "v_add_f32_dpp v197, v197, v197 row_ror:8 row_mask:0xf bank_mask:0xf\n\t"
                 "v_add_f32_dpp v198, v198, v198 row_ror:8 row_mask:0xf bank_mask:0xf\n\t"
                 "v_add_f32_dpp v199, v199, v199 row_ror:8 row_mask:0xf bank_mask:0xf\n\t"
                 "v_permlane16_swap_b32 v196, v197\n\ts_nop 0\n\tv_permlane16_swap_b32 v198, v199\n\t"
                 "v_add_f32 v196, v196, v197\n\tv_add_f32 v198, v198, v199\n\ts_nop 1\n\tv_permlane32_swap_b32 v196, v198\n\t"
                 "v_add_f32 v196, v196, v198\n\tds_write_b32 %1, v196" : "+{v[196:199]}"(cp) : "v"(gc.cw) : "memory");
    {
        u32x4 z; unsigned a;
        asm volatile("v_mov_b32 %1, %2\n\tv_mov_b64 v[200:201], 0\n\tv_mov_b64 v[202:203], 0\n\tds_write_b128 %1, %0\n\tds_write_b128 %1, %0 offset:16"
                     : "=&{v[200:203]}"(z), "=&v"(a) : "s"(lds0 + GCN) : "memory");
    }
    constexpr auto isa = [](int o) { return o == 2 || (o >= 5 && o < 12); };
    if constexpr (GPV >= 1) static_for<0, GL2>([&](auto oc) INL { if constexpr (!isa(decltype(oc)::value)) ldop(CT<0>{}, oc); });
    SCHED();
    if constexpr (GPV == 2) stage_a(CT<1>{});
    wait_vm<(GPV == 2 ? 4 : 0)>();
    BARRIER();
    if constexpr (GPV == 0) stage_a(CT<1>{});
    static_for<0, GL2>([&](auto oc) INL { if constexpr (GPV == 0 || isa(decltype(oc)::value)) { ldop(CT<0>{}, oc); SCHED(); } });
    SCHED();
    if constexpr (GPV == 1) stage_a(CT<1>{});
    static_for<0, NK>([&](auto kc) INL {
        constexpr int KT = decltype(kc)::value;
        unit(CT<KT>{}, CT<0>{});
        if constexpr (KT < NK - 1) {
            WAIT_LGKM0();
            wait_vm<(KT == STK + 1 ? 8 : (KT == NK - 2 ? DV : 4))>();
            if constexpr (KT == 1) spin(lk, cs, gc.f2, tok);
            BARRIER();
            if constexpr (PRT < 28) __builtin_amdgcn_s_setprio(2);
        }
        unit(CT<KT>{}, CT<1>{});
    });
    asm volatile("s_nop 15\n\ts_nop 7" ::: "memory");
    SCHED();
    const int tide = (int)opaque(threadIdx.x);
    {
        __builtin_amdgcn_s_setprio(0);
        auto center = [&](f32x4 a, f32x4 c, float nm) INL {
            float a0, a1, a2, a3;
            asm("v_fma_f32 %0, %4, %5, %9\n\tv_fma_f32 %1, %4, %6, %10\n\tv_fma_f32 %2, %4, %7, %11\n\tv_fma_f32 %3, %4, %8, %12"
                : "=&v"(a0), "=&v"(a1), "=&v"(a2), "=&v"(a3)
                : "v"(nm), "v"(c[0]), "v"(c[1]), "v"(c[2]), "v"(c[3]), "v"(a[0]), "v"(a[1]), "v"(a[2]), "v"(a[3]));
            return f32x4{a0, a1, a2, a3};
        };
        auto glu = [&](f32x4 gp, f32x4 up, float c, float k) INL {
            float t0, t1, t2, t3, u0 = up[0], u1 = up[1], u2 = up[2], u3 = up[3];
            asm("v_mul_f32 %0, %12, %8\n\tv_mul_f32 %1, %12, %9\n\tv_mul_f32 %2, %12, %10\n\tv_mul_f32 %3, %12, %11\n\t"
                "v_exp_f32 %0, %0\n\tv_exp_f32 %1, %1\n\tv_exp_f32 %2, %2\n\tv_exp_f32 %3, %3\n\t"
                "v_mul_f32 %4, %4, %8\n\tv_mul_f32 %5, %5, %9\n\t"
                "v_fma_f32 %0, %0, %13, %13\n\tv_fma_f32 %1, %1, %13, %13\n\t"
                "v_fma_f32 %2, %2, %13, %13\n\tv_fma_f32 %3, %3, %13, %13\n\t"
                "v_rcp_f32 %0, %0\n\tv_rcp_f32 %1, %1\n\tv_rcp_f32 %2, %2\n\tv_rcp_f32 %3, %3\n\t"
                "v_mul_f32 %6, %6, %10\n\tv_mul_f32 %7, %7, %11\n\t"
                "v_mul_f32 %4, %4, %0\n\tv_mul_f32 %5, %5, %1\n\tv_mul_f32 %6, %6, %2\n\tv_mul_f32 %7, %7, %3"
                : "=&v"(t0), "=&v"(t1), "=&v"(t2), "=&v"(t3), "+v"(u0), "+v"(u1), "+v"(u2), "+v"(u3)
                : "v"(gp[0]), "v"(gp[1]), "v"(gp[2]), "v"(gp[3]), "v"(c), "v"(k));
            return u32x2{pk2(u0, u1), pk2(u2, u3)};
        };
        auto centerp = [&](f32x4 a, f32x4 c, float nm) INL {
            const f32x2 n2 = {nm, nm};
            const f32x2 lo = __builtin_elementwise_fma(n2, c.xy, a.xy), hi = __builtin_elementwise_fma(n2, c.zw, a.zw);
            return f32x4{lo.x, lo.y, hi.x, hi.y};
        };
        auto glu2 = [&](f32x2 gp, f32x2 up, float c, float k) INL {
            const f32x2 ea = gp * f32x2{c, c}, k2 = {k, k};
            const f32x2 d = __builtin_elementwise_fma(f32x2{__builtin_amdgcn_exp2f(ea.x), __builtin_amdgcn_exp2f(ea.y)}, k2, k2);
            const f32x2 hv = (gp * up) * f32x2{__builtin_amdgcn_rcpf(d.x), __builtin_amdgcn_rcpf(d.y)};
            return pk2(hv.x, hv.y);
        };
        auto glup = [&](f32x4 gp, f32x4 up, float c, float k) INL {
            return u32x2{glu2(gp.xy, up.xy, c, k), glu2(gp.zw, up.zw, c, k)};
        };
        u32x2 hp[2];
        auto blk = [&](int i, int j, auto pkc) INL {
            if constexpr (decltype(pkc)::value)
                hp[i & 1] = glup(centerp(acc[i][j], ccv[j], stv[i][0]), centerp(acc[i][j + 3], ccv[j + 3], stv[i][0]), stv[i][1], stv[i][2]);
            else
                hp[i & 1] = glu(center(acc[i][j], ccv[j], stv[i][0]), center(acc[i][j + 3], ccv[j + 3], stv[i][0]), stv[i][1], stv[i][2]);
        };
        auto blk2 = [&](int i, int j) INL {
            f32x2 gp[4], up[4], ea[4];
#pragma unroll
            for (int x = 0; x < 4; ++x) {
                const f32x4 a = acc[i + (x >> 1)][j], c = ccv[j];
                const float nm = stv[i + (x >> 1)][0];
                gp[x] = __builtin_elementwise_fma(f32x2{nm, nm}, x & 1 ? c.zw : c.xy, x & 1 ? a.zw : a.xy);
            }
            SCHED();
#pragma unroll
            for (int x = 0; x < 4; ++x) { const float c = stv[i + (x >> 1)][1]; ea[x] = gp[x] * f32x2{c, c}; }
            SCHED();
#pragma unroll
            for (int x = 0; x < 4; ++x) {
                const f32x4 a = acc[i + (x >> 1)][j + 3], c = ccv[j + 3];
                const float nm = stv[i + (x >> 1)][0];
                ea[x] = f32x2{__builtin_amdgcn_exp2f(ea[x].x), __builtin_amdgcn_exp2f(ea[x].y)};
                up[x] = __builtin_elementwise_fma(f32x2{nm, nm}, x & 1 ? c.zw : c.xy, x & 1 ? a.zw : a.xy);
                SCHED();
            }
#pragma unroll
            for (int x = 0; x < 4; ++x) { const float k = stv[i + (x >> 1)][2]; ea[x] = __builtin_elementwise_fma(ea[x], f32x2{k, k}, f32x2{k, k}); }
            SCHED();
#pragma unroll
            for (int x = 0; x < 4; ++x) {
                ea[x] = f32x2{__builtin_amdgcn_rcpf(ea[x].x), __builtin_amdgcn_rcpf(ea[x].y)};
                up[x] = gp[x] * up[x];
                SCHED();
            }
#pragma unroll
            for (int x = 0; x < 4; ++x) up[x] = up[x] * ea[x];
            SCHED();
#pragma unroll
            for (int x = 0; x < 4; ++x) hp[x & 1][x >> 1] = pk2(up[(x & 1) * 2 + (x >> 1)].x, up[(x & 1) * 2 + (x >> 1)].y);
        };
        auto flush = [&](int hf, int j) INL {
            const auto t0 = __builtin_amdgcn_permlane16_swap(hp[0][0], hp[1][0], false, false);
            const auto t1 = __builtin_amdgcn_permlane16_swap(hp[0][1], hp[1][1], false, false);
            store16_wt(C, gof + hf * 8 * (8 * F_) + j * 128, u32x4{t0[0], t1[0], t0[1], t1[1]});
            SCHED();
        };
        auto head = [&](auto pkc) INL { blk(0, 0, pkc); blk(1, 0, pkc); flush(0, 0); blk(0, 1, pkc); };
        auto sync = [&]() INL {
            WAIT_LGKM0();
            BARRIER();
            asm volatile("" ::: "memory");
            SCHED();
        };
        const bool late = wid >= 4;
        if (!late) head(CT<0>{});
        sync();
#pragma unroll
        for (int i = 0; i < MI; ++i)
#pragma unroll
            for (int j = 0; j < NI; ++j) asm volatile("" : "+v"(acc[i][j]));
#pragma unroll
        for (int i = 0; i < MI; i += 2) {
            const f32x4 x = centerp(acc[i][6], ccv[6], stv[i][0]), y = centerp(acc[i + 1][6], ccv[6], stv[i + 1][0]);
            f32x4 gm, um;
#pragma unroll
            for (int r = 0; r < 4; ++r) {
                const auto t = __builtin_amdgcn_permlane32_swap(__float_as_uint(x[r]), __float_as_uint(y[r]), false, false);
                gm[r] = __uint_as_float(t[0]); um[r] = __uint_as_float(t[1]);
            }
            const bool hi = (tide & 32) != 0;
            *(LDS_AS u32x2*)(size_t)(hsw + i * 512) =
                glup(gm, um, hi ? stv[i + 1][1] : stv[i][1], hi ? stv[i + 1][2] : stv[i][2]);
        }
        u32x4 mv;
        auto mixed = [&]() INL {
            wait_vm<2>();
            sync();
            mv = *(const LDS_AS u32x4*)(size_t)(hsr);
            SCHED();
        };
        if constexpr (MXP == 0) { mixed(); store16_wt(C, gofm, mv); }
        if (late) {
            __builtin_amdgcn_s_setprio(LPR);
            blk2(0, 0);
            flush(0, 0);
            blk2(0, 1);
        } else {
            blk(1, 1, CT<1>{});
        }
        flush(0, 1);
        if constexpr (MXP == 1) mixed();
        blk2(0, 2);
        if constexpr (MXP == 1) store16_wt(C, gofm, mv);
        flush(0, 2);
#pragma unroll
        for (int j = 0; j < 3; ++j) {
            blk2(2, j);
            flush(1, j);
        }
        __builtin_amdgcn_s_setprio(0);
        if (!late) {
            pubc<0>(gflags + (tm * 64 + tn) * 32, tok, lds0 + GCN);
            if (wid == 2) code_touch(SINK, tide & 63);
        }
    }
}

constexpr long long W_OB = 12, W_H = 16, W_XA = 32, W_XB = 36, W_CS = 40, W_SPA = 42, W_SPB = 43, W_FL = 44, W_EP = 45, W_SL = 48, W_L1 = 48;
extern "C" __global__ __launch_bounds__(512, 2) void body_k(const u16* __restrict__ x0, const u16* __restrict__ wqkv,
                                                 const u16* __restrict__ wo, const u16* __restrict__ wg,
                                                 const u16* __restrict__ wu, const u16* __restrict__ wd, u16* out,
                                                 char* ws) {
    extern __shared__ __attribute__((aligned(16))) char smem[];
    {
        const int tid = threadIdx.x, lane = tid & 63, wid = __builtin_amdgcn_readfirstlane(tid >> 6);
        if (wid >= 2 && wid < 6) {
            const int id = (blockIdx.x & 7) * 32 + (blockIdx.x >> 3);
            const int tm = (id / 128) * 8 + id % 8, tn = (id % 128) / 8, h = wid & 1;
            const int row = h * 8 + (lane >> 3);
            const unsigned sw = ((lane & 7) ^ ((row >> 1) & 7)) * 16;
            if (wid < 4) {
                qkv_half_a(x0, (unsigned)((tm * 128 + row) * D_ * 2) + sw, qsl(0) * QAS + h * 1024);
            } else {
                const unsigned lh = (unsigned)((tm ? tm * 128 - 16 + row : 0) * D_ * 2) + sw;
                glds16_mask(x0, lh, qsl(0) * QAS + 16384 + h * 1024, h == 0 ? ~0xffull : ~0ull);
                qkv_half_b(wqkv, b_off(h, lane, 3 * D_, tn * 64), QBR + qsl(0) * QBS + h * 1024);
                glds16_mask((const char*)x0 + 128, lh, qsl(1) * QAS + 16384 + h * 1024, h == 0 ? ~0xffull : ~0ull);
            }
        } else if (wid == 1) {
            code_touch(SINK, lane);
        }
    }
    u64 tok = 0;
#pragma unroll 1
    for (int L = 0; L < 2; ++L) {
        auto at = [&](long long mb, int l) INL { return sopq(ws) + ((mb + l * W_L1) << 20); };
        auto fl = [&](int ph) INL { return (unsigned*)at(W_FL, 0) + 16384 + ph * 8192; };
        qkv_phase(smem, L ? (u16*)at(W_XB, 0) : x0, sopq(wqkv), (u16*)at(W_OB, L), (float*)at(W_CS, 0), (float*)at(W_SPB, 0), fl(4),
                  fl(7 - L), sopq(wo), L == 0, sopq(wg), sopq(wu), (u64*)at(W_EP, 0), tok);
        const float* const cs = (float*)at(W_CS, 0);
        GuC gc;
        f32x4 cp;
        u64 lk;
        wo_phase(smem, (u16*)at(W_OB, L), sopq(wo), (u16*)at(W_XA, L), (float*)at(W_SPA, L), fl(7 - L),
                 L == 0, sopq(wg), sopq(wu), cs, CSO + (2 + L) * 32768, gc, cp, lk, tok);
        gu_phase(smem, (u16*)at(W_XA, L), sopq(wg), sopq(wu), (u16*)at(W_H, L), cs, (float*)at(W_SPA, L), sopq(wd),
                 fl(L), fl(8 + 2 * L), gc, cp, lk, tok);
        dn_phase(smem, (u16*)at(W_H, L), sopq(wd), L ? out : (u16*)at(W_XB, 0), L ? nullptr : (float*)at(W_SPB, 0),
                 (float*)at(W_SL, L), fl(8 + 2 * L), fl(4 + L), L ? nullptr : sopq(wqkv), tok);
    }
    asm volatile(".Lbody_end:");
}

extern "C" void kernel_launch(void* const* d_in, const int* in_sizes, int n_in, void* d_out, int out_size,
                              void* d_ws, size_t ws_size, hipStream_t stream) {
    const u16* x0 = (const u16*)d_in[0];
    const u16* wqkv = (const u16*)d_in[1];
    const u16* wo = (const u16*)d_in[2];
    const u16* wg = (const u16*)d_in[4];
    const u16* wu = (const u16*)d_in[5];
    const u16* wd = (const u16*)d_in[6];
    constexpr int smem = 163840;
    (void)hipFuncSetAttribute((const void*)body_k, hipFuncAttributeMaxDynamicSharedMemorySize, smem);
    body_k<<<256, 512, smem, stream>>>(x0, wqkv, wo, wg, wu, wd, (u16*)d_out, (char*)d_ws);
}
```
